```python
import torch

def _softmax(x, axis=-1):
    return torch.softmax(x, dim=axis)
def _gelu(x):
    return torch.nn.functional.gelu(x, approximate="tanh")
def _silu(x):
    return torch.nn.functional.silu(x)
def _sigmoid(x):
    return torch.sigmoid(x)
def _layernorm(x, axis=-1, eps=1e-5):
    m = x.mean(dim=axis, keepdim=True)
    v = x.var(dim=axis, keepdim=True, unbiased=False)
    return (x - m) / torch.sqrt(v + eps)
def _scatter_add(tgt, idx, src, axis=0):
    return tgt.clone().index_add_(axis, idx, src)
def _segment_sum(src, seg, n):
    out = torch.zeros((n,) + src.shape[1:], dtype=src.dtype)
    return out.index_add_(0, seg, src)
def _pad(x, pw):
    flat = tuple(v for pair in reversed(pw) for v in pair)
    return torch.nn.functional.pad(x, flat)

kernel_name = 'parallel_sum__sliding_local_dilated__dense_activation_squared_relu__prompt'

def setup_inputs():

    _w = (lambda a: (lambda t: t.to(torch.bfloat16) if t.is_floating_point() else t)(torch.as_tensor(a)))
    def _A_setup():
        B, S, D = 1, 2048, 1024
        return {
            "x": _w((lambda *s: torch.randn(*s, dtype=torch.bfloat16) * 0.05)(B, S, D)),
            "wqkv": _w((lambda *s: torch.randn(*s, dtype=torch.bfloat16) * 0.1)(D, 3 * D)),
            "wo": _w((lambda *s: torch.randn(*s, dtype=torch.bfloat16) * 0.05)(D, D)),
        }
    _A_in = _A_setup()

    def _B_setup():
        B, S, D, F = 1, 2048, 1024, 3584
        return {
            "x": _w((lambda *s: torch.randn(*s, dtype=torch.bfloat16) * 0.05)(B, S, D)),
            "w1": _w((lambda *s: torch.randn(*s, dtype=torch.bfloat16) * 0.05)(D, F)),
            "b1": _w((lambda *s: torch.randn(*s, dtype=torch.bfloat16) * 0.4)(F)),
            "w2": _w((lambda *s: torch.randn(*s, dtype=torch.bfloat16) * 0.05)(F, D)),
            "b2": _w((lambda *s: torch.randn(*s, dtype=torch.bfloat16) * 3.2)(D)),
        }
    _B_in = _B_setup()

    return {
        "A_x": _A_in["x"],
        "A_wqkv": _A_in["wqkv"],
        "A_wo": _A_in["wo"],
        "B_x": _B_in["x"],
        "B_w1": _B_in["w1"],
        "B_b1": _B_in["b1"],
        "B_w2": _B_in["w2"],
        "B_b2": _B_in["b2"],
    }

def reference(A_x, A_wqkv, A_wo, B_x, B_w1, B_b1, B_w2, B_b2):
    def _A_ref(x, wqkv, wo):
        B, S, D = x.shape
        H = 32; Dh = D // H
        W, dil = 8, 2
        qkv = (lambda x, *d: x.permute(*d))((x @ wqkv).reshape(B, S, 3, H, Dh), 2, 0, 3, 1, 4)
        q, k, v = qkv[0], qkv[1], qkv[2]
        scale = Dh ** -0.5
        pos = torch.arange(S)
        off = torch.arange(W) * dil
        idx = pos[:, None] - off[None, :]
        mask = idx >= 0
        idx_c = torch.where(mask, idx, 0).reshape(-1)
        kg = k[:, :, idx_c, :].reshape(B, H, S, W, Dh)
        vg = v[:, :, idx_c, :].reshape(B, H, S, W, Dh)
        logits = torch.einsum("bhsd,bhswd->bhsw", q, kg) * scale
        logits = torch.where(mask[None, None, :, :], logits, -1e9)
        attn = _softmax(logits, axis=-1)
        o = torch.einsum("bhsw,bhswd->bhsd", attn, vg)
        return (lambda x, *d: x.permute(*d))(o, 0, 2, 1, 3).reshape(B, S, D) @ wo

    def _B_ref(x, w1, b1, w2, b2):
        h = torch.einsum("bsd,df->bsf", x, w1) + b1
        zero = torch.zeros_like(h)
        r = torch.where(h > zero, h, zero)
        h = r * r
        out = torch.einsum("bsf,fd->bsd", h, w2) + b2
        return out

    x = A_x
    xn = _layernorm(x, axis=-1)
    return x + _A_ref(xn, A_wqkv, A_wo) + _B_ref(xn, B_w1, B_b1, B_w2, B_b2)
```

```cpp
#include <hip/hip_runtime.h>

constexpr unsigned LDS_BYTES = 163840;

#define STAMPENTRY
#define LSTW(a_, w_)
#define STAMP(a_, b_)
#define STAMPEND

__global__ __launch_bounds__(256, 1) void fused_kernel(const void* x, const void* wqkv, const void* wo, const void* w1, const void* b1,
                                                        const void* w2, const void* b2, void* out, void* ws) {
  unsigned tid = threadIdx.x;
  asm volatile(
      STAMPENTRY
      "s_load_dwordx16 s[44:59], s[0:1], 0x0\n\t"
      "s_load_dwordx2 s[36:37], s[0:1], 0x40\n\t"
      "s_getpc_b64 s[12:13]\n\t"
      "Lpc_%=:\n\t"
      "s_and_b32 s13, s13, 0xffff\n\t"
      "s_mov_b32 s14, Lend_%=-Lpc_%=\n\t"
      "s_mov_b32 s15, 0x20000\n\t"
      "s_lshr_b32 s4, s2, 3\n\t"
      "s_lshl_b32 s5, s4, 7\n\t"
      "v_lshl_or_b32 v1, v0, 12, s5\n\t"
      "buffer_load_dword v49, v1, s[12:15], 0 offen\n\t"
      "v_and_b32 v2, 7, v0\n\t"
      "v_bfe_u32 v3, v0, 3, 3\n\t"
      "v_lshrrev_b32 v4, 6, v0\n\t"
      "v_and_b32 v5, 63, v0\n\t"
      "s_lshl_b32 s5, s2, 14\n\t"
      "v_lshl_or_b32 v11, v3, 11, s5\n\t"
      "v_lshl_or_b32 v11, v4, 9, v11\n\t"
      "v_xor_b32 v7, v2, v3\n\t"
      "v_lshl_or_b32 v11, v7, 4, v11\n\t"
      "s_mov_b32 s74, 4194304\n\t"
      "s_mov_b32 s75, 0x20000\n\t"
      "s_mov_b32 s42, 6291456\n\t"
      "s_mov_b32 s43, 0x20000\n\t"
      "s_mov_b32 s38, 4194304\n\t"
      "s_mov_b32 s39, 0x20000\n\t"
      "s_mov_b32 s16, 25165824\n\t"
      "s_and_b32 s5, s2, 127\n\t"
      "s_lshl_b32 s5, s5, 2\n\t"
      "v_add_u32 v15, s5, v4\n\t"
      "v_mul_u32_u24 v15, 32, v15\n\t"
      "v_add_lshl_u32 v15, v15, v5, 7\n\t"
      "v_cmp_gt_u32 vcc, 32, v5\n\t"
      "v_bfrev_b32 v6, 1\n\t"
      "v_readfirstlane_b32 s32, v4\n\t"
      "v_cndmask_b32 v15, v6, v15, vcc\n\t"
      "s_lshl_b32 s17, s32, 12\n\t"
      "s_add_u32 s17, s17, 147456\n\t"
      "s_mov_b32 m0, s17\n\t"
      "s_waitcnt lgkmcnt(0)\n\t"
      "s_mov_b32 s72, s44\n\t"
      "s_and_b32 s73, s45, 0xffff\n\t"
      "buffer_load_dwordx4 v11, s[72:75], 0 offen offset:0 lds\n\t"
      "s_add_u32 m0, s17, 896\n\t"
      "s_nop 0\n\t"
      "buffer_load_dwordx4 v11, s[72:75], 0 offen offset:128 lds\n\t"
      "s_add_u32 m0, s17, 1792\n\t"
      "s_nop 0\n\t"
      "buffer_load_dwordx4 v11, s[72:75], 0 offen offset:256 lds\n\t"
      "s_add_u32 m0, s17, 2688\n\t"
      "s_nop 0\n\t"
      "buffer_load_dwordx4 v11, s[72:75], 0 offen offset:384 lds\n\t"
      "v_lshlrev_b32 v47, 4, v7\n\t"
      "v_lshl_or_b32 v47, v2, 7, v47\n\t"
      "v_add_u32 v47, s17, v47\n\t"
      "s_load_dword s8, s[36:37], s16\n\t"
      "s_cmp_lt_u32 s2, 128\n\t"
      "s_cselect_b64 s[40:41], s[46:47], s[50:51]\n\t"
      "s_and_b32 s41, s41, 0xffff\n\t"
      "s_and_b32 s37, s37, 0xffff\n\t"
      "v_lshrrev_b32 v6, 2, v3\n\t"
      "v_lshl_add_u32 v6, v4, 3, v6\n\t"
      "s_lshr_b32 s5, s2, 1\n\t"
      "s_lshl_b32 s5, s5, 10\n\t"
      "v_lshl_add_u32 v12, v6, 17, s5\n\t"
      "v_and_b32 v6, 1, v3\n\t"
      "v_lshl_or_b32 v12, v6, 9, v12\n\t"
      "v_bfe_u32 v6, v3, 1, 1\n\t"
      "v_lshl_or_b32 v12, v6, 8, v12\n\t"
      "s_and_b32 s5, s2, 1\n\t"
      "s_lshl_b32 s5, s5, 7\n\t"
      "v_lshl_or_b32 v6, v2, 4, s5\n\t"
      "v_or_b32 v12, v12, v6\n\t"
      "s_mov_b32 s5, 143360\n\t"
      "v_lshl_add_u32 v14, v2, 5, s5\n\t"
      "v_lshl_add_u32 v13, v4, 3, v14\n\t"
      "s_mov_b32 s80, s48\n\t"
      "s_and_b32 s81, s49, 0xffff\n\t"
      "s_mov_b32 s82, 2097152\n\t"
      "s_mov_b32 s83, 0x20000\n\t"
      "s_mov_b32 s60, s52\n\t"
      "s_and_b32 s61, s53, 0xffff\n\t"
      "s_mov_b32 s62, 7168\n\t"
      "s_mov_b32 s63, 0x20000\n\t"
      "s_mov_b32 s84, s54\n\t"
      "s_and_b32 s85, s55, 0xffff\n\t"
      "s_mov_b32 s86, 7340032\n\t"
      "s_mov_b32 s87, 0x20000\n\t"
      "s_mov_b32 s88, s56\n\t"
      "s_and_b32 s89, s57, 0xffff\n\t"
      "s_mov_b32 s90, 2048\n\t"
      "s_mov_b32 s91, 0x20000\n\t"
      "s_mov_b32 s92, s58\n\t"
      "s_and_b32 s93, s59, 0xffff\n\t"
      "s_mov_b32 s94, 4194304\n\t"
      "s_mov_b32 s95, 0x20000\n\t"
      "s_cmp_lt_u32 s2, 128\n\t"
      "s_cselect_b32 s58, 1, 0\n\t"
      "s_add_u32 s76, s36, 4194304\n\t"
      "s_addc_u32 s77, s37, 0\n\t"
      "s_mov_b32 s78, 18874368\n\t"
      "s_mov_b32 s79, 0x20000\n\t"
      "s_add_u32 s96, s36, 25165824\n\t"
      "s_addc_u32 s97, s37, 0\n\t"
      "s_mov_b32 s98, 135168\n\t"
      "s_mov_b32 s99, 0x20000\n\t"
      "s_and_b32 s5, s2, 1\n\t"
      "s_and_b32 s3, s4, 3\n\t"
      "s_lshl2_add_u32 s64, s5, s3\n\t"
      "s_bfe_u32 s33, s2, 0x20001\n\t"
      "s_bfe_u32 s3, s4, 0x20002\n\t"
      "s_lshl2_add_u32 s65, s33, s3\n\t"
      "s_lshr_b32 s3, s4, 2\n\t"
      "s_and_b32 s5, s33, 1\n\t"
      "s_lshl1_add_u32 s70, s3, s5\n\t"
      "s_lshr_b32 s69, s33, 1\n\t"
      "s_lshl_b32 s34, s69, 11\n\t"
      "s_xor_b32 s35, s34, 2048\n\t"
      "s_lshr_b32 s3, s65, 1\n\t"
      "s_mul_i32 s3, s3, 896\n\t"
      "s_and_b32 s5, s65, 1\n\t"
      "s_lshl_b32 s71, s5, 9\n\t"
      "s_add_u32 s67, s3, s71\n\t"
      "s_lshl_b32 s71, s5, 6\n\t"
      "s_add_u32 s68, s3, s71\n\t"
      "s_add_u32 s68, s68, 384\n\t"
      "s_lshl_b32 s3, s65, 7\n\t"
      "s_and_b32 s71, s65, 3\n\t"
      "s_lshl4_add_u32 s71, s33, s71\n\t"
      "s_lshl_b32 s71, s71, 1\n\t"
      "s_add_u32 s5, s71, 8\n\t"
      "s_add_u32 s10, s65, 128\n\t"
      "s_add_u32 s11, s5, 1\n\t"
      "s_cmp_lg_u32 s58, 0\n\t"
      "s_cselect_b32 s66, 6, 7\n\t"
      "s_cselect_b32 s3, s3, s67\n\t"
      "s_cselect_b32 s30, s5, s71\n\t"
      "s_cselect_b32 s31, s11, s10\n\t"
      "s_cselect_b32 s5, 16, 1\n\t"
      "s_cselect_b32 s10, 0, 16\n\t"
      "s_lshl_b32 s66, s66, 10\n\t"
      "s_lshl_b32 s5, s5, 7\n\t"
      "s_lshl_b32 s30, s30, 17\n\t"
      "s_lshl_b32 s31, s31, 17\n\t"
      "s_lshl_b32 s42, s66, 10\n\t"
      "s_mul_i32 s54, s32, s66\n\t"
      "s_lshl_b32 s71, s54, 4\n\t"
      "s_add_u32 s44, s71, s3\n\t"
      "s_lshl3_add_u32 s45, s66, s44\n\t"
      "s_add_u32 s46, s44, s5\n\t"
      "s_add_u32 s47, s45, s5\n\t"
      "s_add_u32 s48, s46, s5\n\t"
      "s_add_u32 s49, s47, s5\n\t"
      "s_add_u32 s50, s71, s68\n\t"
      "s_add_u32 s44, s44, 4096\n\t"
      "s_add_u32 s45, s45, 3072\n\t"
      "s_add_u32 s46, s46, 2048\n\t"
      "s_add_u32 s47, s47, 1024\n\t"
      "s_add_u32 s48, s48, 4096\n\t"
      "s_add_u32 s49, s49, 3072\n\t"
      "s_add_u32 s50, s50, 2048\n\t"
      "s_mov_b32 s51, 131072\n\t"
      "s_lshl_b32 s52, s66, 6\n\t"
      "s_lshl2_add_u32 s53, s66, s54\n\t"
      "s_mul_i32 s3, s66, 20\n\t"
      "s_add_u32 s55, s54, s3\n\t"
      "s_lshl_b32 s56, s32, 9\n\t"
      "s_add_u32 s56, s56, 143616\n\t"
      "s_lshl_b32 s59, s2, 7\n\t"
      "s_add_u32 s59, s59, 4096\n\t"
      "s_lshl_b32 s3, s32, 15\n\t"
      "s_lshl_b32 s5, s70, 7\n\t"
      "s_add_u32 s20, s3, s5\n\t"
      "s_add_u32 s21, s20, 16384\n\t"
      "s_xor_b32 s22, s20, 128\n\t"
      "s_xor_b32 s23, s21, 128\n\t"
      "s_add_u32 s20, s20, 4096\n\t"
      "s_add_u32 s21, s21, 3072\n\t"
      "s_add_u32 s22, s22, 2048\n\t"
      "s_add_u32 s23, s23, 1024\n\t"
      "s_lshl_b32 s25, s32, 12\n\t"
      "s_add_u32 s24, s25, 98304\n\t"
      "s_add_u32 s26, s25, 131072\n\t"
      "s_lshl_b32 s3, s64, 5\n\t"
      "s_add_u32 s3, s3, s10\n\t"
      "s_add_u32 s3, s3, s65\n\t"
      "s_lshl_b32 s3, s3, 7\n\t"
      "s_add_u32 s27, s3, 36864\n\t"
      "s_lshl2_add_u32 s11, s64, s32\n\t"
      "s_cmp_eq_u32 s11, 0\n\t"
      "s_cselect_b32 s28, 0xf149f2ca, 0\n\t"
      "s_lshl2_add_u32 s3, s2, s32\n\t"
      "s_cmp_eq_u32 s3, 1\n\t"
      "s_cselect_b32 s29, 1, 0\n\t"
      "v_mov_b32 v60, v5\n\t"
      "v_mov_b32 v58, v2\n\t"
      "v_mov_b32 v59, v3\n\t"
      "v_lshrrev_b32 v50, 4, v60\n\t"
      "v_bfe_u32 v51, v60, 2, 2\n\t"
      "v_and_b32 v52, 3, v60\n\t"
      "v_and_b32 v53, 15, v60\n\t"
      "v_lshlrev_b32 v57, 4, v60\n\t"
      "v_bfe_u32 v54, v60, 3, 2\n\t"
      "v_bfe_u32 v61, v60, 2, 3\n\t"
      "v_lshlrev_b32 v55, 7, v61\n\t"
      "v_lshrrev_b32 v61, 5, v60\n\t"
      "v_lshl_or_b32 v55, v61, 10, v55\n\t"
      "v_lshl_or_b32 v55, v52, 3, v55\n\t"
      "v_lshrrev_b32 v62, 2, v60\n\t"
      "v_lshlrev_b32 v56, 6, v62\n\t"
      "v_lshl_or_b32 v56, v52, 3, v56\n\t"
      "s_lshl_b32 s3, s11, 12\n\t"
      "v_or_b32 v0, s3, v57\n\t"
      "v_mov_b32 v22, v0\n\t"
      "s_lshl_b32 s3, s64, 4\n\t"
      "s_sub_u32 s3, s3, 1\n\t"
      "s_max_i32 s3, s3, 0\n\t"
      "s_lshl_b32 s3, s3, 10\n\t"
      "v_or_b32 v1, s3, v57\n\t"
      "v_lshlrev_b32 v61, 1, v50\n\t"
      "v_xor_b32 v61, v61, v58\n\t"
      "v_lshlrev_b32 v61, 4, v61\n\t"
      "v_mad_u32_u24 v2, v59, s66, v61\n\t"
      "v_lshl_add_u32 v17, v59, 11, v61\n\t"
      "v_and_b32 v63, 1, v50\n\t"
      "v_lshlrev_b32 v61, 1, v63\n\t"
      "v_xor_b32 v61, v61, v52\n\t"
      "v_lshlrev_b32 v61, 4, v61\n\t"
      "v_mad_u32_u24 v3, v62, s66, v61\n\t"
      "s_lshl_b32 s5, s66, 2\n\t"
      "v_xor_b32 v61, 0, v54\n\t"
      "v_lshl_add_u32 v61, v61, 5, v55\n\t"
      "v_add_u32 v4, s5, v61\n\t"
      "v_add_u32 v18, 98304, v61\n\t"
      "v_xor_b32 v61, 1, v54\n\t"
      "v_lshl_add_u32 v61, v61, 5, v55\n\t"
      "v_add_u32 v5, s5, v61\n\t"
      "v_add_u32 v19, 98304, v61\n\t"
      "v_xor_b32 v61, 2, v54\n\t"
      "v_lshl_add_u32 v61, v61, 5, v55\n\t"
      "v_add_u32 v6, s5, v61\n\t"
      "v_add_u32 v20, 98304, v61\n\t"
      "v_xor_b32 v61, 3, v54\n\t"
      "v_lshl_add_u32 v61, v61, 5, v55\n\t"
      "v_add_u32 v7, s5, v61\n\t"
      "v_add_u32 v21, 98304, v61\n\t"
      "v_xor_b32 v61, s32, v54\n\t"
      "v_lshl_add_u32 v61, v61, 5, v55\n\t"
      "v_lshl_add_u32 v62, v63, 5, v56\n\t"
      "v_xor_b32 v9, 32, v62\n\t"
      "s_cmp_lg_u32 s58, 0\n\t"
      "s_cselect_b64 vcc, -1, 0\n\t"
      "v_cndmask_b32 v8, v62, v61, vcc\n\t"
      "v_add_u32 v8, s5, v8\n\t"
      "v_add_u32 v9, s5, v9\n\t"
      "v_lshl_add_u32 v10, v50, 3, s56\n\t"
      "v_mov_b32 v16, v0\n\t"
      "s_lshl1_add_u32 s3, s11, s69\n\t"
      "s_lshl_b32 s3, s3, 1\n\t"
      "v_lshrrev_b32 v61, 1, v50\n\t"
      "v_lshlrev_b32 v61, 4, v61\n\t"
      "v_lshl_or_b32 v61, v63, 5, v61\n\t"
      "s_lshl_b32 s5, s70, 7\n\t"
      "v_or_b32 v25, s5, v61\n\t"
      "s_lshl_b32 s3, s3, 15\n\t"
      "s_add_u32 s70, s3, s5\n\t"
      "v_min_u32 v61, 33, v60\n\t"
      "s_lshl_b32 s3, s64, 5\n\t"
      "s_sub_u32 s3, s3, 2\n\t"
      "v_add_u32 v61, s3, v61\n\t"
      "v_max_i32 v61, 0, v61\n\t"
      "v_lshlrev_b32 v61, 7, v61\n\t"
      "v_add_u32 v26, 4096, v61\n\t"
      "s_lshl3_add_u32 s3, s64, s33\n\t"
      "s_lshl_b32 s3, s3, 9\n\t"
      "s_add_u32 s3, s3, 36864\n\t"
      "v_lshl_add_u32 v27, v52, 7, s3\n\t"
      "s_lshl2_add_u32 s3, s2, s32\n\t"
      "s_lshl_b32 s3, s3, 11\n\t"
      "v_lshl_add_u32 v61, v60, 7, s3\n\t"
      "v_bfrev_b32 v62, 1\n\t"
      "v_cmp_gt_u32 vcc, 16, v60\n\t"
      "s_nop 1\n\t"
      "v_cndmask_b32 v28, v62, v61, vcc\n\t"
      "s_lshl_b32 s3, s32, 12\n\t"
      "s_add_u32 s3, s3, 131072\n\t"
      "v_lshl_add_u32 v29, v60, 3, s3\n\t"
      "s_add_u32 s3, s3, 4096\n\t"
      "s_cmp_eq_u32 s32, 3\n\t"
      "s_cselect_b32 s3, 65536, s3\n\t"
      "v_lshl_add_u32 v24, v60, 3, s3\n\t"
      "s_lshl_b32 s3, s32, 9\n\t"
      "s_add_u32 s3, s3, 131072\n\t"
      "v_lshl_add_u32 v30, v60, 3, s3\n\t"
      "s_cmp_lg_u32 s58, 0\n\t"
      "s_mov_b32 s3, 4272246840\n\t"
      "s_cselect_b32 s3, 4289352705, s3\n\t"
      "s_mov_b32 s5, 3885911616\n\t"
      "s_cselect_b32 s5, 3840206736, s5\n\t"
      "v_lshlrev_b32 v61, 1, v53\n\t"
      "v_lshrrev_b32 v62, v61, s3\n\t"
      "v_and_b32 v62, 3, v62\n\t"
      "v_lshrrev_b32 v61, v61, s5\n\t"
      "v_add_u32 v61, s65, v61\n\t"
      "v_and_b32 v61, 3, v61\n\t"
      "v_lshlrev_b32 v63, 2, v62\n\t"
      "v_add_u32 v63, v63, v61\n\t"
      "s_lshl_b32 s3, s33, 4\n\t"
      "v_add_lshl_u32 v63, v63, s3, 18\n\t"
      "v_lshrrev_b32 v54, 1, v61\n\t"
      "v_mul_u32_u24 v54, 448, v54\n\t"
      "v_and_b32 v55, 1, v61\n\t"
      "v_lshl_add_u32 v54, v55, 8, v54\n\t"
      "v_max_u32 v55, 1, v62\n\t"
      "v_lshl_add_u32 v54, v55, 6, v54\n\t"
      "s_mul_i32 s3, s33, 896\n\t"
      "s_sub_u32 s3, s3, 64\n\t"
      "v_add_lshl_u32 v54, v54, s3, 11\n\t"
      "s_lshl_b32 s3, s33, 19\n\t"
      "s_or_b32 s3, s3, 1\n\t"
      "v_lshl_add_u32 v55, v61, 17, s3\n\t"
      "v_cmp_eq_u32 vcc, 1, v62\n\t"
      "s_nop 1\n\t"
      "v_cndmask_b32 v54, v54, v55, vcc\n\t"
      "v_cmp_gt_u32 vcc, 32, v60\n\t"
      "s_nop 1\n\t"
      "v_cndmask_b32 v23, v63, v54, vcc\n\t"
      "s_mul_i32 s3, s33, 1835008\n\t"
      "s_add_u32 s3, s3, 393216\n\t"
      "v_writelane_b32 v23, s3, 16\n\t"
      "s_add_u32 s3, s3, 917504\n\t"
      "v_writelane_b32 v23, s3, 17\n\t"
      "s_lshl_b32 s3, s33, 19\n\t"
      "s_add_u32 s3, s3, 16777216\n\t"
      "v_writelane_b32 v23, s3, 48\n\t"
      "s_add_u32 s3, s3, 262144\n\t"
      "v_writelane_b32 v23, s3, 49\n\t"
      "v_lshl_add_u32 v61, v60, 2, s67\n\t"
      "v_add_u32 v62, 256, v61\n\t"
      "v_lshl_add_u32 v63, v53, 2, s68\n\t"
      "v_cmp_gt_u32 vcc, 32, v60\n\t"
      "s_mov_b32 m0, s56\n\t"
      "s_nop 0\n\t"
      "v_cndmask_b32 v62, v63, v62, vcc\n\t"
      "buffer_load_dword v61, s[60:63], 0 offen lds\n\t"
      "s_add_u32 m0, s56, 256\n\t"
      "s_nop 0\n\t"
      "buffer_load_dword v62, s[60:63], 0 offen lds\n\t"
      "buffer_load_dword v15, v15, s[40:43], 0 offen\n\t"
      STAMP(0, 1)
      "s_waitcnt lgkmcnt(0)\n\t"
      "s_waitcnt vmcnt(6)\n\t"
      "ds_read_b128 v[32:35], v47 offset:0\n\t"
      "s_waitcnt vmcnt(5)\n\t"
      "ds_read_b128 v[36:39], v47 offset:1024\n\t"
      "s_waitcnt lgkmcnt(1)\n\t"
      "v_lshlrev_b32 v64, 16, v32\n\t"
      "v_and_b32 v65, 0xffff0000, v32\n\t"
      "v_lshlrev_b32 v66, 16, v33\n\t"
      "v_and_b32 v67, 0xffff0000, v33\n\t"
      "v_lshlrev_b32 v68, 16, v34\n\t"
      "v_and_b32 v69, 0xffff0000, v34\n\t"
      "v_lshlrev_b32 v70, 16, v35\n\t"
      "v_and_b32 v71, 0xffff0000, v35\n\t"
      "s_waitcnt vmcnt(4)\n\t"
      "ds_read_b128 v[40:43], v47 offset:2048\n\t"
      "s_waitcnt lgkmcnt(1)\n\t"
      "v_lshlrev_b32 v72, 16, v36\n\t"
      "v_and_b32 v73, 0xffff0000, v36\n\t"
      "v_lshlrev_b32 v74, 16, v37\n\t"
      "v_and_b32 v75, 0xffff0000, v37\n\t"
      "v_lshlrev_b32 v76, 16, v38\n\t"
      "v_and_b32 v77, 0xffff0000, v38\n\t"
      LSTW(0, "")
      "v_lshlrev_b32 v78, 16, v39\n\t"
      "v_and_b32 v79, 0xffff0000, v39\n\t"
      "s_waitcnt vmcnt(3)\n\t"
      "ds_read_b128 v[44:47], v47 offset:3072\n\t"
      "s_waitcnt lgkmcnt(1)\n\t"
      "v_lshlrev_b32 v80, 16, v40\n\t"
      "v_and_b32 v81, 0xffff0000, v40\n\t"
      "v_lshlrev_b32 v82, 16, v41\n\t"
      "v_and_b32 v83, 0xffff0000, v41\n\t"
      "v_lshlrev_b32 v84, 16, v42\n\t"
      "v_and_b32 v85, 0xffff0000, v42\n\t"
      "v_lshlrev_b32 v86, 16, v43\n\t"
      "v_and_b32 v87, 0xffff0000, v43\n\t"
      "s_waitcnt lgkmcnt(0)\n\t"
      "v_lshlrev_b32 v88, 16, v44\n\t"
      "v_and_b32 v89, 0xffff0000, v44\n\t"
      "v_lshlrev_b32 v90, 16, v45\n\t"
      "v_and_b32 v91, 0xffff0000, v45\n\t"
      "v_lshlrev_b32 v92, 16, v46\n\t"
      "v_and_b32 v93, 0xffff0000, v46\n\t"
      "v_lshlrev_b32 v94, 16, v47\n\t"
      "v_and_b32 v95, 0xffff0000, v47\n\t"
      STAMP(16, 17)
      "v_pk_add_f32 v[100:101], v[64:65], v[72:73]\n\t"
      "v_pk_add_f32 v[102:103], v[66:67], v[74:75]\n\t"
      "v_pk_add_f32 v[104:105], v[68:69], v[76:77]\n\t"
      "v_pk_add_f32 v[106:107], v[70:71], v[78:79]\n\t"
      "v_pk_add_f32 v[100:101], v[100:101], v[80:81]\n\t"
      "v_pk_add_f32 v[102:103], v[102:103], v[82:83]\n\t"
      "v_pk_add_f32 v[104:105], v[104:105], v[84:85]\n\t"
      "v_pk_add_f32 v[106:107], v[106:107], v[86:87]\n\t"
      "v_pk_add_f32 v[100:101], v[100:101], v[88:89]\n\t"
      "v_pk_add_f32 v[102:103], v[102:103], v[90:91]\n\t"
      "v_pk_add_f32 v[104:105], v[104:105], v[92:93]\n\t"
      "v_pk_add_f32 v[106:107], v[106:107], v[94:95]\n\t"
      "v_pk_add_f32 v[100:101], v[100:101], v[102:103]\n\t"
      "v_pk_add_f32 v[104:105], v[104:105], v[106:107]\n\t"
      "v_pk_add_f32 v[100:101], v[100:101], v[104:105]\n\t"
      "v_add_f32 v100, v100, v101\n\t"
      "s_nop 1\n\t"
      "v_add_f32_dpp v100, v100, v100 row_ror:8 row_mask:0xf bank_mask:0xf\n\t"
      "v_mov_b32 v120, v100\n\t"
      "s_nop 1\n\t"
      "v_permlane16_swap_b32 v100, v120\n\t"
      "v_add_f32 v100, v100, v120\n\t"
      "v_mov_b32 v120, v100\n\t"
      LSTW(16, "")
      "s_nop 1\n\t"
      "v_permlane32_swap_b32 v100, v120\n\t"
      "v_add_f32 v100, v100, v120\n\t"
      "v_mul_f32 v110, 0x3b800000, v100\n\t"
      "v_mul_f32 v112, 0xbb800000, v100\n\t"
      "v_mov_b32 v113, v112\n\t"
      "v_pk_add_f32 v[64:65], v[64:65], v[112:113]\n\t"
      "v_pk_add_f32 v[66:67], v[66:67], v[112:113]\n\t"
      "v_pk_add_f32 v[68:69], v[68:69], v[112:113]\n\t"
      "v_pk_add_f32 v[70:71], v[70:71], v[112:113]\n\t"
      "v_pk_add_f32 v[72:73], v[72:73], v[112:113]\n\t"
      "v_pk_add_f32 v[74:75], v[74:75], v[112:113]\n\t"
      "v_pk_add_f32 v[76:77], v[76:77], v[112:113]\n\t"
      "v_pk_add_f32 v[78:79], v[78:79], v[112:113]\n\t"
      "v_pk_add_f32 v[80:81], v[80:81], v[112:113]\n\t"
      "v_pk_add_f32 v[82:83], v[82:83], v[112:113]\n\t"
      "v_pk_add_f32 v[84:85], v[84:85], v[112:113]\n\t"
      "v_pk_add_f32 v[86:87], v[86:87], v[112:113]\n\t"
      "v_pk_add_f32 v[88:89], v[88:89], v[112:113]\n\t"
      "v_pk_add_f32 v[90:91], v[90:91], v[112:113]\n\t"
      "v_pk_add_f32 v[92:93], v[92:93], v[112:113]\n\t"
      "v_pk_add_f32 v[94:95], v[94:95], v[112:113]\n\t"
      "v_pk_mul_f32 v[100:101], v[64:65], v[64:65]\n\t"
      "v_pk_mul_f32 v[102:103], v[66:67], v[66:67]\n\t"
      "v_pk_mul_f32 v[104:105], v[68:69], v[68:69]\n\t"
      "v_pk_mul_f32 v[106:107], v[70:71], v[70:71]\n\t"
      "v_pk_fma_f32 v[100:101], v[72:73], v[72:73], v[100:101]\n\t"
      "v_pk_fma_f32 v[102:103], v[74:75], v[74:75], v[102:103]\n\t"
      "v_pk_fma_f32 v[104:105], v[76:77], v[76:77], v[104:105]\n\t"
      "v_pk_fma_f32 v[106:107], v[78:79], v[78:79], v[106:107]\n\t"
      "v_pk_fma_f32 v[100:101], v[80:81], v[80:81], v[100:101]\n\t"
      "v_pk_fma_f32 v[102:103], v[82:83], v[82:83], v[102:103]\n\t"
      "v_pk_fma_f32 v[104:105], v[84:85], v[84:85], v[104:105]\n\t"
      "v_pk_fma_f32 v[106:107], v[86:87], v[86:87], v[106:107]\n\t"
      "v_pk_fma_f32 v[100:101], v[88:89], v[88:89], v[100:101]\n\t"
      "v_pk_fma_f32 v[102:103], v[90:91], v[90:91], v[102:103]\n\t"
      "v_pk_fma_f32 v[104:105], v[92:93], v[92:93], v[104:105]\n\t"
      "v_pk_fma_f32 v[106:107], v[94:95], v[94:95], v[106:107]\n\t"
      "v_pk_add_f32 v[100:101], v[100:101], v[102:103]\n\t"
      "v_pk_add_f32 v[104:105], v[104:105], v[106:107]\n\t"
      "v_pk_add_f32 v[100:101], v[100:101], v[104:105]\n\t"
      "v_add_f32 v111, v100, v101\n\t"
      "s_nop 1\n\t"
      "v_add_f32_dpp v111, v111, v111 row_ror:8 row_mask:0xf bank_mask:0xf\n\t"
      "v_mov_b32 v120, v111\n\t"
      "s_nop 1\n\t"
      "v_permlane16_swap_b32 v111, v120\n\t"
      "v_add_f32 v111, v111, v120\n\t"
      "v_mov_b32 v120, v111\n\t"
      "s_nop 1\n\t"
      "v_permlane32_swap_b32 v111, v120\n\t"
      "v_add_f32 v111, v111, v120\n\t"
      "ds_write_b64 v13, v[110:111]\n\t"
      "s_waitcnt lgkmcnt(0)\n\t"
      "s_barrier\n\t"
      "ds_read_b128 v[124:127], v14\n\t"
      "ds_read_b128 v[128:131], v14 offset:16\n\t"
      "s_add_u32 s8, s8, 1\n\t"
      "s_not_b32 s9, s8\n\t"
      "s_mov_b32 s16, 262144\n\t"
      "s_mov_b32 s17, 524288\n\t"
      "s_mov_b32 s18, 786432\n\t"
      "s_waitcnt lgkmcnt(0)\n\t"
      "v_pk_add_f32 v[100:101], v[124:125], v[126:127]\n\t"
      "v_pk_add_f32 v[102:103], v[128:129], v[130:131]\n\t"
      "v_pk_add_f32 v[100:101], v[100:101], v[102:103]\n\t"
      "v_mul_f32 v100, 0x3e800000, v100\n\t"
      "v_sub_f32 v102, v124, v100\n\t"
      "v_sub_f32 v103, v126, v100\n\t"
      "v_sub_f32 v104, v128, v100\n\t"
      "v_sub_f32 v105, v130, v100\n\t"
      "v_mul_f32 v106, v102, v102\n\t"
      "v_fmac_f32 v106, v103, v103\n\t"
      "v_fmac_f32 v106, v104, v104\n\t"
      "v_fmac_f32 v106, v105, v105\n\t"
      "v_mov_b32 v107, 0x3727c5ac\n\t"
      "v_fmac_f32 v107, 0x3a800000, v101\n\t"
      "v_fmac_f32 v107, 0x3e800000, v106\n\t"
      "v_rsq_f32 v108, v107\n\t"
      "v_sub_f32 v114, v110, v100\n\t"
      "v_mov_b32 v109, v108\n\t"
      "v_mul_f32 v114, v114, v108\n\t"
      "v_mov_b32 v115, v114\n\t"
      "v_pk_fma_f32 v[64:65], v[64:65], v[108:109], v[114:115]\n\t"
      "v_pk_fma_f32 v[66:67], v[66:67], v[108:109], v[114:115]\n\t"
      "v_pk_fma_f32 v[68:69], v[68:69], v[108:109], v[114:115]\n\t"
      "v_pk_fma_f32 v[70:71], v[70:71], v[108:109], v[114:115]\n\t"
      "v_cvt_pk_bf16_f32 v32, v64, v65\n\t"
      "v_cvt_pk_bf16_f32 v33, v66, v67\n\t"
      "v_cvt_pk_bf16_f32 v34, v68, v69\n\t"
      "v_cvt_pk_bf16_f32 v35, v70, v71\n\t"
      "v_pk_fma_f32 v[72:73], v[72:73], v[108:109], v[114:115]\n\t"
      "v_pk_fma_f32 v[74:75], v[74:75], v[108:109], v[114:115]\n\t"
      "v_pk_fma_f32 v[76:77], v[76:77], v[108:109], v[114:115]\n\t"
      "v_pk_fma_f32 v[78:79], v[78:79], v[108:109], v[114:115]\n\t"
      "v_cvt_pk_bf16_f32 v36, v72, v73\n\t"
      "v_cvt_pk_bf16_f32 v37, v74, v75\n\t"
      "v_cvt_pk_bf16_f32 v38, v76, v77\n\t"
      "v_cvt_pk_bf16_f32 v39, v78, v79\n\t"
      "v_permlane16_swap_b32 v32, v34\n\t"
      "v_permlane16_swap_b32 v33, v35\n\t"
      "buffer_store_dwordx4 v[32:35], v12, s[36:39], 0 offen sc1\n\t"
      "v_pk_fma_f32 v[80:81], v[80:81], v[108:109], v[114:115]\n\t"
      "v_pk_fma_f32 v[82:83], v[82:83], v[108:109], v[114:115]\n\t"
      "v_pk_fma_f32 v[84:85], v[84:85], v[108:109], v[114:115]\n\t"
      "v_pk_fma_f32 v[86:87], v[86:87], v[108:109], v[114:115]\n\t"
      "v_cvt_pk_bf16_f32 v40, v80, v81\n\t"
      "v_cvt_pk_bf16_f32 v41, v82, v83\n\t"
      "v_cvt_pk_bf16_f32 v42, v84, v85\n\t"
      "v_cvt_pk_bf16_f32 v43, v86, v87\n\t"
      "v_permlane16_swap_b32 v36, v38\n\t"
      "v_permlane16_swap_b32 v37, v39\n\t"
      "buffer_store_dwordx4 v[36:39], v12, s[36:39], s16 offen sc1\n\t"
      "v_pk_fma_f32 v[88:89], v[88:89], v[108:109], v[114:115]\n\t"
      "v_pk_fma_f32 v[90:91], v[90:91], v[108:109], v[114:115]\n\t"
      "v_pk_fma_f32 v[92:93], v[92:93], v[108:109], v[114:115]\n\t"
      "v_pk_fma_f32 v[94:95], v[94:95], v[108:109], v[114:115]\n\t"
      "v_cvt_pk_bf16_f32 v44, v88, v89\n\t"
      "v_cvt_pk_bf16_f32 v45, v90, v91\n\t"
      "v_cvt_pk_bf16_f32 v46, v92, v93\n\t"
      "v_cvt_pk_bf16_f32 v47, v94, v95\n\t"
      "v_permlane16_swap_b32 v40, v42\n\t"
      "v_permlane16_swap_b32 v41, v43\n\t"
      "buffer_store_dwordx4 v[40:43], v12, s[36:39], s17 offen sc1\n\t"
      "v_permlane16_swap_b32 v44, v46\n\t"
      "v_permlane16_swap_b32 v45, v47\n\t"
      "buffer_store_dwordx4 v[44:47], v12, s[36:39], s18 offen sc1\n\t"
      "s_nop 1\n\t"
      STAMP(18, 19)
      "s_cmp_lg_u32 s58, 0\n\t"
      LSTW(18, "s_waitcnt lgkmcnt(0)\n\t")
      "s_cbranch_scc1 Lqkv_%=\n\t"
      "s_waitcnt vmcnt(5)\n\t"
      "ds_read_b64 v[46:47], v10 offset:0\n\t"
      "ds_read_b64 v[62:63], v10 offset:32\n\t"
      "ds_read_b64 v[78:79], v10 offset:64\n\t"
      "ds_read_b64 v[94:95], v10 offset:96\n\t"
      "ds_read_b64 v[110:111], v10 offset:128\n\t"
      "ds_read_b64 v[126:127], v10 offset:160\n\t"
      "ds_read_b64 v[142:143], v10 offset:192\n\t"
      "ds_read_b64 v[158:159], v10 offset:224\n\t"
      "ds_read_b64 v[174:175], v10 offset:256\n\t"
      "ds_read_b64 v[190:191], v10 offset:288\n\t"
      "ds_read_b64 v[206:207], v10 offset:320\n\t"
      "ds_read_b64 v[222:223], v10 offset:352\n\t"
      "ds_read_b64 v[238:239], v10 offset:384\n\t"
      "ds_read_b64 v[254:255], v10 offset:416\n\t"
      "s_mov_b32 s64, 2\n\t"
      "s_mov_b32 s66, -114688\n\t"
      "s_mov_b32 s67, 28672\n\t"
      "s_mov_b32 s68, 6\n\t"
      "s_add_u32 s60, s36, 33554432\n\t"
      "s_addc_u32 s61, s37, 0\n\t"
      "s_mov_b32 s62, 16777216\n\t"
      "v_add_u32 v29, s35, v22\n\t"
      "v_add_u32 v22, s34, v22\n\t"
      "s_add_u32 s53, s53, 28672\n\t"
      "v_add_u32 v4, 28672, v4\n\t"
      "v_add_u32 v5, 28672, v5\n\t"
      "v_add_u32 v6, 28672, v6\n\t"
      "v_add_u32 v7, 28672, v7\n\t"
      "v_add_u32 v8, 28672, v8\n\t"
      "v_add_u32 v9, 28672, v9\n\t"
      "v_mov_b32 v14, 0\n\t"
      "v_mov_b32 v11, s80\n\t"
      "v_mov_b32 v13, s81\n\t"
      "s_cmp_lg_u32 s58, 0\n\t"
      "s_cselect_b32 s3, 2, 1\n\t"
      "s_cselect_b32 s5, 4, 6\n\t"
      "s_lshl_b32 s3, s3, 20\n\t"
      "s_lshl_b32 s5, s5, 20\n\t"
      "s_add_u32 s10, s40, s3\n\t"
      "s_addc_u32 s11, s41, 0\n\t"
      "v_writelane_b32 v11, s10, 0\n\t"
      "v_writelane_b32 v13, s11, 0\n\t"
      "v_writelane_b32 v14, s5, 0\n\t"
      "s_add_u32 s10, s10, 1048576\n\t"
      "s_addc_u32 s11, s11, 0\n\t"
      "s_sub_u32 s5, s5, 1048576\n\t"
      "v_writelane_b32 v11, s10, 1\n\t"
      "v_writelane_b32 v13, s11, 1\n\t"
      "v_writelane_b32 v14, s5, 1\n\t"
      "s_add_u32 s10, s10, 1048576\n\t"
      "s_addc_u32 s11, s11, 0\n\t"
      "s_sub_u32 s5, s5, 1048576\n\t"
      "v_writelane_b32 v11, s10, 2\n\t"
      "v_writelane_b32 v13, s11, 2\n\t"
      "v_writelane_b32 v14, s5, 2\n\t"
      "s_add_u32 s10, s10, 1048576\n\t"
      "s_addc_u32 s11, s11, 0\n\t"
      "s_sub_u32 s5, s5, 1048576\n\t"
      "v_writelane_b32 v11, s10, 3\n\t"
      "v_writelane_b32 v13, s11, 3\n\t"
      "v_writelane_b32 v14, s5, 3\n\t"
      "s_add_u32 s10, s10, 1048576\n\t"
      "s_addc_u32 s11, s11, 0\n\t"
      "s_sub_u32 s5, s5, 1048576\n\t"
      "v_writelane_b32 v11, s10, 4\n\t"
      "v_writelane_b32 v13, s11, 4\n\t"
      "v_writelane_b32 v14, s5, 4\n\t"
      "v_writelane_b32 v14, s82, 5\n\t"
      "s_add_u32 s10, s84, 0\n\t"
      "s_addc_u32 s11, s85, 0\n\t"
      "s_mov_b32 s5, 7340032\n\t"
      "v_writelane_b32 v11, s10, 6\n\t"
      "v_writelane_b32 v13, s11, 6\n\t"
      "v_writelane_b32 v14, s5, 6\n\t"
      "s_add_u32 s10, s84, 2097152\n\t"
      "s_addc_u32 s11, s85, 0\n\t"
      "s_mov_b32 s5, 5242880\n\t"
      "v_writelane_b32 v11, s10, 7\n\t"
      "v_writelane_b32 v13, s11, 7\n\t"
      "v_writelane_b32 v14, s5, 7\n\t"
      "s_add_u32 s10, s84, 4194304\n\t"
      "s_addc_u32 s11, s85, 0\n\t"
      "s_mov_b32 s5, 3145728\n\t"
      "v_writelane_b32 v11, s10, 8\n\t"
      "v_writelane_b32 v13, s11, 8\n\t"
      "v_writelane_b32 v14, s5, 8\n\t"
      "s_add_u32 s10, s84, 6291456\n\t"
      "s_addc_u32 s11, s85, 0\n\t"
      "s_mov_b32 s5, 1048576\n\t"
      "v_writelane_b32 v11, s10, 9\n\t"
      "v_writelane_b32 v13, s11, 9\n\t"
      "v_writelane_b32 v14, s5, 9\n\t"
      "s_lshl2_add_u32 s3, s4, s32\n\t"
      "s_lshl_b32 s3, s3, 11\n\t"
      "s_lshl2_add_u32 s5, s2, s32\n\t"
      "s_lshl_b32 s5, s5, 11\n\t"
      "s_sub_u32 s5, s5, s3\n\t"
      "s_sub_u32 s10, s12, s5\n\t"
      "s_subb_u32 s11, s13, 0\n\t"
      "s_add_u32 s5, s5, s14\n\t"
      "v_writelane_b32 v11, s10, 10\n\t"
      "v_writelane_b32 v13, s11, 10\n\t"
      "v_writelane_b32 v14, s5, 10\n\t"
      "s_mov_b32 s14, 0\n\t"
      "s_mov_b32 s19, 0\n\t"
      "s_add_u32 s57, s53, 4096\n\t"
      "s_sub_u32 s40, s40, 4096\n\t"
      "s_subb_u32 s41, s41, 0\n\t"
      "s_add_u32 s42, s42, 4096\n\t"
      "s_waitcnt lgkmcnt(0)\n\t"
      "v_lshlrev_b32 v32, 16, v46\n\t"
      "v_and_b32 v33, 0xffff0000, v46\n\t"
      "v_lshlrev_b32 v34, 16, v47\n\t"
      "v_and_b32 v35, 0xffff0000, v47\n\t"
      "v_mov_b64 v[36:37], v[32:33]\n\t"
      "v_mov_b64 v[38:39], v[34:35]\n\t"
      "v_mov_b64 v[40:41], v[32:33]\n\t"
      "v_mov_b64 v[42:43], v[34:35]\n\t"
      "v_mov_b64 v[44:45], v[32:33]\n\t"
      "v_mov_b64 v[46:47], v[34:35]\n\t"
      "v_lshlrev_b32 v48, 16, v62\n\t"
      "v_and_b32 v49, 0xffff0000, v62\n\t"
      "v_lshlrev_b32 v50, 16, v63\n\t"
      "v_and_b32 v51, 0xffff0000, v63\n\t"
      "v_mov_b64 v[52:53], v[48:49]\n\t"
      "v_mov_b64 v[54:55], v[50:51]\n\t"
      "v_mov_b64 v[56:57], v[48:49]\n\t"
      "v_mov_b64 v[58:59], v[50:51]\n\t"
      "v_mov_b64 v[60:61], v[48:49]\n\t"
      "v_mov_b64 v[62:63], v[50:51]\n\t"
      "v_lshlrev_b32 v64, 16, v78\n\t"
      "v_and_b32 v65, 0xffff0000, v78\n\t"
      "v_lshlrev_b32 v66, 16, v79\n\t"
      "v_and_b32 v67, 0xffff0000, v79\n\t"
      "v_mov_b64 v[68:69], v[64:65]\n\t"
      "v_mov_b64 v[70:71], v[66:67]\n\t"
      "v_mov_b64 v[72:73], v[64:65]\n\t"
      "v_mov_b64 v[74:75], v[66:67]\n\t"
      "v_mov_b64 v[76:77], v[64:65]\n\t"
      "v_mov_b64 v[78:79], v[66:67]\n\t"
      "v_lshlrev_b32 v80, 16, v94\n\t"
      "v_and_b32 v81, 0xffff0000, v94\n\t"
      "v_lshlrev_b32 v82, 16, v95\n\t"
      "v_and_b32 v83, 0xffff0000, v95\n\t"
      "v_mov_b64 v[84:85], v[80:81]\n\t"
      "v_mov_b64 v[86:87], v[82:83]\n\t"
      "v_mov_b64 v[88:89], v[80:81]\n\t"
      "v_mov_b64 v[90:91], v[82:83]\n\t"
      "v_mov_b64 v[92:93], v[80:81]\n\t"
      "v_mov_b64 v[94:95], v[82:83]\n\t"
      "v_lshlrev_b32 v96, 16, v110\n\t"
      "v_and_b32 v97, 0xffff0000, v110\n\t"
      "v_lshlrev_b32 v98, 16, v111\n\t"
      "v_and_b32 v99, 0xffff0000, v111\n\t"
      "v_mov_b64 v[100:101], v[96:97]\n\t"
      "v_mov_b64 v[102:103], v[98:99]\n\t"
      "v_mov_b64 v[104:105], v[96:97]\n\t"
      "v_mov_b64 v[106:107], v[98:99]\n\t"
      "v_mov_b64 v[108:109], v[96:97]\n\t"
      "v_mov_b64 v[110:111], v[98:99]\n\t"
      "v_lshlrev_b32 v112, 16, v126\n\t"
      "v_and_b32 v113, 0xffff0000, v126\n\t"
      "v_lshlrev_b32 v114, 16, v127\n\t"
      "v_and_b32 v115, 0xffff0000, v127\n\t"
      "v_mov_b64 v[116:117], v[112:113]\n\t"
      "v_mov_b64 v[118:119], v[114:115]\n\t"
      "v_mov_b64 v[120:121], v[112:113]\n\t"
      "v_mov_b64 v[122:123], v[114:115]\n\t"
      "v_mov_b64 v[124:125], v[112:113]\n\t"
      "v_mov_b64 v[126:127], v[114:115]\n\t"
      "v_lshlrev_b32 v128, 16, v142\n\t"
      "v_and_b32 v129, 0xffff0000, v142\n\t"
      "v_lshlrev_b32 v130, 16, v143\n\t"
      "v_and_b32 v131, 0xffff0000, v143\n\t"
      "v_mov_b64 v[132:133], v[128:129]\n\t"
      "v_mov_b64 v[134:135], v[130:131]\n\t"
      "v_mov_b64 v[136:137], v[128:129]\n\t"
      "v_mov_b64 v[138:139], v[130:131]\n\t"
      "v_mov_b64 v[140:141], v[128:129]\n\t"
      "v_mov_b64 v[142:143], v[130:131]\n\t"
      "v_lshlrev_b32 v144, 16, v158\n\t"
      "v_and_b32 v145, 0xffff0000, v158\n\t"
      "v_lshlrev_b32 v146, 16, v159\n\t"
      "v_and_b32 v147, 0xffff0000, v159\n\t"
      "v_mov_b64 v[148:149], v[144:145]\n\t"
      "v_mov_b64 v[150:151], v[146:147]\n\t"
      "v_mov_b64 v[152:153], v[144:145]\n\t"
      "v_mov_b64 v[154:155], v[146:147]\n\t"
      "v_mov_b64 v[156:157], v[144:145]\n\t"
      "v_mov_b64 v[158:159], v[146:147]\n\t"
      "v_lshlrev_b32 v160, 16, v174\n\t"
      "v_and_b32 v161, 0xffff0000, v174\n\t"
      "v_lshlrev_b32 v162, 16, v175\n\t"
      "v_and_b32 v163, 0xffff0000, v175\n\t"
      "v_mov_b64 v[164:165], v[160:161]\n\t"
      "v_mov_b64 v[166:167], v[162:163]\n\t"
      "v_mov_b64 v[168:169], v[160:161]\n\t"
      "v_mov_b64 v[170:171], v[162:163]\n\t"
      "v_mov_b64 v[172:173], v[160:161]\n\t"
      "v_mov_b64 v[174:175], v[162:163]\n\t"
      "v_lshlrev_b32 v176, 16, v190\n\t"
      "v_and_b32 v177, 0xffff0000, v190\n\t"
      "v_lshlrev_b32 v178, 16, v191\n\t"
      "v_and_b32 v179, 0xffff0000, v191\n\t"
      "v_mov_b64 v[180:181], v[176:177]\n\t"
      "v_mov_b64 v[182:183], v[178:179]\n\t"
      "v_mov_b64 v[184:185], v[176:177]\n\t"
      "v_mov_b64 v[186:187], v[178:179]\n\t"
      "v_mov_b64 v[188:189], v[176:177]\n\t"
      "v_mov_b64 v[190:191], v[178:179]\n\t"
      "v_lshlrev_b32 v192, 16, v206\n\t"
      "v_and_b32 v193, 0xffff0000, v206\n\t"
      "v_lshlrev_b32 v194, 16, v207\n\t"
      "v_and_b32 v195, 0xffff0000, v207\n\t"
      "v_mov_b64 v[196:197], v[192:193]\n\t"
      "v_mov_b64 v[198:199], v[194:195]\n\t"
      "v_mov_b64 v[200:201], v[192:193]\n\t"
      "v_mov_b64 v[202:203], v[194:195]\n\t"
      "v_mov_b64 v[204:205], v[192:193]\n\t"
      "v_mov_b64 v[206:207], v[194:195]\n\t"
      "v_lshlrev_b32 v208, 16, v222\n\t"
      "v_and_b32 v209, 0xffff0000, v222\n\t"
      "v_lshlrev_b32 v210, 16, v223\n\t"
      "v_and_b32 v211, 0xffff0000, v223\n\t"
      "v_mov_b64 v[212:213], v[208:209]\n\t"
      "v_mov_b64 v[214:215], v[210:211]\n\t"
      "v_mov_b64 v[216:217], v[208:209]\n\t"
      "v_mov_b64 v[218:219], v[210:211]\n\t"
      "v_mov_b64 v[220:221], v[208:209]\n\t"
      "s_waitcnt vmcnt(0)\n\t"
      "s_barrier\n\t"
      "s_cmp_lg_u32 s32, 0\n\t"
      "s_cbranch_scc1 Lf21_%=\n\t"
      "v_mov_b32 v248, s8\n\t"
      "v_mov_b32 v249, s9\n\t"
      "s_mov_b64 exec, 1\n\t"
      "buffer_store_dwordx2 v[248:249], off, s[96:99], s59 sc1\n\t"
      "s_mov_b64 exec, -1\n\t"
      "Lf21_%=:\n\t"
      STAMP(2, 3)
      "s_mov_b32 m0, s53\n\t"
      "s_nop 0\n\t"
      "buffer_load_dwordx4 v2, s[40:43], s44 offen lds\n\t"
      "buffer_load_dwordx4 v2, s[40:43], s45 offen offset:1024 lds\n\t"
      "buffer_load_dwordx4 v2, s[40:43], s46 offen offset:2048 lds\n\t"
      "buffer_load_dwordx4 v2, s[40:43], s47 offen offset:3072 lds\n\t"
      "s_mov_b32 m0, s57\n\t"
      "s_nop 0\n\t"
      "buffer_load_dwordx4 v2, s[40:43], s48 offen lds\n\t"
      "buffer_load_dwordx4 v2, s[40:43], s49 offen offset:1024 lds\n\t"
      "buffer_load_dwordx4 v3, s[40:43], s50 offen offset:2048 lds\n\t"
      "s_add_u32 s53, s53, 28672\n\t"
      "v_add_u32 v2, s52, v2\n\t"
      "v_add_u32 v3, s52, v3\n\t"
      "s_cmp_eq_u32 s53, s55\n\t"
      "s_cselect_b32 s53, s54, s53\n\t"
      "s_add_u32 s57, s53, 4096\n\t"
      "s_mov_b32 m0, s53\n\t"
      "s_nop 0\n\t"
      "buffer_load_dwordx4 v2, s[40:43], s44 offen lds\n\t"
      "buffer_load_dwordx4 v2, s[40:43], s45 offen offset:1024 lds\n\t"
      "buffer_load_dwordx4 v2, s[40:43], s46 offen offset:2048 lds\n\t"
      "buffer_load_dwordx4 v2, s[40:43], s47 offen offset:3072 lds\n\t"
      LSTW(2, "")
      "s_mov_b32 m0, s57\n\t"
      "s_nop 0\n\t"
      "buffer_load_dwordx4 v2, s[40:43], s48 offen lds\n\t"
      "buffer_load_dwordx4 v2, s[40:43], s49 offen offset:1024 lds\n\t"
      "buffer_load_dwordx4 v3, s[40:43], s50 offen offset:2048 lds\n\t"
      "s_add_u32 s53, s53, 28672\n\t"
      "v_add_u32 v2, s52, v2\n\t"
      "v_add_u32 v3, s52, v3\n\t"
      "s_cmp_eq_u32 s53, s55\n\t"
      "s_cselect_b32 s53, s54, s53\n\t"
      "s_add_u32 s57, s53, 4096\n\t"
      "s_cmp_lg_u32 s32, 0\n\t"
      "s_cbranch_scc1 Lw22_%=\n\t"
      "s_mov_b32 s10, 200000\n\t"
      "Lp23_%=:\n\t"
      "buffer_load_dwordx2 v[248:249], v26, s[96:99], 0 offen offset:0 sc1\n\t"
      "s_waitcnt vmcnt(0)\n\t"
      "v_cmp_eq_u64 vcc, s[8:9], v[248:249]\n\t"
      "s_nop 0\n\t"
      "s_cmp_eq_u64 vcc, -1\n\t"
      "s_cbranch_scc1 Lpd23_%=\n\t"
      "s_sleep 2\n\t"
      "s_sub_u32 s10, s10, 1\n\t"
      "s_cmp_lg_u32 s10, 0\n\t"
      "s_cbranch_scc1 Lp23_%=\n\t"
      "Lpd23_%=:\n\t"
      "Lw22_%=:\n\t"
      "s_waitcnt vmcnt(7)\n\t"
      "s_barrier\n\t"
      STAMP(4, 5)
      "buffer_load_dwordx4 a[32:35], v0, s[36:39], s34 offen sc1\n\t"
      "buffer_load_dword v12, off, s[12:15], 0\n\t"
      "buffer_load_dwordx4 a[36:39], v0, s[36:39], s34 offen offset:1024 sc1\n\t"
      "buffer_load_dword v12, off, s[12:15], 0\n\t"
      "buffer_load_dwordx4 a[40:43], v0, s[36:39], s35 offen sc1\n\t"
      "buffer_load_dwordx4 a[44:47], v0, s[36:39], s35 offen offset:1024 sc1\n\t"
      "v_add_u32 v0, s51, v0\n\t"
      "buffer_load_dwordx4 a[48:51], v0, s[36:39], s34 offen sc1\n\t"
      "s_mov_b32 m0, s53\n\t"
      "s_nop 0\n\t"
      "buffer_load_dwordx4 v2, s[40:43], s44 offen lds\n\t"
      "buffer_load_dwordx4 v2, s[40:43], s45 offen offset:1024 lds\n\t"
      "buffer_load_dwordx4 a[52:55], v0, s[36:39], s34 offen offset:1024 sc1\n\t"
      "buffer_load_dwordx4 v2, s[40:43], s46 offen offset:2048 lds\n\t"
      "buffer_load_dwordx4 a[56:59], v0, s[36:39], s35 offen sc1\n\t"
      "buffer_load_dwordx4 v2, s[40:43], s47 offen offset:3072 lds\n\t"
      "buffer_load_dwordx4 a[60:63], v0, s[36:39], s35 offen offset:1024 sc1\n\t"
      "v_add_u32 v0, s51, v0\n\t"
      "s_mov_b32 m0, s57\n\t"
      "s_nop 0\n\t"
      "buffer_load_dwordx4 v2, s[40:43], s48 offen lds\n\t"
      "buffer_load_dwordx4 a[64:67], v0, s[36:39], s34 offen sc1\n\t"
      "buffer_load_dwordx4 v2, s[40:43], s49 offen offset:1024 lds\n\t"
      LSTW(4, "")
      "buffer_load_dwordx4 a[68:71], v0, s[36:39], s34 offen offset:1024 sc1\n\t"
      "buffer_load_dwordx4 v3, s[40:43], s50 offen offset:2048 lds\n\t"
      "s_add_u32 s53, s53, 28672\n\t"
      "v_add_u32 v2, s52, v2\n\t"
      "v_add_u32 v3, s52, v3\n\t"
      "s_cmp_eq_u32 s53, s55\n\t"
      "s_cselect_b32 s53, s54, s53\n\t"
      "s_add_u32 s57, s53, 4096\n\t"
      "buffer_load_dwordx4 a[72:75], v0, s[36:39], s35 offen sc1\n\t"
      "buffer_load_dwordx4 a[76:79], v0, s[36:39], s35 offen offset:1024 sc1\n\t"
      "v_add_u32 v0, s51, v0\n\t"
      "ds_read_b64_tr_b16 a[0:1], v4 offset:0\n\t"
      "ds_read_b64_tr_b16 a[2:3], v4 offset:7168\n\t"
      "ds_read_b64_tr_b16 a[4:5], v5 offset:0\n\t"
      "ds_read_b64_tr_b16 a[6:7], v5 offset:7168\n\t"
      "ds_read_b64_tr_b16 a[8:9], v6 offset:0\n\t"
      "ds_read_b64_tr_b16 a[10:11], v6 offset:7168\n\t"
      "ds_read_b64_tr_b16 a[12:13], v7 offset:0\n\t"
      "ds_read_b64_tr_b16 a[14:15], v7 offset:7168\n\t"
      "v_mov_b64 v[222:223], v[210:211]\n\t"
      "v_lshlrev_b32 v224, 16, v238\n\t"
      "v_and_b32 v225, 0xffff0000, v238\n\t"
      "v_lshlrev_b32 v226, 16, v239\n\t"
      "v_and_b32 v227, 0xffff0000, v239\n\t"
      "v_mov_b64 v[228:229], v[224:225]\n\t"
      "v_mov_b64 v[230:231], v[226:227]\n\t"
      "v_mov_b64 v[232:233], v[224:225]\n\t"
      "v_mov_b64 v[234:235], v[226:227]\n\t"
      "v_mov_b64 v[236:237], v[224:225]\n\t"
      "v_mov_b64 v[238:239], v[226:227]\n\t"
      "v_lshlrev_b32 v240, 16, v254\n\t"
      "v_and_b32 v241, 0xffff0000, v254\n\t"
      "v_lshlrev_b32 v242, 16, v255\n\t"
      "v_and_b32 v243, 0xffff0000, v255\n\t"
      "v_mov_b64 v[244:245], v[240:241]\n\t"
      "v_mov_b64 v[246:247], v[242:243]\n\t"
      "v_mov_b64 v[248:249], v[240:241]\n\t"
      "v_mov_b64 v[250:251], v[242:243]\n\t"
      "v_mov_b64 v[252:253], v[240:241]\n\t"
      "v_mov_b64 v[254:255], v[242:243]\n\t"
      STAMP(20, 21)
      "s_waitcnt vmcnt(15)\n\t"
      "s_waitcnt lgkmcnt(6)\n\t"
      LSTW(20, "s_waitcnt lgkmcnt(0)\n\t")
      "Lg1f_%=:\n\t"
      "v_mfma_f32_16x16x32_bf16 v[32:35], a[0:3], a[32:35], v[32:35]\n\t"
      "ds_read_b64_tr_b16 a[16:17], v4 offset:2048\n\t"
      "v_mfma_f32_16x16x32_bf16 v[36:39], a[0:3], a[36:39], v[36:39]\n\t"
      "ds_read_b64_tr_b16 a[18:19], v4 offset:9216\n\t"
      "v_mfma_f32_16x16x32_bf16 v[40:43], a[0:3], a[40:43], v[40:43]\n\t"
      "buffer_load_dwordx4 a[80:83], v0, s[36:39], s34 offen sc1\n\t"
      "v_mfma_f32_16x16x32_bf16 v[44:47], a[0:3], a[44:47], v[44:47]\n\t"
      "s_waitcnt lgkmcnt(6)\n\t"
      "s_mov_b32 m0, s53\n\t"
      "v_mfma_f32_16x16x32_bf16 v[60:63], a[4:7], a[44:47], v[60:63]\n\t"
      "ds_read_b64_tr_b16 a[20:21], v5 offset:2048\n\t"
      "buffer_load_dwordx4 v2, s[40:43], s44 offen lds\n\t"
      "v_mfma_f32_16x16x32_bf16 v[56:59], a[4:7], a[40:43], v[56:59]\n\t"
      "ds_read_b64_tr_b16 a[22:23], v5 offset:9216\n\t"
      "v_mfma_f32_16x16x32_bf16 v[52:55], a[4:7], a[36:39], v[52:55]\n\t"
      "v_mfma_f32_16x16x32_bf16 v[48:51], a[4:7], a[32:35], v[48:51]\n\t"
      "s_waitcnt lgkmcnt(6)\n\t"
      "v_mfma_f32_16x16x32_bf16 v[64:67], a[8:11], a[32:35], v[64:67]\n\t"
      "ds_read_b64_tr_b16 a[24:25], v6 offset:2048\n\t"
      "v_mfma_f32_16x16x32_bf16 v[68:71], a[8:11], a[36:39], v[68:71]\n\t"
      "ds_read_b64_tr_b16 a[26:27], v6 offset:9216\n\t"
      "v_mfma_f32_16x16x32_bf16 v[72:75], a[8:11], a[40:43], v[72:75]\n\t"
      "v_mfma_f32_16x16x32_bf16 v[76:79], a[8:11], a[44:47], v[76:79]\n\t"
      "s_waitcnt lgkmcnt(6)\n\t"
      "v_mfma_f32_16x16x32_bf16 v[92:95], a[12:15], a[44:47], v[92:95]\n\t"
      "ds_read_b64_tr_b16 a[28:29], v7 offset:2048\n\t"
      "v_mfma_f32_16x16x32_bf16 v[88:91], a[12:15], a[40:43], v[88:91]\n\t"
      "ds_read_b64_tr_b16 a[30:31], v7 offset:9216\n\t"
      "v_mfma_f32_16x16x32_bf16 v[84:87], a[12:15], a[36:39], v[84:87]\n\t"
      "v_mfma_f32_16x16x32_bf16 v[80:83], a[12:15], a[32:35], v[80:83]\n\t"
      "s_waitcnt lgkmcnt(6)\n\t"
      "buffer_load_dwordx4 v2, s[40:43], s45 offen offset:1024 lds\n\t"
      "v_mfma_f32_16x16x32_bf16 v[96:99], a[16:19], a[32:35], v[96:99]\n\t"
      "ds_read_b64_tr_b16 a[0:1], v4 offset:4096\n\t"
      "v_mfma_f32_16x16x32_bf16 v[100:103], a[16:19], a[36:39], v[100:103]\n\t"
      "ds_read_b64_tr_b16 a[2:3], v4 offset:11264\n\t"
      "buffer_load_dwordx4 a[84:87], v0, s[36:39], s34 offen offset:1024 sc1\n\t"
      "v_mfma_f32_16x16x32_bf16 v[104:107], a[16:19], a[40:43], v[104:107]\n\t"
      "v_mfma_f32_16x16x32_bf16 v[108:111], a[16:19], a[44:47], v[108:111]\n\t"
      "s_waitcnt lgkmcnt(6)\n\t"
      "v_mfma_f32_16x16x32_bf16 v[124:127], a[20:23], a[44:47], v[124:127]\n\t"
      "ds_read_b64_tr_b16 a[4:5], v5 offset:4096\n\t"
      "v_mfma_f32_16x16x32_bf16 v[120:123], a[20:23], a[40:43], v[120:123]\n\t"
      "ds_read_b64_tr_b16 a[6:7], v5 offset:11264\n\t"
      "v_mfma_f32_16x16x32_bf16 v[116:119], a[20:23], a[36:39], v[116:119]\n\t"
      "v_mfma_f32_16x16x32_bf16 v[112:115], a[20:23], a[32:35], v[112:115]\n\t"
      "s_waitcnt lgkmcnt(6)\n\t"
      "v_mfma_f32_16x16x32_bf16 v[128:131], a[24:27], a[32:35], v[128:131]\n\t"
      "ds_read_b64_tr_b16 a[8:9], v6 offset:4096\n\t"
      "v_mfma_f32_16x16x32_bf16 v[132:135], a[24:27], a[36:39], v[132:135]\n\t"
      "ds_read_b64_tr_b16 a[10:11], v6 offset:11264\n\t"
      "v_mfma_f32_16x16x32_bf16 v[136:139], a[24:27], a[40:43], v[136:139]\n\t"
      "buffer_load_dwordx4 v2, s[40:43], s46 offen offset:2048 lds\n\t"
      "v_mfma_f32_16x16x32_bf16 v[140:143], a[24:27], a[44:47], v[140:143]\n\t"
      "s_waitcnt lgkmcnt(6)\n\t"
      "v_mfma_f32_16x16x32_bf16 v[156:159], a[28:31], a[44:47], v[156:159]\n\t"
      "ds_read_b64_tr_b16 a[12:13], v7 offset:4096\n\t"
      "v_mfma_f32_16x16x32_bf16 v[152:155], a[28:31], a[40:43], v[152:155]\n\t"
      "ds_read_b64_tr_b16 a[14:15], v7 offset:11264\n\t"
      "v_mfma_f32_16x16x32_bf16 v[148:151], a[28:31], a[36:39], v[148:151]\n\t"
      "buffer_load_dwordx4 a[88:91], v0, s[36:39], s35 offen sc1\n\t"
      "v_mfma_f32_16x16x32_bf16 v[144:147], a[28:31], a[32:35], v[144:147]\n\t"
      "s_waitcnt lgkmcnt(6)\n\t"
      "v_mfma_f32_16x16x32_bf16 v[160:163], a[0:3], a[32:35], v[160:163]\n\t"
      "ds_read_b64_tr_b16 a[16:17], v8 offset:6144\n\t"
      "v_mfma_f32_16x16x32_bf16 v[164:167], a[0:3], a[36:39], v[164:167]\n\t"
      "ds_read_b64_tr_b16 a[18:19], v8 offset:13312\n\t"
      "v_mfma_f32_16x16x32_bf16 v[168:171], a[0:3], a[40:43], v[168:171]\n\t"
      "v_mfma_f32_16x16x32_bf16 v[172:175], a[0:3], a[44:47], v[172:175]\n\t"
      "s_waitcnt lgkmcnt(6)\n\t"
      "v_mfma_f32_16x16x32_bf16 v[188:191], a[4:7], a[44:47], v[188:191]\n\t"
      "ds_read_b64_tr_b16 a[20:21], v9 offset:6144\n\t"
      "v_mfma_f32_16x16x32_bf16 v[184:187], a[4:7], a[40:43], v[184:187]\n\t"
      "ds_read_b64_tr_b16 a[22:23], v9 offset:13312\n\t"
      "v_mfma_f32_16x16x32_bf16 v[180:183], a[4:7], a[36:39], v[180:183]\n\t"
      "buffer_load_dwordx4 v2, s[40:43], s47 offen offset:3072 lds\n\t"
      "v_mfma_f32_16x16x32_bf16 v[176:179], a[4:7], a[32:35], v[176:179]\n\t"
      "s_waitcnt lgkmcnt(6)\n\t"
      "v_mfma_f32_16x16x32_bf16 v[192:195], a[8:11], a[32:35], v[192:195]\n\t"
      "ds_read_b64_tr_b16 a[24:25], v4 offset:14336\n\t"
      "v_mfma_f32_16x16x32_bf16 v[196:199], a[8:11], a[36:39], v[196:199]\n\t"
      "ds_read_b64_tr_b16 a[26:27], v4 offset:21504\n\t"
      "v_mfma_f32_16x16x32_bf16 v[200:203], a[8:11], a[40:43], v[200:203]\n\t"
      "v_mfma_f32_16x16x32_bf16 v[204:207], a[8:11], a[44:47], v[204:207]\n\t"
      "s_waitcnt lgkmcnt(6)\n\t"
      "v_mfma_f32_16x16x32_bf16 v[220:223], a[12:15], a[44:47], v[220:223]\n\t"
      "ds_read_b64_tr_b16 a[28:29], v5 offset:14336\n\t"
      "buffer_load_dwordx4 a[92:95], v0, s[36:39], s35 offen offset:1024 sc1\n\t"
      "v_mfma_f32_16x16x32_bf16 v[216:219], a[12:15], a[40:43], v[216:219]\n\t"
      "ds_read_b64_tr_b16 a[30:31], v5 offset:21504\n\t"
      "v_add_u32 v0, s51, v0\n\t"
      "v_mfma_f32_16x16x32_bf16 v[212:215], a[12:15], a[36:39], v[212:215]\n\t"
      "v_mfma_f32_16x16x32_bf16 v[208:211], a[12:15], a[32:35], v[208:211]\n\t"
      "s_waitcnt lgkmcnt(6)\n\t"
      "v_mfma_f32_16x16x32_bf16 v[224:227], a[16:19], a[32:35], v[224:227]\n\t"
      "s_mov_b32 m0, s57\n\t"
      "ds_read_b64_tr_b16 a[0:1], v6 offset:14336\n\t"
      "v_mfma_f32_16x16x32_bf16 v[228:231], a[16:19], a[36:39], v[228:231]\n\t"
      "ds_read_b64_tr_b16 a[2:3], v6 offset:21504\n\t"
      "buffer_load_dwordx4 v2, s[40:43], s48 offen lds\n\t"
      "v_mfma_f32_16x16x32_bf16 v[232:235], a[16:19], a[40:43], v[232:235]\n\t"
      "v_mfma_f32_16x16x32_bf16 v[236:239], a[16:19], a[44:47], v[236:239]\n\t"
      "s_waitcnt lgkmcnt(6)\n\t"
      "v_mfma_f32_16x16x32_bf16 v[252:255], a[20:23], a[44:47], v[252:255]\n\t"
      "ds_read_b64_tr_b16 a[4:5], v7 offset:14336\n\t"
      "v_mfma_f32_16x16x32_bf16 v[248:251], a[20:23], a[40:43], v[248:251]\n\t"
      "ds_read_b64_tr_b16 a[6:7], v7 offset:21504\n\t"
      "v_mfma_f32_16x16x32_bf16 v[244:247], a[20:23], a[36:39], v[244:247]\n\t"
      "s_waitcnt vmcnt(16)\n\t"
      "v_mfma_f32_16x16x32_bf16 v[240:243], a[20:23], a[32:35], v[240:243]\n\t"
      "s_waitcnt lgkmcnt(6)\n\t"
      "v_mfma_f32_16x16x32_bf16 v[32:35], a[24:27], a[48:51], v[32:35]\n\t"
      "ds_read_b64_tr_b16 a[8:9], v4 offset:16384\n\t"
      "v_mfma_f32_16x16x32_bf16 v[36:39], a[24:27], a[52:55], v[36:39]\n\t"
      "ds_read_b64_tr_b16 a[10:11], v4 offset:23552\n\t"
      "v_mfma_f32_16x16x32_bf16 v[40:43], a[24:27], a[56:59], v[40:43]\n\t"
      "buffer_load_dwordx4 a[32:35], v0, s[36:39], s34 offen sc1\n\t"
      "v_mfma_f32_16x16x32_bf16 v[44:47], a[24:27], a[60:63], v[44:47]\n\t"
      "s_waitcnt lgkmcnt(6)\n\t"
      "v_mfma_f32_16x16x32_bf16 v[60:63], a[28:31], a[60:63], v[60:63]\n\t"
      "ds_read_b64_tr_b16 a[12:13], v5 offset:16384\n\t"
      "v_mfma_f32_16x16x32_bf16 v[56:59], a[28:31], a[56:59], v[56:59]\n\t"
      "ds_read_b64_tr_b16 a[14:15], v5 offset:23552\n\t"
      "buffer_load_dwordx4 v2, s[40:43], s49 offen offset:1024 lds\n\t"
      "v_mfma_f32_16x16x32_bf16 v[52:55], a[28:31], a[52:55], v[52:55]\n\t"
      "v_mfma_f32_16x16x32_bf16 v[48:51], a[28:31], a[48:51], v[48:51]\n\t"
      "s_waitcnt lgkmcnt(6)\n\t"
      "v_mfma_f32_16x16x32_bf16 v[64:67], a[0:3], a[48:51], v[64:67]\n\t"
      "ds_read_b64_tr_b16 a[16:17], v6 offset:16384\n\t"
      "v_mfma_f32_16x16x32_bf16 v[68:71], a[0:3], a[52:55], v[68:71]\n\t"
      "ds_read_b64_tr_b16 a[18:19], v6 offset:23552\n\t"
      "v_mfma_f32_16x16x32_bf16 v[72:75], a[0:3], a[56:59], v[72:75]\n\t"
      "s_add_u32 s64, s64, 1\n\t"
      "s_cmp_eq_u32 s64, 5\n\t"
      "s_cselect_b32 s65, s66, s67\n\t"
      "v_mfma_f32_16x16x32_bf16 v[76:79], a[0:3], a[60:63], v[76:79]\n\t"
      "s_waitcnt lgkmcnt(6)\n\t"
      "s_cselect_b32 s64, 0, s64\n\t"
      "v_mfma_f32_16x16x32_bf16 v[92:95], a[4:7], a[60:63], v[92:95]\n\t"
      "ds_read_b64_tr_b16 a[20:21], v7 offset:16384\n\t"
      "v_mfma_f32_16x16x32_bf16 v[88:91], a[4:7], a[56:59], v[88:91]\n\t"
      "ds_read_b64_tr_b16 a[22:23], v7 offset:23552\n\t"
      "v_mfma_f32_16x16x32_bf16 v[84:87], a[4:7], a[52:55], v[84:87]\n\t"
      "v_mfma_f32_16x16x32_bf16 v[80:83], a[4:7], a[48:51], v[80:83]\n\t"
      "s_waitcnt lgkmcnt(6)\n\t"
      "v_mfma_f32_16x16x32_bf16 v[96:99], a[8:11], a[48:51], v[96:99]\n\t"
      "ds_read_b64_tr_b16 a[24:25], v4 offset:18432\n\t"
      "buffer_load_dwordx4 a[36:39], v0, s[36:39], s34 offen offset:1024 sc1\n\t"
      "v_mfma_f32_16x16x32_bf16 v[100:103], a[8:11], a[52:55], v[100:103]\n\t"
      "ds_read_b64_tr_b16 a[26:27], v4 offset:25600\n\t"
      "v_mfma_f32_16x16x32_bf16 v[104:107], a[8:11], a[56:59], v[104:107]\n\t"
      "buffer_load_dwordx4 v3, s[40:43], s50 offen offset:2048 lds\n\t"
      "v_add_u32 v4, s65, v4\n\t"
      "v_mfma_f32_16x16x32_bf16 v[108:111], a[8:11], a[60:63], v[108:111]\n\t"
      "s_waitcnt lgkmcnt(6)\n\t"
      "v_add_u32 v2, s52, v2\n\t"
      "v_add_u32 v3, s52, v3\n\t"
      "v_mfma_f32_16x16x32_bf16 v[124:127], a[12:15], a[60:63], v[124:127]\n\t"
      "s_add_u32 s53, s53, 28672\n\t"
      "s_cmp_eq_u32 s53, s55\n\t"
      "ds_read_b64_tr_b16 a[28:29], v5 offset:18432\n\t"
      "v_mfma_f32_16x16x32_bf16 v[120:123], a[12:15], a[56:59], v[120:123]\n\t"
      "s_cselect_b32 s53, s54, s53\n\t"
      "s_add_u32 s57, s53, 4096\n\t"
      "ds_read_b64_tr_b16 a[30:31], v5 offset:25600\n\t"
      "v_mfma_f32_16x16x32_bf16 v[116:119], a[12:15], a[52:55], v[116:119]\n\t"
      "v_add_u32 v5, s65, v5\n\t"
      "v_mfma_f32_16x16x32_bf16 v[112:115], a[12:15], a[48:51], v[112:115]\n\t"
      "s_waitcnt lgkmcnt(6)\n\t"
      "v_mfma_f32_16x16x32_bf16 v[128:131], a[16:19], a[48:51], v[128:131]\n\t"
      "ds_read_b64_tr_b16 a[0:1], v6 offset:18432\n\t"
      "v_mfma_f32_16x16x32_bf16 v[132:135], a[16:19], a[52:55], v[132:135]\n\t"
      "ds_read_b64_tr_b16 a[2:3], v6 offset:25600\n\t"
      "v_mfma_f32_16x16x32_bf16 v[136:139], a[16:19], a[56:59], v[136:139]\n\t"
      "v_add_u32 v6, s65, v6\n\t"
      "v_mfma_f32_16x16x32_bf16 v[140:143], a[16:19], a[60:63], v[140:143]\n\t"
      "s_waitcnt lgkmcnt(6)\n\t"
      "v_mfma_f32_16x16x32_bf16 v[156:159], a[20:23], a[60:63], v[156:159]\n\t"
      "ds_read_b64_tr_b16 a[4:5], v7 offset:18432\n\t"
      "v_mfma_f32_16x16x32_bf16 v[152:155], a[20:23], a[56:59], v[152:155]\n\t"
      "ds_read_b64_tr_b16 a[6:7], v7 offset:25600\n\t"
      "v_mfma_f32_16x16x32_bf16 v[148:151], a[20:23], a[52:55], v[148:151]\n\t"
      "buffer_load_dwordx4 a[40:43], v0, s[36:39], s35 offen sc1\n\t"
      "v_add_u32 v7, s65, v7\n\t"
      "v_mfma_f32_16x16x32_bf16 v[144:147], a[20:23], a[48:51], v[144:147]\n\t"
      "s_waitcnt lgkmcnt(6)\n\t"
      "v_mfma_f32_16x16x32_bf16 v[160:163], a[24:27], a[48:51], v[160:163]\n\t"
      "ds_read_b64_tr_b16 a[8:9], v8 offset:20480\n\t"
      "v_mfma_f32_16x16x32_bf16 v[164:167], a[24:27], a[52:55], v[164:167]\n\t"
      "ds_read_b64_tr_b16 a[10:11], v8 offset:27648\n\t"
      "v_mfma_f32_16x16x32_bf16 v[168:171], a[24:27], a[56:59], v[168:171]\n\t"
      "v_add_u32 v8, s65, v8\n\t"
      "v_readlane_b32 s12, v11, s19\n\t"
      "v_mfma_f32_16x16x32_bf16 v[172:175], a[24:27], a[60:63], v[172:175]\n\t"
      "s_waitcnt lgkmcnt(6)\n\t"
      "v_readlane_b32 s13, v13, s19\n\t"
      "v_readlane_b32 s14, v14, s19\n\t"
      "v_mfma_f32_16x16x32_bf16 v[188:191], a[28:31], a[60:63], v[188:191]\n\t"
      "s_add_u32 s19, s19, 1\n\t"
      "ds_read_b64_tr_b16 a[12:13], v9 offset:20480\n\t"
      "v_mfma_f32_16x16x32_bf16 v[184:187], a[28:31], a[56:59], v[184:187]\n\t"
      "ds_read_b64_tr_b16 a[14:15], v9 offset:27648\n\t"
      "v_mfma_f32_16x16x32_bf16 v[180:183], a[28:31], a[52:55], v[180:183]\n\t"
      "v_add_u32 v9, s65, v9\n\t"
      "v_mfma_f32_16x16x32_bf16 v[176:179], a[28:31], a[48:51], v[176:179]\n\t"
      "s_waitcnt lgkmcnt(6)\n\t"
      "v_mfma_f32_16x16x32_bf16 v[192:195], a[0:3], a[48:51], v[192:195]\n\t"
      "s_waitcnt vmcnt(33)\n\t"
      "s_barrier\n\t"
      "ds_read_b64_tr_b16 a[16:17], v4 offset:0\n\t"
      "v_mfma_f32_16x16x32_bf16 v[196:199], a[0:3], a[52:55], v[196:199]\n\t"
      "ds_read_b64_tr_b16 a[18:19], v4 offset:7168\n\t"
      "v_mfma_f32_16x16x32_bf16 v[200:203], a[0:3], a[56:59], v[200:203]\n\t"
      "buffer_load_dword v12, v28, s[12:15], 0 offen\n\t"
      "v_mfma_f32_16x16x32_bf16 v[204:207], a[0:3], a[60:63], v[204:207]\n\t"
      "s_waitcnt lgkmcnt(6)\n\t"
      "v_mfma_f32_16x16x32_bf16 v[220:223], a[4:7], a[60:63], v[220:223]\n\t"
      "ds_read_b64_tr_b16 a[20:21], v5 offset:0\n\t"
      "buffer_load_dwordx4 a[44:47], v0, s[36:39], s35 offen offset:1024 sc1\n\t"
      "v_mfma_f32_16x16x32_bf16 v[216:219], a[4:7], a[56:59], v[216:219]\n\t"
      "ds_read_b64_tr_b16 a[22:23], v5 offset:7168\n\t"
      "v_add_u32 v0, s51, v0\n\t"
      "v_mfma_f32_16x16x32_bf16 v[212:215], a[4:7], a[52:55], v[212:215]\n\t"
      "v_mfma_f32_16x16x32_bf16 v[208:211], a[4:7], a[48:51], v[208:211]\n\t"
      "s_waitcnt lgkmcnt(6)\n\t"
      "v_mfma_f32_16x16x32_bf16 v[224:227], a[8:11], a[48:51], v[224:227]\n\t"
      "ds_read_b64_tr_b16 a[24:25], v6 offset:0\n\t"
      "v_mfma_f32_16x16x32_bf16 v[228:231], a[8:11], a[52:55], v[228:231]\n\t"
      "ds_read_b64_tr_b16 a[26:27], v6 offset:7168\n\t"
      "v_mfma_f32_16x16x32_bf16 v[232:235], a[8:11], a[56:59], v[232:235]\n\t"
      "v_mfma_f32_16x16x32_bf16 v[236:239], a[8:11], a[60:63], v[236:239]\n\t"
      "s_waitcnt lgkmcnt(6)\n\t"
      "v_mfma_f32_16x16x32_bf16 v[252:255], a[12:15], a[60:63], v[252:255]\n\t"
      "ds_read_b64_tr_b16 a[28:29], v7 offset:0\n\t"
      "v_mfma_f32_16x16x32_bf16 v[248:251], a[12:15], a[56:59], v[248:251]\n\t"
      "ds_read_b64_tr_b16 a[30:31], v7 offset:7168\n\t"
      "v_mfma_f32_16x16x32_bf16 v[244:247], a[12:15], a[52:55], v[244:247]\n\t"
      "s_waitcnt vmcnt(16)\n\t"
      "v_mfma_f32_16x16x32_bf16 v[240:243], a[12:15], a[48:51], v[240:243]\n\t"
      "s_waitcnt lgkmcnt(6)\n\t"
      "v_mfma_f32_16x16x32_bf16 v[32:35], a[16:19], a[64:67], v[32:35]\n\t"
      "ds_read_b64_tr_b16 a[0:1], v4 offset:2048\n\t"
      "v_mfma_f32_16x16x32_bf16 v[36:39], a[16:19], a[68:71], v[36:39]\n\t"
      "ds_read_b64_tr_b16 a[2:3], v4 offset:9216\n\t"
      "v_mfma_f32_16x16x32_bf16 v[40:43], a[16:19], a[72:75], v[40:43]\n\t"
      "buffer_load_dwordx4 a[48:51], v0, s[36:39], s34 offen sc1\n\t"
      "v_mfma_f32_16x16x32_bf16 v[44:47], a[16:19], a[76:79], v[44:47]\n\t"
      "s_waitcnt lgkmcnt(6)\n\t"
      "s_mov_b32 m0, s53\n\t"
      "v_mfma_f32_16x16x32_bf16 v[60:63], a[20:23], a[76:79], v[60:63]\n\t"
      "ds_read_b64_tr_b16 a[4:5], v5 offset:2048\n\t"
      "buffer_load_dwordx4 v2, s[40:43], s44 offen lds\n\t"
      "v_mfma_f32_16x16x32_bf16 v[56:59], a[20:23], a[72:75], v[56:59]\n\t"
      "ds_read_b64_tr_b16 a[6:7], v5 offset:9216\n\t"
      "v_mfma_f32_16x16x32_bf16 v[52:55], a[20:23], a[68:71], v[52:55]\n\t"
      "v_mfma_f32_16x16x32_bf16 v[48:51], a[20:23], a[64:67], v[48:51]\n\t"
      "s_waitcnt lgkmcnt(6)\n\t"
      "v_mfma_f32_16x16x32_bf16 v[64:67], a[24:27], a[64:67], v[64:67]\n\t"
      "ds_read_b64_tr_b16 a[8:9], v6 offset:2048\n\t"
      "v_mfma_f32_16x16x32_bf16 v[68:71], a[24:27], a[68:71], v[68:71]\n\t"
      "ds_read_b64_tr_b16 a[10:11], v6 offset:9216\n\t"
      "v_mfma_f32_16x16x32_bf16 v[72:75], a[24:27], a[72:75], v[72:75]\n\t"
      "v_mfma_f32_16x16x32_bf16 v[76:79], a[24:27], a[76:79], v[76:79]\n\t"
      "s_waitcnt lgkmcnt(6)\n\t"
      "v_mfma_f32_16x16x32_bf16 v[92:95], a[28:31], a[76:79], v[92:95]\n\t"
      "ds_read_b64_tr_b16 a[12:13], v7 offset:2048\n\t"
      "v_mfma_f32_16x16x32_bf16 v[88:91], a[28:31], a[72:75], v[88:91]\n\t"
      "ds_read_b64_tr_b16 a[14:15], v7 offset:9216\n\t"
      "v_mfma_f32_16x16x32_bf16 v[84:87], a[28:31], a[68:71], v[84:87]\n\t"
      "v_mfma_f32_16x16x32_bf16 v[80:83], a[28:31], a[64:67], v[80:83]\n\t"
      "s_waitcnt lgkmcnt(6)\n\t"
      "buffer_load_dwordx4 v2, s[40:43], s45 offen offset:1024 lds\n\t"
      "v_mfma_f32_16x16x32_bf16 v[96:99], a[0:3], a[64:67], v[96:99]\n\t"
      "ds_read_b64_tr_b16 a[16:17], v4 offset:4096\n\t"
      "v_mfma_f32_16x16x32_bf16 v[100:103], a[0:3], a[68:71], v[100:103]\n\t"
      "ds_read_b64_tr_b16 a[18:19], v4 offset:11264\n\t"
      "buffer_load_dwordx4 a[52:55], v0, s[36:39], s34 offen offset:1024 sc1\n\t"
      "v_mfma_f32_16x16x32_bf16 v[104:107], a[0:3], a[72:75], v[104:107]\n\t"
      "v_mfma_f32_16x16x32_bf16 v[108:111], a[0:3], a[76:79], v[108:111]\n\t"
      "s_waitcnt lgkmcnt(6)\n\t"
      "v_mfma_f32_16x16x32_bf16 v[124:127], a[4:7], a[76:79], v[124:127]\n\t"
      "ds_read_b64_tr_b16 a[20:21], v5 offset:4096\n\t"
      "v_mfma_f32_16x16x32_bf16 v[120:123], a[4:7], a[72:75], v[120:123]\n\t"
      "ds_read_b64_tr_b16 a[22:23], v5 offset:11264\n\t"
      "v_mfma_f32_16x16x32_bf16 v[116:119], a[4:7], a[68:71], v[116:119]\n\t"
      "v_mfma_f32_16x16x32_bf16 v[112:115], a[4:7], a[64:67], v[112:115]\n\t"
      "s_waitcnt lgkmcnt(6)\n\t"
      "v_mfma_f32_16x16x32_bf16 v[128:131], a[8:11], a[64:67], v[128:131]\n\t"
      "ds_read_b64_tr_b16 a[24:25], v6 offset:4096\n\t"
      "v_mfma_f32_16x16x32_bf16 v[132:135], a[8:11], a[68:71], v[132:135]\n\t"
      "ds_read_b64_tr_b16 a[26:27], v6 offset:11264\n\t"
      "v_mfma_f32_16x16x32_bf16 v[136:139], a[8:11], a[72:75], v[136:139]\n\t"
      "buffer_load_dwordx4 v2, s[40:43], s46 offen offset:2048 lds\n\t"
      "v_mfma_f32_16x16x32_bf16 v[140:143], a[8:11], a[76:79], v[140:143]\n\t"
      "s_waitcnt lgkmcnt(6)\n\t"
      "v_mfma_f32_16x16x32_bf16 v[156:159], a[12:15], a[76:79], v[156:159]\n\t"
      "ds_read_b64_tr_b16 a[28:29], v7 offset:4096\n\t"
      "v_mfma_f32_16x16x32_bf16 v[152:155], a[12:15], a[72:75], v[152:155]\n\t"
      "ds_read_b64_tr_b16 a[30:31], v7 offset:11264\n\t"
      "v_mfma_f32_16x16x32_bf16 v[148:151], a[12:15], a[68:71], v[148:151]\n\t"
      "buffer_load_dwordx4 a[56:59], v0, s[36:39], s35 offen sc1\n\t"
      "v_mfma_f32_16x16x32_bf16 v[144:147], a[12:15], a[64:67], v[144:147]\n\t"
      "s_waitcnt lgkmcnt(6)\n\t"
      "v_mfma_f32_16x16x32_bf16 v[160:163], a[16:19], a[64:67], v[160:163]\n\t"
      "ds_read_b64_tr_b16 a[0:1], v8 offset:6144\n\t"
      "v_mfma_f32_16x16x32_bf16 v[164:167], a[16:19], a[68:71], v[164:167]\n\t"
      "ds_read_b64_tr_b16 a[2:3], v8 offset:13312\n\t"
      "v_mfma_f32_16x16x32_bf16 v[168:171], a[16:19], a[72:75], v[168:171]\n\t"
      "v_mfma_f32_16x16x32_bf16 v[172:175], a[16:19], a[76:79], v[172:175]\n\t"
      "s_waitcnt lgkmcnt(6)\n\t"
      "v_mfma_f32_16x16x32_bf16 v[188:191], a[20:23], a[76:79], v[188:191]\n\t"
      "ds_read_b64_tr_b16 a[4:5], v9 offset:6144\n\t"
      "v_mfma_f32_16x16x32_bf16 v[184:187], a[20:23], a[72:75], v[184:187]\n\t"
      "ds_read_b64_tr_b16 a[6:7], v9 offset:13312\n\t"
      "v_mfma_f32_16x16x32_bf16 v[180:183], a[20:23], a[68:71], v[180:183]\n\t"
      "buffer_load_dwordx4 v2, s[40:43], s47 offen offset:3072 lds\n\t"
      "v_mfma_f32_16x16x32_bf16 v[176:179], a[20:23], a[64:67], v[176:179]\n\t"
      "s_waitcnt lgkmcnt(6)\n\t"
      "v_mfma_f32_16x16x32_bf16 v[192:195], a[24:27], a[64:67], v[192:195]\n\t"
      "ds_read_b64_tr_b16 a[8:9], v4 offset:14336\n\t"
      "v_mfma_f32_16x16x32_bf16 v[196:199], a[24:27], a[68:71], v[196:199]\n\t"
      "ds_read_b64_tr_b16 a[10:11], v4 offset:21504\n\t"
      "v_mfma_f32_16x16x32_bf16 v[200:203], a[24:27], a[72:75], v[200:203]\n\t"
      "v_mfma_f32_16x16x32_bf16 v[204:207], a[24:27], a[76:79], v[204:207]\n\t"
      "s_waitcnt lgkmcnt(6)\n\t"
      "v_mfma_f32_16x16x32_bf16 v[220:223], a[28:31], a[76:79], v[220:223]\n\t"
      "ds_read_b64_tr_b16 a[12:13], v5 offset:14336\n\t"
      "buffer_load_dwordx4 a[60:63], v0, s[36:39], s35 offen offset:1024 sc1\n\t"
      "v_mfma_f32_16x16x32_bf16 v[216:219], a[28:31], a[72:75], v[216:219]\n\t"
      "ds_read_b64_tr_b16 a[14:15], v5 offset:21504\n\t"
      "v_add_u32 v0, s51, v0\n\t"
      "v_mfma_f32_16x16x32_bf16 v[212:215], a[28:31], a[68:71], v[212:215]\n\t"
      "v_mfma_f32_16x16x32_bf16 v[208:211], a[28:31], a[64:67], v[208:211]\n\t"
      "s_waitcnt lgkmcnt(6)\n\t"
      "v_mfma_f32_16x16x32_bf16 v[224:227], a[0:3], a[64:67], v[224:227]\n\t"
      "s_mov_b32 m0, s57\n\t"
      "ds_read_b64_tr_b16 a[16:17], v6 offset:14336\n\t"
      "v_mfma_f32_16x16x32_bf16 v[228:231], a[0:3], a[68:71], v[228:231]\n\t"
      "ds_read_b64_tr_b16 a[18:19], v6 offset:21504\n\t"
      "buffer_load_dwordx4 v2, s[40:43], s48 offen lds\n\t"
      "v_mfma_f32_16x16x32_bf16 v[232:235], a[0:3], a[72:75], v[232:235]\n\t"
      "v_mfma_f32_16x16x32_bf16 v[236:239], a[0:3], a[76:79], v[236:239]\n\t"
      "s_waitcnt lgkmcnt(6)\n\t"
      "v_mfma_f32_16x16x32_bf16 v[252:255], a[4:7], a[76:79], v[252:255]\n\t"
      "ds_read_b64_tr_b16 a[20:21], v7 offset:14336\n\t"
      "v_mfma_f32_16x16x32_bf16 v[248:251], a[4:7], a[72:75], v[248:251]\n\t"
      "ds_read_b64_tr_b16 a[22:23], v7 offset:21504\n\t"
      "v_mfma_f32_16x16x32_bf16 v[244:247], a[4:7], a[68:71], v[244:247]\n\t"
      "s_waitcnt vmcnt(17)\n\t"
      "v_mfma_f32_16x16x32_bf16 v[240:243], a[4:7], a[64:67], v[240:243]\n\t"
      "s_waitcnt lgkmcnt(6)\n\t"
      "v_mfma_f32_16x16x32_bf16 v[32:35], a[8:11], a[80:83], v[32:35]\n\t"
      "ds_read_b64_tr_b16 a[24:25], v4 offset:16384\n\t"
      "v_mfma_f32_16x16x32_bf16 v[36:39], a[8:11], a[84:87], v[36:39]\n\t"
      "ds_read_b64_tr_b16 a[26:27], v4 offset:23552\n\t"
      "v_mfma_f32_16x16x32_bf16 v[40:43], a[8:11], a[88:91], v[40:43]\n\t"
      "buffer_load_dwordx4 a[64:67], v0, s[36:39], s34 offen sc1\n\t"
      "v_mfma_f32_16x16x32_bf16 v[44:47], a[8:11], a[92:95], v[44:47]\n\t"
      "s_waitcnt lgkmcnt(6)\n\t"
      "v_mfma_f32_16x16x32_bf16 v[60:63], a[12:15], a[92:95], v[60:63]\n\t"
      "ds_read_b64_tr_b16 a[28:29], v5 offset:16384\n\t"
      "v_mfma_f32_16x16x32_bf16 v[56:59], a[12:15], a[88:91], v[56:59]\n\t"
      "ds_read_b64_tr_b16 a[30:31], v5 offset:23552\n\t"
      "buffer_load_dwordx4 v2, s[40:43], s49 offen offset:1024 lds\n\t"
      "v_mfma_f32_16x16x32_bf16 v[52:55], a[12:15], a[84:87], v[52:55]\n\t"
      "v_mfma_f32_16x16x32_bf16 v[48:51], a[12:15], a[80:83], v[48:51]\n\t"
      "s_waitcnt lgkmcnt(6)\n\t"
      "v_mfma_f32_16x16x32_bf16 v[64:67], a[16:19], a[80:83], v[64:67]\n\t"
      "ds_read_b64_tr_b16 a[0:1], v6 offset:16384\n\t"
      "v_mfma_f32_16x16x32_bf16 v[68:71], a[16:19], a[84:87], v[68:71]\n\t"
      "ds_read_b64_tr_b16 a[2:3], v6 offset:23552\n\t"
      "v_mfma_f32_16x16x32_bf16 v[72:75], a[16:19], a[88:91], v[72:75]\n\t"
      "s_add_u32 s64, s64, 1\n\t"
      "s_cmp_eq_u32 s64, 5\n\t"
      "s_cselect_b32 s65, s66, s67\n\t"
      "v_mfma_f32_16x16x32_bf16 v[76:79], a[16:19], a[92:95], v[76:79]\n\t"
      "s_waitcnt lgkmcnt(6)\n\t"
      "s_cselect_b32 s64, 0, s64\n\t"
      "v_mfma_f32_16x16x32_bf16 v[92:95], a[20:23], a[92:95], v[92:95]\n\t"
      "ds_read_b64_tr_b16 a[4:5], v7 offset:16384\n\t"
      "v_mfma_f32_16x16x32_bf16 v[88:91], a[20:23], a[88:91], v[88:91]\n\t"
      "ds_read_b64_tr_b16 a[6:7], v7 offset:23552\n\t"
      "v_mfma_f32_16x16x32_bf16 v[84:87], a[20:23], a[84:87], v[84:87]\n\t"
      "v_mfma_f32_16x16x32_bf16 v[80:83], a[20:23], a[80:83], v[80:83]\n\t"
      "s_waitcnt lgkmcnt(6)\n\t"
      "v_mfma_f32_16x16x32_bf16 v[96:99], a[24:27], a[80:83], v[96:99]\n\t"
      "ds_read_b64_tr_b16 a[8:9], v4 offset:18432\n\t"
      "buffer_load_dwordx4 a[68:71], v0, s[36:39], s34 offen offset:1024 sc1\n\t"
      "v_mfma_f32_16x16x32_bf16 v[100:103], a[24:27], a[84:87], v[100:103]\n\t"
      "ds_read_b64_tr_b16 a[10:11], v4 offset:25600\n\t"
      "v_mfma_f32_16x16x32_bf16 v[104:107], a[24:27], a[88:91], v[104:107]\n\t"
      "buffer_load_dwordx4 v3, s[40:43], s50 offen offset:2048 lds\n\t"
      "v_add_u32 v4, s65, v4\n\t"
      "v_mfma_f32_16x16x32_bf16 v[108:111], a[24:27], a[92:95], v[108:111]\n\t"
      "s_waitcnt lgkmcnt(6)\n\t"
      "v_add_u32 v2, s52, v2\n\t"
      "v_add_u32 v3, s52, v3\n\t"
      "v_mfma_f32_16x16x32_bf16 v[124:127], a[28:31], a[92:95], v[124:127]\n\t"
      "s_add_u32 s53, s53, 28672\n\t"
      "s_cmp_eq_u32 s53, s55\n\t"
      "ds_read_b64_tr_b16 a[12:13], v5 offset:18432\n\t"
      "v_mfma_f32_16x16x32_bf16 v[120:123], a[28:31], a[88:91], v[120:123]\n\t"
      "s_cselect_b32 s53, s54, s53\n\t"
      "s_add_u32 s57, s53, 4096\n\t"
      "ds_read_b64_tr_b16 a[14:15], v5 offset:25600\n\t"
      "v_mfma_f32_16x16x32_bf16 v[116:119], a[28:31], a[84:87], v[116:119]\n\t"
      "v_add_u32 v5, s65, v5\n\t"
      "v_mfma_f32_16x16x32_bf16 v[112:115], a[28:31], a[80:83], v[112:115]\n\t"
      "s_waitcnt lgkmcnt(6)\n\t"
      "v_mfma_f32_16x16x32_bf16 v[128:131], a[0:3], a[80:83], v[128:131]\n\t"
      "ds_read_b64_tr_b16 a[16:17], v6 offset:18432\n\t"
      "v_mfma_f32_16x16x32_bf16 v[132:135], a[0:3], a[84:87], v[132:135]\n\t"
      "ds_read_b64_tr_b16 a[18:19], v6 offset:25600\n\t"
      "v_mfma_f32_16x16x32_bf16 v[136:139], a[0:3], a[88:91], v[136:139]\n\t"
      "v_add_u32 v6, s65, v6\n\t"
      "v_mfma_f32_16x16x32_bf16 v[140:143], a[0:3], a[92:95], v[140:143]\n\t"
      "s_waitcnt lgkmcnt(6)\n\t"
      "v_mfma_f32_16x16x32_bf16 v[156:159], a[4:7], a[92:95], v[156:159]\n\t"
      "ds_read_b64_tr_b16 a[20:21], v7 offset:18432\n\t"
      "v_mfma_f32_16x16x32_bf16 v[152:155], a[4:7], a[88:91], v[152:155]\n\t"
      "ds_read_b64_tr_b16 a[22:23], v7 offset:25600\n\t"
      "v_mfma_f32_16x16x32_bf16 v[148:151], a[4:7], a[84:87], v[148:151]\n\t"
      "buffer_load_dwordx4 a[72:75], v0, s[36:39], s35 offen sc1\n\t"
      "v_add_u32 v7, s65, v7\n\t"
      "v_mfma_f32_16x16x32_bf16 v[144:147], a[4:7], a[80:83], v[144:147]\n\t"
      "s_waitcnt lgkmcnt(6)\n\t"
      "v_mfma_f32_16x16x32_bf16 v[160:163], a[8:11], a[80:83], v[160:163]\n\t"
      "ds_read_b64_tr_b16 a[24:25], v8 offset:20480\n\t"
      "v_mfma_f32_16x16x32_bf16 v[164:167], a[8:11], a[84:87], v[164:167]\n\t"
      "ds_read_b64_tr_b16 a[26:27], v8 offset:27648\n\t"
      "v_mfma_f32_16x16x32_bf16 v[168:171], a[8:11], a[88:91], v[168:171]\n\t"
      "v_add_u32 v8, s65, v8\n\t"
      "v_readlane_b32 s12, v11, s19\n\t"
      "v_mfma_f32_16x16x32_bf16 v[172:175], a[8:11], a[92:95], v[172:175]\n\t"
      "s_waitcnt lgkmcnt(6)\n\t"
      "v_readlane_b32 s13, v13, s19\n\t"
      "v_readlane_b32 s14, v14, s19\n\t"
      "v_mfma_f32_16x16x32_bf16 v[188:191], a[12:15], a[92:95], v[188:191]\n\t"
      "s_add_u32 s19, s19, 1\n\t"
      "ds_read_b64_tr_b16 a[28:29], v9 offset:20480\n\t"
      "v_mfma_f32_16x16x32_bf16 v[184:187], a[12:15], a[88:91], v[184:187]\n\t"
      "ds_read_b64_tr_b16 a[30:31], v9 offset:27648\n\t"
      "v_mfma_f32_16x16x32_bf16 v[180:183], a[12:15], a[84:87], v[180:183]\n\t"
      "v_add_u32 v9, s65, v9\n\t"
      "v_mfma_f32_16x16x32_bf16 v[176:179], a[12:15], a[80:83], v[176:179]\n\t"
      "s_waitcnt lgkmcnt(6)\n\t"
      "v_mfma_f32_16x16x32_bf16 v[192:195], a[16:19], a[80:83], v[192:195]\n\t"
      "s_waitcnt vmcnt(32)\n\t"
      "s_barrier\n\t"
      "ds_read_b64_tr_b16 a[0:1], v4 offset:0\n\t"
      "v_mfma_f32_16x16x32_bf16 v[196:199], a[16:19], a[84:87], v[196:199]\n\t"
      "ds_read_b64_tr_b16 a[2:3], v4 offset:7168\n\t"
      "v_mfma_f32_16x16x32_bf16 v[200:203], a[16:19], a[88:91], v[200:203]\n\t"
      "buffer_load_dword v12, v28, s[12:15], 0 offen\n\t"
      "v_mfma_f32_16x16x32_bf16 v[204:207], a[16:19], a[92:95], v[204:207]\n\t"
      "s_waitcnt lgkmcnt(6)\n\t"
      "v_mfma_f32_16x16x32_bf16 v[220:223], a[20:23], a[92:95], v[220:223]\n\t"
      "ds_read_b64_tr_b16 a[4:5], v5 offset:0\n\t"
      "buffer_load_dwordx4 a[76:79], v0, s[36:39], s35 offen offset:1024 sc1\n\t"
      "v_mfma_f32_16x16x32_bf16 v[216:219], a[20:23], a[88:91], v[216:219]\n\t"
      "ds_read_b64_tr_b16 a[6:7], v5 offset:7168\n\t"
      "v_add_u32 v0, s51, v0\n\t"
      "v_mfma_f32_16x16x32_bf16 v[212:215], a[20:23], a[84:87], v[212:215]\n\t"
      "v_mfma_f32_16x16x32_bf16 v[208:211], a[20:23], a[80:83], v[208:211]\n\t"
      "s_waitcnt lgkmcnt(6)\n\t"
      "v_mfma_f32_16x16x32_bf16 v[224:227], a[24:27], a[80:83], v[224:227]\n\t"
      "ds_read_b64_tr_b16 a[8:9], v6 offset:0\n\t"
      "v_mfma_f32_16x16x32_bf16 v[228:231], a[24:27], a[84:87], v[228:231]\n\t"
      "ds_read_b64_tr_b16 a[10:11], v6 offset:7168\n\t"
      "v_mfma_f32_16x16x32_bf16 v[232:235], a[24:27], a[88:91], v[232:235]\n\t"
      "v_mfma_f32_16x16x32_bf16 v[236:239], a[24:27], a[92:95], v[236:239]\n\t"
      "s_waitcnt lgkmcnt(6)\n\t"
      "v_mfma_f32_16x16x32_bf16 v[252:255], a[28:31], a[92:95], v[252:255]\n\t"
      "ds_read_b64_tr_b16 a[12:13], v7 offset:0\n\t"
      "v_mfma_f32_16x16x32_bf16 v[248:251], a[28:31], a[88:91], v[248:251]\n\t"
      "s_sub_u32 s68, s68, 1\n\t"
      "ds_read_b64_tr_b16 a[14:15], v7 offset:7168\n\t"
      "v_mfma_f32_16x16x32_bf16 v[244:247], a[28:31], a[84:87], v[244:247]\n\t"
      "s_cmp_lg_u32 s68, 0\n\t"
      "s_waitcnt vmcnt(16)\n\t"
      "v_mfma_f32_16x16x32_bf16 v[240:243], a[28:31], a[80:83], v[240:243]\n\t"
      "s_waitcnt lgkmcnt(6)\n\t"
      "s_cbranch_scc1 Lg1f_%=\n\t"
      "v_mfma_f32_16x16x32_bf16 v[32:35], a[0:3], a[32:35], v[32:35]\n\t"
      "ds_read_b64_tr_b16 a[16:17], v4 offset:2048\n\t"
      "v_mfma_f32_16x16x32_bf16 v[36:39], a[0:3], a[36:39], v[36:39]\n\t"
      "ds_read_b64_tr_b16 a[18:19], v4 offset:9216\n\t"
      "v_mfma_f32_16x16x32_bf16 v[40:43], a[0:3], a[40:43], v[40:43]\n\t"
      "buffer_load_dwordx4 a[80:83], v0, s[36:39], s34 offen sc1\n\t"
      "v_mfma_f32_16x16x32_bf16 v[44:47], a[0:3], a[44:47], v[44:47]\n\t"
      "s_waitcnt lgkmcnt(6)\n\t"
      "s_mov_b32 m0, s53\n\t"
      "v_mfma_f32_16x16x32_bf16 v[60:63], a[4:7], a[44:47], v[60:63]\n\t"
      "ds_read_b64_tr_b16 a[20:21], v5 offset:2048\n\t"
      "buffer_load_dwordx4 v2, s[40:43], s44 offen lds\n\t"
      "v_mfma_f32_16x16x32_bf16 v[56:59], a[4:7], a[40:43], v[56:59]\n\t"
      "ds_read_b64_tr_b16 a[22:23], v5 offset:9216\n\t"
      "v_mfma_f32_16x16x32_bf16 v[52:55], a[4:7], a[36:39], v[52:55]\n\t"
      "v_mfma_f32_16x16x32_bf16 v[48:51], a[4:7], a[32:35], v[48:51]\n\t"
      "s_waitcnt lgkmcnt(6)\n\t"
      "buffer_load_dwordx4 a[84:87], v0, s[36:39], s34 offen offset:1024 sc1\n\t"
      "v_mfma_f32_16x16x32_bf16 v[64:67], a[8:11], a[32:35], v[64:67]\n\t"
      "ds_read_b64_tr_b16 a[24:25], v6 offset:2048\n\t"
      "v_mfma_f32_16x16x32_bf16 v[68:71], a[8:11], a[36:39], v[68:71]\n\t"
      "ds_read_b64_tr_b16 a[26:27], v6 offset:9216\n\t"
      "v_mfma_f32_16x16x32_bf16 v[72:75], a[8:11], a[40:43], v[72:75]\n\t"
      "buffer_load_dwordx4 v2, s[40:43], s45 offen offset:1024 lds\n\t"
      "v_mfma_f32_16x16x32_bf16 v[76:79], a[8:11], a[44:47], v[76:79]\n\t"
      "s_waitcnt lgkmcnt(6)\n\t"
      "v_mfma_f32_16x16x32_bf16 v[92:95], a[12:15], a[44:47], v[92:95]\n\t"
      "ds_read_b64_tr_b16 a[28:29], v7 offset:2048\n\t"
      "buffer_load_dwordx4 a[88:91], v0, s[36:39], s35 offen sc1\n\t"
      "v_mfma_f32_16x16x32_bf16 v[88:91], a[12:15], a[40:43], v[88:91]\n\t"
      "ds_read_b64_tr_b16 a[30:31], v7 offset:9216\n\t"
      "v_mfma_f32_16x16x32_bf16 v[84:87], a[12:15], a[36:39], v[84:87]\n\t"
      "v_mfma_f32_16x16x32_bf16 v[80:83], a[12:15], a[32:35], v[80:83]\n\t"
      "s_waitcnt lgkmcnt(6)\n\t"
      "v_mfma_f32_16x16x32_bf16 v[96:99], a[16:19], a[32:35], v[96:99]\n\t"
      "ds_read_b64_tr_b16 a[0:1], v4 offset:4096\n\t"
      "v_mfma_f32_16x16x32_bf16 v[100:103], a[16:19], a[36:39], v[100:103]\n\t"
      "ds_read_b64_tr_b16 a[2:3], v4 offset:11264\n\t"
      "buffer_load_dwordx4 a[92:95], v0, s[36:39], s35 offen offset:1024 sc1\n\t"
      "v_mfma_f32_16x16x32_bf16 v[104:107], a[16:19], a[40:43], v[104:107]\n\t"
      "v_add_u32 v0, s51, v0\n\t"
      "v_mfma_f32_16x16x32_bf16 v[108:111], a[16:19], a[44:47], v[108:111]\n\t"
      "s_waitcnt lgkmcnt(6)\n\t"
      "buffer_load_dwordx4 v2, s[40:43], s46 offen offset:2048 lds\n\t"
      "v_mfma_f32_16x16x32_bf16 v[124:127], a[20:23], a[44:47], v[124:127]\n\t"
      "ds_read_b64_tr_b16 a[4:5], v5 offset:4096\n\t"
      "v_mfma_f32_16x16x32_bf16 v[120:123], a[20:23], a[40:43], v[120:123]\n\t"
      "ds_read_b64_tr_b16 a[6:7], v5 offset:11264\n\t"
      "v_mfma_f32_16x16x32_bf16 v[116:119], a[20:23], a[36:39], v[116:119]\n\t"
      "buffer_load_dwordx4 a[96:99], v0, s[36:39], s34 offen sc1\n\t"
      "v_mfma_f32_16x16x32_bf16 v[112:115], a[20:23], a[32:35], v[112:115]\n\t"
      "s_waitcnt lgkmcnt(6)\n\t"
      "v_mfma_f32_16x16x32_bf16 v[128:131], a[24:27], a[32:35], v[128:131]\n\t"
      "ds_read_b64_tr_b16 a[8:9], v6 offset:4096\n\t"
      "v_mfma_f32_16x16x32_bf16 v[132:135], a[24:27], a[36:39], v[132:135]\n\t"
      "ds_read_b64_tr_b16 a[10:11], v6 offset:11264\n\t"
      "v_mfma_f32_16x16x32_bf16 v[136:139], a[24:27], a[40:43], v[136:139]\n\t"
      "buffer_load_dwordx4 v2, s[40:43], s47 offen offset:3072 lds\n\t"
      "v_mfma_f32_16x16x32_bf16 v[140:143], a[24:27], a[44:47], v[140:143]\n\t"
      "s_waitcnt lgkmcnt(6)\n\t"
      "v_mfma_f32_16x16x32_bf16 v[156:159], a[28:31], a[44:47], v[156:159]\n\t"
      "ds_read_b64_tr_b16 a[12:13], v7 offset:4096\n\t"
      "buffer_load_dwordx4 a[100:103], v0, s[36:39], s34 offen offset:1024 sc1\n\t"
      "v_mfma_f32_16x16x32_bf16 v[152:155], a[28:31], a[40:43], v[152:155]\n\t"
      "ds_read_b64_tr_b16 a[14:15], v7 offset:11264\n\t"
      "v_mfma_f32_16x16x32_bf16 v[148:151], a[28:31], a[36:39], v[148:151]\n\t"
      "v_mfma_f32_16x16x32_bf16 v[144:147], a[28:31], a[32:35], v[144:147]\n\t"
      "s_waitcnt lgkmcnt(6)\n\t"
      "v_mfma_f32_16x16x32_bf16 v[160:163], a[0:3], a[32:35], v[160:163]\n\t"
      "ds_read_b64_tr_b16 a[16:17], v8 offset:6144\n\t"
      "buffer_load_dwordx4 a[104:107], v0, s[36:39], s35 offen sc1\n\t"
      "v_mfma_f32_16x16x32_bf16 v[164:167], a[0:3], a[36:39], v[164:167]\n\t"
      "ds_read_b64_tr_b16 a[18:19], v8 offset:13312\n\t"
      "v_mfma_f32_16x16x32_bf16 v[168:171], a[0:3], a[40:43], v[168:171]\n\t"
      "s_mov_b32 m0, s57\n\t"
      "v_mfma_f32_16x16x32_bf16 v[172:175], a[0:3], a[44:47], v[172:175]\n\t"
      "s_waitcnt lgkmcnt(6)\n\t"
      "buffer_load_dwordx4 v2, s[40:43], s48 offen lds\n\t"
      "v_mfma_f32_16x16x32_bf16 v[188:191], a[4:7], a[44:47], v[188:191]\n\t"
      "ds_read_b64_tr_b16 a[20:21], v9 offset:6144\n\t"
      "v_mfma_f32_16x16x32_bf16 v[184:187], a[4:7], a[40:43], v[184:187]\n\t"
      "ds_read_b64_tr_b16 a[22:23], v9 offset:13312\n\t"
      "buffer_load_dwordx4 a[108:111], v0, s[36:39], s35 offen offset:1024 sc1\n\t"
      "v_mfma_f32_16x16x32_bf16 v[180:183], a[4:7], a[36:39], v[180:183]\n\t"
      "v_add_u32 v0, s51, v0\n\t"
      "v_mfma_f32_16x16x32_bf16 v[176:179], a[4:7], a[32:35], v[176:179]\n\t"
      "s_waitcnt lgkmcnt(6)\n\t"
      "v_mfma_f32_16x16x32_bf16 v[192:195], a[8:11], a[32:35], v[192:195]\n\t"
      "ds_read_b64_tr_b16 a[24:25], v4 offset:14336\n\t"
      "v_mfma_f32_16x16x32_bf16 v[196:199], a[8:11], a[36:39], v[196:199]\n\t"
      "ds_read_b64_tr_b16 a[26:27], v4 offset:21504\n\t"
      "v_mfma_f32_16x16x32_bf16 v[200:203], a[8:11], a[40:43], v[200:203]\n\t"
      "buffer_load_dwordx4 a[112:115], v0, s[36:39], s34 offen sc1\n\t"
      "v_mfma_f32_16x16x32_bf16 v[204:207], a[8:11], a[44:47], v[204:207]\n\t"
      "s_waitcnt lgkmcnt(6)\n\t"
      "v_mfma_f32_16x16x32_bf16 v[220:223], a[12:15], a[44:47], v[220:223]\n\t"
      "ds_read_b64_tr_b16 a[28:29], v5 offset:14336\n\t"
      "buffer_load_dwordx4 v2, s[40:43], s49 offen offset:1024 lds\n\t"
      "v_mfma_f32_16x16x32_bf16 v[216:219], a[12:15], a[40:43], v[216:219]\n\t"
      "ds_read_b64_tr_b16 a[30:31], v5 offset:21504\n\t"
      "v_mfma_f32_16x16x32_bf16 v[212:215], a[12:15], a[36:39], v[212:215]\n\t"
      "v_mfma_f32_16x16x32_bf16 v[208:211], a[12:15], a[32:35], v[208:211]\n\t"
      "s_waitcnt lgkmcnt(6)\n\t"
      "buffer_load_dwordx4 a[116:119], v0, s[36:39], s34 offen offset:1024 sc1\n\t"
      "v_mfma_f32_16x16x32_bf16 v[224:227], a[16:19], a[32:35], v[224:227]\n\t"
      "ds_read_b64_tr_b16 a[0:1], v6 offset:14336\n\t"
      "v_mfma_f32_16x16x32_bf16 v[228:231], a[16:19], a[36:39], v[228:231]\n\t"
      "ds_read_b64_tr_b16 a[2:3], v6 offset:21504\n\t"
      "v_mfma_f32_16x16x32_bf16 v[232:235], a[16:19], a[40:43], v[232:235]\n\t"
      "v_mfma_f32_16x16x32_bf16 v[236:239], a[16:19], a[44:47], v[236:239]\n\t"
      "s_waitcnt lgkmcnt(6)\n\t"
      "buffer_load_dwordx4 v3, s[40:43], s50 offen offset:2048 lds\n\t"
      "v_mfma_f32_16x16x32_bf16 v[252:255], a[20:23], a[44:47], v[252:255]\n\t"
      "ds_read_b64_tr_b16 a[4:5], v7 offset:14336\n\t"
      "v_mfma_f32_16x16x32_bf16 v[248:251], a[20:23], a[40:43], v[248:251]\n\t"
      "ds_read_b64_tr_b16 a[6:7], v7 offset:21504\n\t"
      "buffer_load_dwordx4 a[120:123], v0, s[36:39], s35 offen sc1\n\t"
      "v_mfma_f32_16x16x32_bf16 v[244:247], a[20:23], a[36:39], v[244:247]\n\t"
      "s_waitcnt vmcnt(26)\n\t"
      "v_mfma_f32_16x16x32_bf16 v[240:243], a[20:23], a[32:35], v[240:243]\n\t"
      "s_waitcnt lgkmcnt(6)\n\t"
      "v_mfma_f32_16x16x32_bf16 v[32:35], a[24:27], a[48:51], v[32:35]\n\t"
      "ds_read_b64_tr_b16 a[8:9], v4 offset:16384\n\t"
      "v_mfma_f32_16x16x32_bf16 v[36:39], a[24:27], a[52:55], v[36:39]\n\t"
      "ds_read_b64_tr_b16 a[10:11], v4 offset:23552\n\t"
      "buffer_load_dwordx4 a[124:127], v0, s[36:39], s35 offen offset:1024 sc1\n\t"
      "v_mfma_f32_16x16x32_bf16 v[40:43], a[24:27], a[56:59], v[40:43]\n\t"
      "v_add_u32 v0, s51, v0\n\t"
      "v_mfma_f32_16x16x32_bf16 v[44:47], a[24:27], a[60:63], v[44:47]\n\t"
      "s_waitcnt lgkmcnt(6)\n\t"
      "v_mfma_f32_16x16x32_bf16 v[60:63], a[28:31], a[60:63], v[60:63]\n\t"
      "ds_read_b64_tr_b16 a[12:13], v5 offset:16384\n\t"
      "v_mfma_f32_16x16x32_bf16 v[56:59], a[28:31], a[56:59], v[56:59]\n\t"
      "ds_read_b64_tr_b16 a[14:15], v5 offset:23552\n\t"
      "v_mfma_f32_16x16x32_bf16 v[52:55], a[28:31], a[52:55], v[52:55]\n\t"
      "buffer_load_dwordx4 a[128:131], v0, s[36:39], s34 offen sc1\n\t"
      "v_mfma_f32_16x16x32_bf16 v[48:51], a[28:31], a[48:51], v[48:51]\n\t"
      "s_waitcnt lgkmcnt(6)\n\t"
      "v_mfma_f32_16x16x32_bf16 v[64:67], a[0:3], a[48:51], v[64:67]\n\t"
      "ds_read_b64_tr_b16 a[16:17], v6 offset:16384\n\t"
      "v_mfma_f32_16x16x32_bf16 v[68:71], a[0:3], a[52:55], v[68:71]\n\t"
      "ds_read_b64_tr_b16 a[18:19], v6 offset:23552\n\t"
      "v_mfma_f32_16x16x32_bf16 v[72:75], a[0:3], a[56:59], v[72:75]\n\t"
      "s_add_u32 s64, s64, 1\n\t"
      "s_cmp_eq_u32 s64, 5\n\t"
      "s_cselect_b32 s65, s66, s67\n\t"
      "v_mfma_f32_16x16x32_bf16 v[76:79], a[0:3], a[60:63], v[76:79]\n\t"
      "s_waitcnt lgkmcnt(6)\n\t"
      "s_cselect_b32 s64, 0, s64\n\t"
      "buffer_load_dwordx4 a[132:135], v0, s[36:39], s34 offen offset:1024 sc1\n\t"
      "v_mfma_f32_16x16x32_bf16 v[92:95], a[4:7], a[60:63], v[92:95]\n\t"
      "ds_read_b64_tr_b16 a[20:21], v7 offset:16384\n\t"
      "v_mfma_f32_16x16x32_bf16 v[88:91], a[4:7], a[56:59], v[88:91]\n\t"
      "ds_read_b64_tr_b16 a[22:23], v7 offset:23552\n\t"
      "v_mfma_f32_16x16x32_bf16 v[84:87], a[4:7], a[52:55], v[84:87]\n\t"
      "v_mfma_f32_16x16x32_bf16 v[80:83], a[4:7], a[48:51], v[80:83]\n\t"
      "s_waitcnt lgkmcnt(6)\n\t"
      "v_mfma_f32_16x16x32_bf16 v[96:99], a[8:11], a[48:51], v[96:99]\n\t"
      "ds_read_b64_tr_b16 a[24:25], v4 offset:18432\n\t"
      "buffer_load_dwordx4 a[136:139], v0, s[36:39], s35 offen sc1\n\t"
      "v_mfma_f32_16x16x32_bf16 v[100:103], a[8:11], a[52:55], v[100:103]\n\t"
      "ds_read_b64_tr_b16 a[26:27], v4 offset:25600\n\t"
      "v_mfma_f32_16x16x32_bf16 v[104:107], a[8:11], a[56:59], v[104:107]\n\t"
      "v_add_u32 v4, s65, v4\n\t"
      "v_mfma_f32_16x16x32_bf16 v[108:111], a[8:11], a[60:63], v[108:111]\n\t"
      "s_waitcnt lgkmcnt(6)\n\t"
      "v_mfma_f32_16x16x32_bf16 v[124:127], a[12:15], a[60:63], v[124:127]\n\t"
      "ds_read_b64_tr_b16 a[28:29], v5 offset:18432\n\t"
      "v_mfma_f32_16x16x32_bf16 v[120:123], a[12:15], a[56:59], v[120:123]\n\t"
      "ds_read_b64_tr_b16 a[30:31], v5 offset:25600\n\t"
      "buffer_load_dwordx4 a[140:143], v0, s[36:39], s35 offen offset:1024 sc1\n\t"
      "v_mfma_f32_16x16x32_bf16 v[116:119], a[12:15], a[52:55], v[116:119]\n\t"
      "v_add_u32 v0, s51, v0\n\t"
      "v_add_u32 v5, s65, v5\n\t"
      "v_mfma_f32_16x16x32_bf16 v[112:115], a[12:15], a[48:51], v[112:115]\n\t"
      "s_waitcnt lgkmcnt(6)\n\t"
      "v_mfma_f32_16x16x32_bf16 v[128:131], a[16:19], a[48:51], v[128:131]\n\t"
      "ds_read_b64_tr_b16 a[0:1], v6 offset:18432\n\t"
      "v_mfma_f32_16x16x32_bf16 v[132:135], a[16:19], a[52:55], v[132:135]\n\t"
      "ds_read_b64_tr_b16 a[2:3], v6 offset:25600\n\t"
      "v_mfma_f32_16x16x32_bf16 v[136:139], a[16:19], a[56:59], v[136:139]\n\t"
      "buffer_load_dwordx4 a[144:147], v0, s[36:39], s34 offen sc1\n\t"
      "v_add_u32 v6, s65, v6\n\t"
      "v_mfma_f32_16x16x32_bf16 v[140:143], a[16:19], a[60:63], v[140:143]\n\t"
      "s_waitcnt lgkmcnt(6)\n\t"
      "v_mfma_f32_16x16x32_bf16 v[156:159], a[20:23], a[60:63], v[156:159]\n\t"
      "ds_read_b64_tr_b16 a[4:5], v7 offset:18432\n\t"
      "v_mfma_f32_16x16x32_bf16 v[152:155], a[20:23], a[56:59], v[152:155]\n\t"
      "ds_read_b64_tr_b16 a[6:7], v7 offset:25600\n\t"
      "v_mfma_f32_16x16x32_bf16 v[148:151], a[20:23], a[52:55], v[148:151]\n\t"
      "v_add_u32 v7, s65, v7\n\t"
      "v_mfma_f32_16x16x32_bf16 v[144:147], a[20:23], a[48:51], v[144:147]\n\t"
      "s_waitcnt lgkmcnt(6)\n\t"
      "buffer_load_dwordx4 a[148:151], v0, s[36:39], s34 offen offset:1024 sc1\n\t"
      "v_mfma_f32_16x16x32_bf16 v[160:163], a[24:27], a[48:51], v[160:163]\n\t"
      "ds_read_b64_tr_b16 a[8:9], v8 offset:20480\n\t"
      "v_mfma_f32_16x16x32_bf16 v[164:167], a[24:27], a[52:55], v[164:167]\n\t"
      "ds_read_b64_tr_b16 a[10:11], v8 offset:27648\n\t"
      "v_mfma_f32_16x16x32_bf16 v[168:171], a[24:27], a[56:59], v[168:171]\n\t"
      "v_add_u32 v8, s65, v8\n\t"
      "v_mfma_f32_16x16x32_bf16 v[172:175], a[24:27], a[60:63], v[172:175]\n\t"
      "s_waitcnt lgkmcnt(6)\n\t"
      "v_mfma_f32_16x16x32_bf16 v[188:191], a[28:31], a[60:63], v[188:191]\n\t"
      "ds_read_b64_tr_b16 a[12:13], v9 offset:20480\n\t"
      "buffer_load_dwordx4 a[152:155], v0, s[36:39], s35 offen sc1\n\t"
      "v_mfma_f32_16x16x32_bf16 v[184:187], a[28:31], a[56:59], v[184:187]\n\t"
      "ds_read_b64_tr_b16 a[14:15], v9 offset:27648\n\t"
      "v_mfma_f32_16x16x32_bf16 v[180:183], a[28:31], a[52:55], v[180:183]\n\t"
      "v_add_u32 v9, s65, v9\n\t"
      "v_mfma_f32_16x16x32_bf16 v[176:179], a[28:31], a[48:51], v[176:179]\n\t"
      "s_waitcnt lgkmcnt(6)\n\t"
      "v_mfma_f32_16x16x32_bf16 v[192:195], a[0:3], a[48:51], v[192:195]\n\t"
      "s_waitcnt vmcnt(45)\n\t"
      "s_barrier\n\t"
      "ds_read_b64_tr_b16 a[16:17], v4 offset:0\n\t"
      "v_mfma_f32_16x16x32_bf16 v[196:199], a[0:3], a[52:55], v[196:199]\n\t"
      "ds_read_b64_tr_b16 a[18:19], v4 offset:7168\n\t"
      "buffer_load_dwordx4 a[156:159], v0, s[36:39], s35 offen offset:1024 sc1\n\t"
      "v_mfma_f32_16x16x32_bf16 v[200:203], a[0:3], a[56:59], v[200:203]\n\t"
      "v_add_u32 v11, 57344, v4\n\t"
      "v_add_u32 v13, 57344, v5\n\t"
      "v_mfma_f32_16x16x32_bf16 v[204:207], a[0:3], a[60:63], v[204:207]\n\t"
      "s_waitcnt lgkmcnt(6)\n\t"
      "v_add_u32 v14, 57344, v6\n\t"
      "v_add_u32 v15, 57344, v7\n\t"
      "v_mfma_f32_16x16x32_bf16 v[220:223], a[4:7], a[60:63], v[220:223]\n\t"
      "ds_read_b64_tr_b16 a[20:21], v5 offset:0\n\t"
      "v_add_u32 v28, 57344, v8\n\t"
      "v_mfma_f32_16x16x32_bf16 v[216:219], a[4:7], a[56:59], v[216:219]\n\t"
      "ds_read_b64_tr_b16 a[22:23], v5 offset:7168\n\t"
      "v_add_u32 v10, 57344, v9\n\t"
      "v_mfma_f32_16x16x32_bf16 v[212:215], a[4:7], a[52:55], v[212:215]\n\t"
      "v_mfma_f32_16x16x32_bf16 v[208:211], a[4:7], a[48:51], v[208:211]\n\t"
      "s_waitcnt lgkmcnt(6)\n\t"
      "v_mfma_f32_16x16x32_bf16 v[224:227], a[8:11], a[48:51], v[224:227]\n\t"
      "ds_read_b64_tr_b16 a[24:25], v4 offset:14336\n\t"
      "v_mfma_f32_16x16x32_bf16 v[228:231], a[8:11], a[52:55], v[228:231]\n\t"
      "ds_read_b64_tr_b16 a[26:27], v4 offset:21504\n\t"
      "v_mfma_f32_16x16x32_bf16 v[232:235], a[8:11], a[56:59], v[232:235]\n\t"
      "v_mfma_f32_16x16x32_bf16 v[236:239], a[8:11], a[60:63], v[236:239]\n\t"
      "s_waitcnt lgkmcnt(6)\n\t"
      "v_mfma_f32_16x16x32_bf16 v[252:255], a[12:15], a[60:63], v[252:255]\n\t"
      "ds_read_b64_tr_b16 a[28:29], v5 offset:14336\n\t"
      "v_mfma_f32_16x16x32_bf16 v[248:251], a[12:15], a[56:59], v[248:251]\n\t"
      "ds_read_b64_tr_b16 a[30:31], v5 offset:21504\n\t"
      "v_mfma_f32_16x16x32_bf16 v[244:247], a[12:15], a[52:55], v[244:247]\n\t"
      "s_waitcnt vmcnt(27)\n\t"
      "v_mfma_f32_16x16x32_bf16 v[240:243], a[12:15], a[48:51], v[240:243]\n\t"
      "s_waitcnt lgkmcnt(6)\n\t"
      "v_mfma_f32_16x16x32_bf16 v[32:35], a[16:19], a[64:67], v[32:35]\n\t"
      "s_waitcnt vmcnt(30)\n\t"
      "s_barrier\n\t"
      "ds_read_b64_tr_b16 a[0:1], v4 offset:28672\n\t"
      "v_mfma_f32_16x16x32_bf16 v[36:39], a[16:19], a[68:71], v[36:39]\n\t"
      "ds_read_b64_tr_b16 a[2:3], v4 offset:35840\n\t"
      "v_mfma_f32_16x16x32_bf16 v[40:43], a[16:19], a[72:75], v[40:43]\n\t"
      "v_mfma_f32_16x16x32_bf16 v[44:47], a[16:19], a[76:79], v[44:47]\n\t"
      "s_waitcnt lgkmcnt(6)\n\t"
      "v_mfma_f32_16x16x32_bf16 v[60:63], a[20:23], a[76:79], v[60:63]\n\t"
      "ds_read_b64_tr_b16 a[4:5], v5 offset:28672\n\t"
      "v_mfma_f32_16x16x32_bf16 v[56:59], a[20:23], a[72:75], v[56:59]\n\t"
      "ds_read_b64_tr_b16 a[6:7], v5 offset:35840\n\t"
      "v_mfma_f32_16x16x32_bf16 v[52:55], a[20:23], a[68:71], v[52:55]\n\t"
      "s_waitcnt vmcnt(21)\n\t"
      "v_mfma_f32_16x16x32_bf16 v[48:51], a[20:23], a[64:67], v[48:51]\n\t"
      "s_waitcnt lgkmcnt(6)\n\t"
      "v_mfma_f32_16x16x32_bf16 v[32:35], a[24:27], a[80:83], v[32:35]\n\t"
      "ds_read_b64_tr_b16 a[8:9], v4 offset:43008\n\t"
      "v_mfma_f32_16x16x32_bf16 v[36:39], a[24:27], a[84:87], v[36:39]\n\t"
      "ds_read_b64_tr_b16 a[10:11], v4 offset:50176\n\t"
      "v_mfma_f32_16x16x32_bf16 v[40:43], a[24:27], a[88:91], v[40:43]\n\t"
      "v_mfma_f32_16x16x32_bf16 v[44:47], a[24:27], a[92:95], v[44:47]\n\t"
      "s_waitcnt lgkmcnt(6)\n\t"
      "v_mfma_f32_16x16x32_bf16 v[60:63], a[28:31], a[92:95], v[60:63]\n\t"
      "ds_read_b64_tr_b16 a[12:13], v5 offset:43008\n\t"
      "v_mfma_f32_16x16x32_bf16 v[56:59], a[28:31], a[88:91], v[56:59]\n\t"
      "ds_read_b64_tr_b16 a[14:15], v5 offset:50176\n\t"
      "v_mfma_f32_16x16x32_bf16 v[52:55], a[28:31], a[84:87], v[52:55]\n\t"
      "s_waitcnt vmcnt(14)\n\t"
      "v_mfma_f32_16x16x32_bf16 v[48:51], a[28:31], a[80:83], v[48:51]\n\t"
      "s_waitcnt lgkmcnt(6)\n\t"
      "v_mfma_f32_16x16x32_bf16 v[32:35], a[0:3], a[96:99], v[32:35]\n\t"
      "s_waitcnt vmcnt(10)\n\t"
      "s_barrier\n\t"
      "ds_read_b64_tr_b16 a[16:17], v11 offset:0\n\t"
      "v_mfma_f32_16x16x32_bf16 v[36:39], a[0:3], a[100:103], v[36:39]\n\t"
      "ds_read_b64_tr_b16 a[18:19], v11 offset:7168\n\t"
      STAMP(6, 7)
      "s_mov_b32 s16, 32768\n\t"
      "s_mov_b32 s64, 98304\n\t"
      "v_mfma_f32_16x16x32_bf16 v[40:43], a[0:3], a[104:107], v[40:43]\n\t"
      "s_and_b32 s55, s27, 0xfffff000\n\t"
      "s_mov_b32 s48, 38\n\t"
      "s_mov_b32 s50, 8\n\t"
      "v_mfma_f32_16x16x32_bf16 v[44:47], a[0:3], a[108:111], v[44:47]\n\t"
      "s_waitcnt lgkmcnt(6)\n\t"
      "s_sub_u32 s80, s80, 4096\n\t"
      "s_subb_u32 s81, s81, 0\n\t"
      "v_mfma_f32_16x16x32_bf16 v[60:63], a[4:7], a[108:111], v[60:63]\n\t"
      "ds_read_b64_tr_b16 a[20:21], v13 offset:0\n\t"
      "s_add_u32 s82, s82, 4096\n\t"
      "v_mfma_f32_16x16x32_bf16 v[56:59], a[4:7], a[104:107], v[56:59]\n\t"
      "ds_read_b64_tr_b16 a[22:23], v13 offset:7168\n\t"
      "s_sub_u32 s84, s84, 4096\n\t"
      "s_subb_u32 s85, s85, 0\n\t"
      "v_mfma_f32_16x16x32_bf16 v[52:55], a[4:7], a[100:103], v[52:55]\n\t"
      "s_waitcnt vmcnt(8)\n\t"
      "s_add_u32 s86, s86, 4096\n\t"
      "s_lshl2_add_u32 s56, s2, s32\n\t"
      "v_mfma_f32_16x16x32_bf16 v[48:51], a[4:7], a[96:99], v[48:51]\n\t"
      "s_waitcnt lgkmcnt(6)\n\t"
      LSTW(6, "")
      "s_lshl_b32 s56, s56, 14\n\t"
      "s_xor_b32 s57, s56, 131072\n\t"
      "v_mfma_f32_16x16x32_bf16 v[32:35], a[8:11], a[112:115], v[32:35]\n\t"
      "ds_read_b64_tr_b16 a[24:25], v11 offset:14336\n\t"
      "s_xor_b32 s58, s56, 266240\n\t"
      "s_xor_b32 s69, s56, 401408\n\t"
      "v_mfma_f32_16x16x32_bf16 v[36:39], a[8:11], a[116:119], v[36:39]\n\t"
      "ds_read_b64_tr_b16 a[26:27], v11 offset:21504\n\t"
      "s_add_u32 s67, s27, 32768\n\t"
      "s_add_u32 s19, s59, 65536\n\t"
      "v_mfma_f32_16x16x32_bf16 v[40:43], a[8:11], a[120:123], v[40:43]\n\t"
      "s_add_u32 s18, s19, 32768\n\t"
      "s_mov_b32 s68, 3\n\t"
      "v_readlane_b32 s54, v23, 0\n\t"
      "v_mfma_f32_16x16x32_bf16 v[44:47], a[8:11], a[124:127], v[44:47]\n\t"
      "s_waitcnt lgkmcnt(6)\n\t"
      "s_bitcmp1_b32 s54, 0\n\t"
      "s_cselect_b64 s[36:37], s[80:81], s[84:85]\n\t"
      "v_mfma_f32_16x16x32_bf16 v[60:63], a[12:15], a[124:127], v[60:63]\n\t"
      "ds_read_b64_tr_b16 a[28:29], v13 offset:14336\n\t"
      "s_cselect_b32 s38, s82, s86\n\t"
      "v_mfma_f32_16x16x32_bf16 v[56:59], a[12:15], a[120:123], v[56:59]\n\t"
      "ds_read_b64_tr_b16 a[30:31], v13 offset:21504\n\t"
      "s_and_b32 s54, s54, -2\n\t"
      "v_add_u32 v1, s54, v17\n\t"
      "v_mfma_f32_16x16x32_bf16 v[52:55], a[12:15], a[116:119], v[52:55]\n\t"
      "s_waitcnt vmcnt(4)\n\t"
      "v_mfma_f32_16x16x32_bf16 v[48:51], a[12:15], a[112:115], v[48:51]\n\t"
      "s_waitcnt lgkmcnt(6)\n\t"
      "v_mfma_f32_16x16x32_bf16 v[32:35], a[16:19], a[128:131], v[32:35]\n\t"
      "ds_read_b64_tr_b16 a[0:1], v6 offset:0\n\t"
      "v_mfma_f32_16x16x32_bf16 v[36:39], a[16:19], a[132:135], v[36:39]\n\t"
      "ds_read_b64_tr_b16 a[2:3], v6 offset:7168\n\t"
      "v_mfma_f32_16x16x32_bf16 v[40:43], a[16:19], a[136:139], v[40:43]\n\t"
      "v_mfma_f32_16x16x32_bf16 v[44:47], a[16:19], a[140:143], v[44:47]\n\t"
      "s_waitcnt lgkmcnt(6)\n\t"
      "v_mfma_f32_16x16x32_bf16 v[60:63], a[20:23], a[140:143], v[60:63]\n\t"
      "ds_read_b64_tr_b16 a[4:5], v7 offset:0\n\t"
      "v_mfma_f32_16x16x32_bf16 v[56:59], a[20:23], a[136:139], v[56:59]\n\t"
      "ds_read_b64_tr_b16 a[6:7], v7 offset:7168\n\t"
      "v_mfma_f32_16x16x32_bf16 v[52:55], a[20:23], a[132:135], v[52:55]\n\t"
      "s_waitcnt vmcnt(0)\n\t"
      "v_mfma_f32_16x16x32_bf16 v[48:51], a[20:23], a[128:131], v[48:51]\n\t"
      "s_waitcnt lgkmcnt(6)\n\t"
      "v_mfma_f32_16x16x32_bf16 v[32:35], a[24:27], a[144:147], v[32:35]\n\t"
      "ds_read_b64_tr_b16 a[8:9], v6 offset:14336\n\t"
      "v_mfma_f32_16x16x32_bf16 v[36:39], a[24:27], a[148:151], v[36:39]\n\t"
      "ds_read_b64_tr_b16 a[10:11], v6 offset:21504\n\t"
      "v_mfma_f32_16x16x32_bf16 v[40:43], a[24:27], a[152:155], v[40:43]\n\t"
      "s_mov_b32 m0, s24\n\t"
      "s_nop 0\n\t"
      "buffer_load_dwordx4 v1, s[36:39], s20 offen lds\n\t"
      "v_mfma_f32_16x16x32_bf16 v[44:47], a[24:27], a[156:159], v[44:47]\n\t"
      "s_waitcnt lgkmcnt(6)\n\t"
      "v_mfma_f32_16x16x32_bf16 v[60:63], a[28:31], a[156:159], v[60:63]\n\t"
      "ds_read_b64_tr_b16 a[12:13], v7 offset:14336\n\t"
      "v_mfma_f32_16x16x32_bf16 v[56:59], a[28:31], a[152:155], v[56:59]\n\t"
      "ds_read_b64_tr_b16 a[14:15], v7 offset:21504\n\t"
      "v_mfma_f32_16x16x32_bf16 v[52:55], a[28:31], a[148:151], v[52:55]\n\t"
      "v_mfma_f32_16x16x32_bf16 v[48:51], a[28:31], a[144:147], v[48:51]\n\t"
      "s_waitcnt lgkmcnt(6)\n\t"
      "v_mfma_f32_16x16x32_bf16 v[64:67], a[0:3], a[64:67], v[64:67]\n\t"
      "ds_read_b64_tr_b16 a[16:17], v6 offset:28672\n\t"
      "v_mfma_f32_16x16x32_bf16 v[68:71], a[0:3], a[68:71], v[68:71]\n\t"
      "ds_read_b64_tr_b16 a[18:19], v6 offset:35840\n\t"
      "v_mfma_f32_16x16x32_bf16 v[72:75], a[0:3], a[72:75], v[72:75]\n\t"
      "v_mul_f32 v32, v32, |v32|\n\t"
      "v_mul_f32 v33, v33, |v33|\n\t"
      "v_mfma_f32_16x16x32_bf16 v[76:79], a[0:3], a[76:79], v[76:79]\n\t"
      "s_waitcnt lgkmcnt(6)\n\t"
      "v_mul_f32 v34, v34, |v34|\n\t"
      "v_mul_f32 v35, v35, |v35|\n\t"
      "v_mfma_f32_16x16x32_bf16 v[92:95], a[4:7], a[76:79], v[92:95]\n\t"
      "ds_read_b64_tr_b16 a[20:21], v7 offset:28672\n\t"
      "v_mul_f32 v48, v48, |v48|\n\t"
      "v_mfma_f32_16x16x32_bf16 v[88:91], a[4:7], a[72:75], v[88:91]\n\t"
      "ds_read_b64_tr_b16 a[22:23], v7 offset:35840\n\t"
      "v_mul_f32 v49, v49, |v49|\n\t"
      "v_mfma_f32_16x16x32_bf16 v[84:87], a[4:7], a[68:71], v[84:87]\n\t"
      "buffer_load_dwordx4 v1, s[36:39], s21 offen offset:1024 lds\n\t"
      "v_mul_f32 v50, v50, |v50|\n\t"
      "v_mfma_f32_16x16x32_bf16 v[80:83], a[4:7], a[64:67], v[80:83]\n\t"
      "s_waitcnt lgkmcnt(6)\n\t"
      "v_mul_f32 v51, v51, |v51|\n\t"
      "v_cvt_pk_bf16_f32 v32, v32, v33\n\t"
      "v_mfma_f32_16x16x32_bf16 v[64:67], a[8:11], a[80:83], v[64:67]\n\t"
      "ds_read_b64_tr_b16 a[24:25], v6 offset:43008\n\t"
      "v_cvt_pk_bf16_f32 v33, v34, v35\n\t"
      "v_mfma_f32_16x16x32_bf16 v[68:71], a[8:11], a[84:87], v[68:71]\n\t"
      "ds_read_b64_tr_b16 a[26:27], v6 offset:50176\n\t"
      "v_cvt_pk_bf16_f32 v34, v48, v49\n\t"
      "v_mfma_f32_16x16x32_bf16 v[72:75], a[8:11], a[88:91], v[72:75]\n\t"
      "v_cvt_pk_bf16_f32 v35, v50, v51\n\t"
      "v_pk_max_i16 v32, v32, 0\n\t"
      "v_mfma_f32_16x16x32_bf16 v[76:79], a[8:11], a[92:95], v[76:79]\n\t"
      "s_waitcnt lgkmcnt(6)\n\t"
      "v_pk_max_i16 v33, v33, 0\n\t"
      "v_pk_max_i16 v34, v34, 0\n\t"
      "v_mfma_f32_16x16x32_bf16 v[92:95], a[12:15], a[92:95], v[92:95]\n\t"
      "ds_read_b64_tr_b16 a[28:29], v7 offset:43008\n\t"
      "v_pk_max_i16 v35, v35, 0\n\t"
      "v_mfma_f32_16x16x32_bf16 v[88:91], a[12:15], a[88:91], v[88:91]\n\t"
      "ds_read_b64_tr_b16 a[30:31], v7 offset:50176\n\t"
      "buffer_store_dwordx4 v[32:35], v22, s[76:79], s30 offen offset:0 sc1\n\t"
      "v_mfma_f32_16x16x32_bf16 v[84:87], a[12:15], a[84:87], v[84:87]\n\t"
      "v_mul_f32 v36, v36, |v36|\n\t"
      "v_mul_f32 v37, v37, |v37|\n\t"
      "v_mfma_f32_16x16x32_bf16 v[80:83], a[12:15], a[80:83], v[80:83]\n\t"
      "s_waitcnt lgkmcnt(6)\n\t"
      "v_mul_f32 v38, v38, |v38|\n\t"
      "v_mul_f32 v39, v39, |v39|\n\t"
      "v_mfma_f32_16x16x32_bf16 v[64:67], a[16:19], a[96:99], v[64:67]\n\t"
      "ds_read_b64_tr_b16 a[0:1], v14 offset:0\n\t"
      "v_mul_f32 v52, v52, |v52|\n\t"
      "v_mfma_f32_16x16x32_bf16 v[68:71], a[16:19], a[100:103], v[68:71]\n\t"
      "ds_read_b64_tr_b16 a[2:3], v14 offset:7168\n\t"
      "v_mul_f32 v53, v53, |v53|\n\t"
      "v_mfma_f32_16x16x32_bf16 v[72:75], a[16:19], a[104:107], v[72:75]\n\t"
      "buffer_load_dwordx4 v1, s[36:39], s22 offen offset:2048 lds\n\t"
      "v_mul_f32 v54, v54, |v54|\n\t"
      "v_mfma_f32_16x16x32_bf16 v[76:79], a[16:19], a[108:111], v[76:79]\n\t"
      "s_waitcnt lgkmcnt(6)\n\t"
      "v_mul_f32 v55, v55, |v55|\n\t"
      "v_cvt_pk_bf16_f32 v36, v36, v37\n\t"
      "v_mfma_f32_16x16x32_bf16 v[92:95], a[20:23], a[108:111], v[92:95]\n\t"
      "ds_read_b64_tr_b16 a[4:5], v15 offset:0\n\t"
      "v_cvt_pk_bf16_f32 v37, v38, v39\n\t"
      "v_mfma_f32_16x16x32_bf16 v[88:91], a[20:23], a[104:107], v[88:91]\n\t"
      "ds_read_b64_tr_b16 a[6:7], v15 offset:7168\n\t"
      "v_cvt_pk_bf16_f32 v38, v52, v53\n\t"
      "v_mfma_f32_16x16x32_bf16 v[84:87], a[20:23], a[100:103], v[84:87]\n\t"
      "v_cvt_pk_bf16_f32 v39, v54, v55\n\t"
      "v_pk_max_i16 v36, v36, 0\n\t"
      "v_mfma_f32_16x16x32_bf16 v[80:83], a[20:23], a[96:99], v[80:83]\n\t"
      "s_waitcnt lgkmcnt(6)\n\t"
      "v_pk_max_i16 v37, v37, 0\n\t"
      "v_pk_max_i16 v38, v38, 0\n\t"
      "v_mfma_f32_16x16x32_bf16 v[64:67], a[24:27], a[112:115], v[64:67]\n\t"
      "ds_read_b64_tr_b16 a[8:9], v14 offset:14336\n\t"
      "v_pk_max_i16 v39, v39, 0\n\t"
      "v_mfma_f32_16x16x32_bf16 v[68:71], a[24:27], a[116:119], v[68:71]\n\t"
      "ds_read_b64_tr_b16 a[10:11], v14 offset:21504\n\t"
      "buffer_store_dwordx4 v[36:39], v22, s[76:79], s30 offen offset:1024 sc1\n\t"
      "v_mfma_f32_16x16x32_bf16 v[72:75], a[24:27], a[120:123], v[72:75]\n\t"
      "v_mul_f32 v40, v40, |v40|\n\t"
      "v_mul_f32 v41, v41, |v41|\n\t"
      "v_mfma_f32_16x16x32_bf16 v[76:79], a[24:27], a[124:127], v[76:79]\n\t"
      "s_waitcnt lgkmcnt(6)\n\t"
      "v_mul_f32 v42, v42, |v42|\n\t"
      "v_mul_f32 v43, v43, |v43|\n\t"
      "v_mfma_f32_16x16x32_bf16 v[92:95], a[28:31], a[124:127], v[92:95]\n\t"
      "ds_read_b64_tr_b16 a[12:13], v15 offset:14336\n\t"
      "v_mul_f32 v56, v56, |v56|\n\t"
      "v_mfma_f32_16x16x32_bf16 v[88:91], a[28:31], a[120:123], v[88:91]\n\t"
      "ds_read_b64_tr_b16 a[14:15], v15 offset:21504\n\t"
      "v_mul_f32 v57, v57, |v57|\n\t"
      "v_mfma_f32_16x16x32_bf16 v[84:87], a[28:31], a[116:119], v[84:87]\n\t"
      "buffer_load_dwordx4 v1, s[36:39], s23 offen offset:3072 lds\n\t"
      "s_add_u32 s24, s24, 16384\n\t"
      "s_and_b32 s24, s24, 0x1ffff\n\t"
      "v_mfma_f32_16x16x32_bf16 v[80:83], a[28:31], a[112:115], v[80:83]\n\t"
      "s_waitcnt lgkmcnt(6)\n\t"
      "v_readlane_b32 s54, v23, 1\n\t"
      "s_bitcmp1_b32 s54, 0\n\t"
      "v_mfma_f32_16x16x32_bf16 v[64:67], a[0:3], a[128:131], v[64:67]\n\t"
      "ds_read_b64_tr_b16 a[16:17], v8 offset:6144\n\t"
      "s_cselect_b64 s[36:37], s[80:81], s[84:85]\n\t"
      "s_cselect_b32 s38, s82, s86\n\t"
      "v_mfma_f32_16x16x32_bf16 v[68:71], a[0:3], a[132:135], v[68:71]\n\t"
      "ds_read_b64_tr_b16 a[18:19], v8 offset:13312\n\t"
      "s_and_b32 s54, s54, -2\n\t"
      "v_add_u32 v1, s54, v17\n\t"
      "v_mfma_f32_16x16x32_bf16 v[72:75], a[0:3], a[136:139], v[72:75]\n\t"
      "v_mul_f32 v58, v58, |v58|\n\t"
      "v_mul_f32 v59, v59, |v59|\n\t"
      "v_mfma_f32_16x16x32_bf16 v[76:79], a[0:3], a[140:143], v[76:79]\n\t"
      "s_waitcnt lgkmcnt(6)\n\t"
      "v_cvt_pk_bf16_f32 v40, v40, v41\n\t"
      "v_cvt_pk_bf16_f32 v41, v42, v43\n\t"
      "v_mfma_f32_16x16x32_bf16 v[92:95], a[4:7], a[140:143], v[92:95]\n\t"
      "ds_read_b64_tr_b16 a[20:21], v9 offset:6144\n\t"
      "v_cvt_pk_bf16_f32 v42, v56, v57\n\t"
      "v_mfma_f32_16x16x32_bf16 v[88:91], a[4:7], a[136:139], v[88:91]\n\t"
      "ds_read_b64_tr_b16 a[22:23], v9 offset:13312\n\t"
      "v_cvt_pk_bf16_f32 v43, v58, v59\n\t"
      "v_mfma_f32_16x16x32_bf16 v[84:87], a[4:7], a[132:135], v[84:87]\n\t"
      "v_pk_max_i16 v40, v40, 0\n\t"
      "v_pk_max_i16 v41, v41, 0\n\t"
      "v_mfma_f32_16x16x32_bf16 v[80:83], a[4:7], a[128:131], v[80:83]\n\t"
      "s_waitcnt lgkmcnt(6)\n\t"
      "v_pk_max_i16 v42, v42, 0\n\t"
      "v_pk_max_i16 v43, v43, 0\n\t"
      "v_mfma_f32_16x16x32_bf16 v[64:67], a[8:11], a[144:147], v[64:67]\n\t"
      "ds_read_b64_tr_b16 a[24:25], v8 offset:20480\n\t"
      "buffer_store_dwordx4 v[40:43], v29, s[76:79], s30 offen offset:0 sc1\n\t"
      "v_mfma_f32_16x16x32_bf16 v[68:71], a[8:11], a[148:151], v[68:71]\n\t"
      "ds_read_b64_tr_b16 a[26:27], v8 offset:27648\n\t"
      "v_mul_f32 v44, v44, |v44|\n\t"
      "v_mfma_f32_16x16x32_bf16 v[72:75], a[8:11], a[152:155], v[72:75]\n\t"
      "v_mul_f32 v45, v45, |v45|\n\t"
      "v_mul_f32 v46, v46, |v46|\n\t"
      "v_mfma_f32_16x16x32_bf16 v[76:79], a[8:11], a[156:159], v[76:79]\n\t"
      "s_waitcnt lgkmcnt(6)\n\t"
      "v_mul_f32 v47, v47, |v47|\n\t"
      "v_mul_f32 v60, v60, |v60|\n\t"
      "v_mfma_f32_16x16x32_bf16 v[92:95], a[12:15], a[156:159], v[92:95]\n\t"
      "ds_read_b64_tr_b16 a[28:29], v9 offset:20480\n\t"
      "v_mul_f32 v61, v61, |v61|\n\t"
      "v_mfma_f32_16x16x32_bf16 v[88:91], a[12:15], a[152:155], v[88:91]\n\t"
      "ds_read_b64_tr_b16 a[30:31], v9 offset:27648\n\t"
      "v_mul_f32 v62, v62, |v62|\n\t"
      "v_mfma_f32_16x16x32_bf16 v[84:87], a[12:15], a[148:151], v[84:87]\n\t"
      "s_mov_b32 m0, s24\n\t"
      "s_nop 0\n\t"
      "buffer_load_dwordx4 v1, s[36:39], s20 offen lds\n\t"
      "v_mfma_f32_16x16x32_bf16 v[80:83], a[12:15], a[144:147], v[80:83]\n\t"
      "s_waitcnt lgkmcnt(6)\n\t"
      "v_mul_f32 v63, v63, |v63|\n\t"
      "v_cvt_pk_bf16_f32 v44, v44, v45\n\t"
      "v_mfma_f32_16x16x32_bf16 v[224:227], a[16:19], a[64:67], v[224:227]\n\t"
      "ds_read_b64_tr_b16 a[0:1], v8 offset:34816\n\t"
      "v_cvt_pk_bf16_f32 v45, v46, v47\n\t"
      "v_mfma_f32_16x16x32_bf16 v[228:231], a[16:19], a[68:71], v[228:231]\n\t"
      "ds_read_b64_tr_b16 a[2:3], v8 offset:41984\n\t"
      "v_cvt_pk_bf16_f32 v46, v60, v61\n\t"
      "v_mfma_f32_16x16x32_bf16 v[232:235], a[16:19], a[72:75], v[232:235]\n\t"
      "v_cvt_pk_bf16_f32 v47, v62, v63\n\t"
      "v_pk_max_i16 v44, v44, 0\n\t"
      "v_mfma_f32_16x16x32_bf16 v[236:239], a[16:19], a[76:79], v[236:239]\n\t"
      "s_waitcnt lgkmcnt(6)\n\t"
      "v_pk_max_i16 v45, v45, 0\n\t"
      "v_pk_max_i16 v46, v46, 0\n\t"
      "v_mfma_f32_16x16x32_bf16 v[252:255], a[20:23], a[76:79], v[252:255]\n\t"
      "ds_read_b64_tr_b16 a[4:5], v9 offset:34816\n\t"
      "v_pk_max_i16 v47, v47, 0\n\t"
      "v_mfma_f32_16x16x32_bf16 v[248:251], a[20:23], a[72:75], v[248:251]\n\t"
      "ds_read_b64_tr_b16 a[6:7], v9 offset:41984\n\t"
      "buffer_store_dwordx4 v[44:47], v29, s[76:79], s30 offen offset:1024 sc1\n\t"
      "v_mfma_f32_16x16x32_bf16 v[244:247], a[20:23], a[68:71], v[244:247]\n\t"
      "v_mul_f32 v64, v64, |v64|\n\t"
      "v_mul_f32 v65, v65, |v65|\n\t"
      "v_mfma_f32_16x16x32_bf16 v[240:243], a[20:23], a[64:67], v[240:243]\n\t"
      "s_waitcnt lgkmcnt(6)\n\t"
      "v_mul_f32 v66, v66, |v66|\n\t"
      "v_mul_f32 v67, v67, |v67|\n\t"
      "v_mfma_f32_16x16x32_bf16 v[224:227], a[24:27], a[80:83], v[224:227]\n\t"
      "ds_read_b64_tr_b16 a[8:9], v8 offset:49152\n\t"
      "v_mul_f32 v80, v80, |v80|\n\t"
      "v_mfma_f32_16x16x32_bf16 v[228:231], a[24:27], a[84:87], v[228:231]\n\t"
      "ds_read_b64_tr_b16 a[10:11], v8 offset:56320\n\t"
      "v_mul_f32 v81, v81, |v81|\n\t"
      "v_mfma_f32_16x16x32_bf16 v[232:235], a[24:27], a[88:91], v[232:235]\n\t"
      "buffer_load_dwordx4 v1, s[36:39], s21 offen offset:1024 lds\n\t"
      "v_mul_f32 v82, v82, |v82|\n\t"
      "v_mfma_f32_16x16x32_bf16 v[236:239], a[24:27], a[92:95], v[236:239]\n\t"
      "s_waitcnt lgkmcnt(6)\n\t"
      "v_mul_f32 v83, v83, |v83|\n\t"
      "v_cvt_pk_bf16_f32 v64, v64, v65\n\t"
      "v_mfma_f32_16x16x32_bf16 v[252:255], a[28:31], a[92:95], v[252:255]\n\t"
      "ds_read_b64_tr_b16 a[12:13], v9 offset:49152\n\t"
      "v_cvt_pk_bf16_f32 v65, v66, v67\n\t"
      "v_mfma_f32_16x16x32_bf16 v[248:251], a[28:31], a[88:91], v[248:251]\n\t"
      "ds_read_b64_tr_b16 a[14:15], v9 offset:56320\n\t"
      "v_cvt_pk_bf16_f32 v66, v80, v81\n\t"
      "v_mfma_f32_16x16x32_bf16 v[244:247], a[28:31], a[84:87], v[244:247]\n\t"
      "v_cvt_pk_bf16_f32 v67, v82, v83\n\t"
      "v_pk_max_i16 v64, v64, 0\n\t"
      "v_mfma_f32_16x16x32_bf16 v[240:243], a[28:31], a[80:83], v[240:243]\n\t"
      "s_waitcnt lgkmcnt(6)\n\t"
      "v_pk_max_i16 v65, v65, 0\n\t"
      "v_pk_max_i16 v66, v66, 0\n\t"
      "v_mfma_f32_16x16x32_bf16 v[224:227], a[0:3], a[96:99], v[224:227]\n\t"
      "ds_read_b64_tr_b16 a[16:17], v28 offset:6144\n\t"
      "v_pk_max_i16 v67, v67, 0\n\t"
      "s_add_u32 s30, s30, 131072\n\t"
      "v_mfma_f32_16x16x32_bf16 v[228:231], a[0:3], a[100:103], v[228:231]\n\t"
      "ds_read_b64_tr_b16 a[18:19], v28 offset:13312\n\t"
      "buffer_store_dwordx4 v[64:67], v22, s[76:79], s30 offen offset:0 sc1\n\t"
      "v_mfma_f32_16x16x32_bf16 v[232:235], a[0:3], a[104:107], v[232:235]\n\t"
      "v_mul_f32 v68, v68, |v68|\n\t"
      "v_mul_f32 v69, v69, |v69|\n\t"
      "v_mfma_f32_16x16x32_bf16 v[236:239], a[0:3], a[108:111], v[236:239]\n\t"
      "s_waitcnt lgkmcnt(6)\n\t"
      "v_mul_f32 v70, v70, |v70|\n\t"
      "v_mul_f32 v71, v71, |v71|\n\t"
      "v_mfma_f32_16x16x32_bf16 v[252:255], a[4:7], a[108:111], v[252:255]\n\t"
      "ds_read_b64_tr_b16 a[20:21], v10 offset:6144\n\t"
      "v_mul_f32 v84, v84, |v84|\n\t"
      "v_mfma_f32_16x16x32_bf16 v[248:251], a[4:7], a[104:107], v[248:251]\n\t"
      "ds_read_b64_tr_b16 a[22:23], v10 offset:13312\n\t"
      "v_mul_f32 v85, v85, |v85|\n\t"
      "v_mfma_f32_16x16x32_bf16 v[244:247], a[4:7], a[100:103], v[244:247]\n\t"
      "buffer_load_dwordx4 v1, s[36:39], s22 offen offset:2048 lds\n\t"
      "v_mul_f32 v86, v86, |v86|\n\t"
      "v_mfma_f32_16x16x32_bf16 v[240:243], a[4:7], a[96:99], v[240:243]\n\t"
      "s_waitcnt lgkmcnt(6)\n\t"
      "v_mul_f32 v87, v87, |v87|\n\t"
      "v_cvt_pk_bf16_f32 v68, v68, v69\n\t"
      "v_mfma_f32_16x16x32_bf16 v[224:227], a[8:11], a[112:115], v[224:227]\n\t"
      "ds_read_b64_tr_b16 a[24:25], v28 offset:20480\n\t"
      "v_cvt_pk_bf16_f32 v69, v70, v71\n\t"
      "v_mfma_f32_16x16x32_bf16 v[228:231], a[8:11], a[116:119], v[228:231]\n\t"
      "ds_read_b64_tr_b16 a[26:27], v28 offset:27648\n\t"
      "v_cvt_pk_bf16_f32 v70, v84, v85\n\t"
      "v_mfma_f32_16x16x32_bf16 v[232:235], a[8:11], a[120:123], v[232:235]\n\t"
      "v_cvt_pk_bf16_f32 v71, v86, v87\n\t"
      "v_pk_max_i16 v68, v68, 0\n\t"
      "v_mfma_f32_16x16x32_bf16 v[236:239], a[8:11], a[124:127], v[236:239]\n\t"
      "s_waitcnt lgkmcnt(6)\n\t"
      "v_pk_max_i16 v69, v69, 0\n\t"
      "v_pk_max_i16 v70, v70, 0\n\t"
      "v_mfma_f32_16x16x32_bf16 v[252:255], a[12:15], a[124:127], v[252:255]\n\t"
      "ds_read_b64_tr_b16 a[28:29], v10 offset:20480\n\t"
      "v_pk_max_i16 v71, v71, 0\n\t"
      "v_mfma_f32_16x16x32_bf16 v[248:251], a[12:15], a[120:123], v[248:251]\n\t"
      "ds_read_b64_tr_b16 a[30:31], v10 offset:27648\n\t"
      "buffer_store_dwordx4 v[68:71], v22, s[76:79], s30 offen offset:1024 sc1\n\t"
      "v_mfma_f32_16x16x32_bf16 v[244:247], a[12:15], a[116:119], v[244:247]\n\t"
      "v_mul_f32 v72, v72, |v72|\n\t"
      "v_mul_f32 v73, v73, |v73|\n\t"
      "v_mfma_f32_16x16x32_bf16 v[240:243], a[12:15], a[112:115], v[240:243]\n\t"
      "s_waitcnt lgkmcnt(6)\n\t"
      "v_mul_f32 v74, v74, |v74|\n\t"
      "v_mul_f32 v75, v75, |v75|\n\t"
      "v_mfma_f32_16x16x32_bf16 v[224:227], a[16:19], a[128:131], v[224:227]\n\t"
      "ds_read_b64_tr_b16 a[0:1], v4 offset:2048\n\t"
      "v_mul_f32 v88, v88, |v88|\n\t"
      "v_mfma_f32_16x16x32_bf16 v[228:231], a[16:19], a[132:135], v[228:231]\n\t"
      "ds_read_b64_tr_b16 a[2:3], v4 offset:9216\n\t"
      "v_mul_f32 v89, v89, |v89|\n\t"
      "v_mfma_f32_16x16x32_bf16 v[232:235], a[16:19], a[136:139], v[232:235]\n\t"
      "buffer_load_dwordx4 v1, s[36:39], s23 offen offset:3072 lds\n\t"
      "s_add_u32 s24, s24, 16384\n\t"
      "s_and_b32 s24, s24, 0x1ffff\n\t"
      "v_mfma_f32_16x16x32_bf16 v[236:239], a[16:19], a[140:143], v[236:239]\n\t"
      "s_waitcnt lgkmcnt(6)\n\t"
      "v_readlane_b32 s54, v23, 2\n\t"
      "s_bitcmp1_b32 s54, 0\n\t"
      "v_mfma_f32_16x16x32_bf16 v[252:255], a[20:23], a[140:143], v[252:255]\n\t"
      "ds_read_b64_tr_b16 a[4:5], v5 offset:2048\n\t"
      "s_cselect_b64 s[36:37], s[80:81], s[84:85]\n\t"
      "s_cselect_b32 s38, s82, s86\n\t"
      "v_mfma_f32_16x16x32_bf16 v[248:251], a[20:23], a[136:139], v[248:251]\n\t"
      "ds_read_b64_tr_b16 a[6:7], v5 offset:9216\n\t"
      "s_and_b32 s54, s54, -2\n\t"
      "v_add_u32 v1, s54, v17\n\t"
      "v_mfma_f32_16x16x32_bf16 v[244:247], a[20:23], a[132:135], v[244:247]\n\t"
      "v_mul_f32 v90, v90, |v90|\n\t"
      "v_mul_f32 v91, v91, |v91|\n\t"
      "v_mfma_f32_16x16x32_bf16 v[240:243], a[20:23], a[128:131], v[240:243]\n\t"
      "s_waitcnt lgkmcnt(6)\n\t"
      "v_cvt_pk_bf16_f32 v72, v72, v73\n\t"
      "v_cvt_pk_bf16_f32 v73, v74, v75\n\t"
      "v_mfma_f32_16x16x32_bf16 v[224:227], a[24:27], a[144:147], v[224:227]\n\t"
      "ds_read_b64_tr_b16 a[8:9], v4 offset:16384\n\t"
      "v_cvt_pk_bf16_f32 v74, v88, v89\n\t"
      "v_mfma_f32_16x16x32_bf16 v[228:231], a[24:27], a[148:151], v[228:231]\n\t"
      "ds_read_b64_tr_b16 a[10:11], v4 offset:23552\n\t"
      "v_cvt_pk_bf16_f32 v75, v90, v91\n\t"
      "v_mfma_f32_16x16x32_bf16 v[232:235], a[24:27], a[152:155], v[232:235]\n\t"
      "v_pk_max_i16 v72, v72, 0\n\t"
      "v_pk_max_i16 v73, v73, 0\n\t"
      "v_mfma_f32_16x16x32_bf16 v[236:239], a[24:27], a[156:159], v[236:239]\n\t"
      "s_waitcnt lgkmcnt(6)\n\t"
      "v_pk_max_i16 v74, v74, 0\n\t"
      "v_pk_max_i16 v75, v75, 0\n\t"
      "v_mfma_f32_16x16x32_bf16 v[252:255], a[28:31], a[156:159], v[252:255]\n\t"
      "ds_read_b64_tr_b16 a[12:13], v5 offset:16384\n\t"
      "buffer_store_dwordx4 v[72:75], v29, s[76:79], s30 offen offset:0 sc1\n\t"
      "v_mfma_f32_16x16x32_bf16 v[248:251], a[28:31], a[152:155], v[248:251]\n\t"
      "ds_read_b64_tr_b16 a[14:15], v5 offset:23552\n\t"
      "v_mul_f32 v76, v76, |v76|\n\t"
      "v_mfma_f32_16x16x32_bf16 v[244:247], a[28:31], a[148:151], v[244:247]\n\t"
      "v_mul_f32 v77, v77, |v77|\n\t"
      "v_mul_f32 v78, v78, |v78|\n\t"
      "v_mfma_f32_16x16x32_bf16 v[240:243], a[28:31], a[144:147], v[240:243]\n\t"
      "s_waitcnt lgkmcnt(6)\n\t"
      "v_mul_f32 v79, v79, |v79|\n\t"
      "v_mul_f32 v92, v92, |v92|\n\t"
      "v_mfma_f32_16x16x32_bf16 v[96:99], a[0:3], a[64:67], v[96:99]\n\t"
      "ds_read_b64_tr_b16 a[16:17], v4 offset:30720\n\t"
      "v_mul_f32 v93, v93, |v93|\n\t"
      "v_mfma_f32_16x16x32_bf16 v[100:103], a[0:3], a[68:71], v[100:103]\n\t"
      "ds_read_b64_tr_b16 a[18:19], v4 offset:37888\n\t"
      "v_mul_f32 v94, v94, |v94|\n\t"
      "v_mfma_f32_16x16x32_bf16 v[104:107], a[0:3], a[72:75], v[104:107]\n\t"
      "v_mul_f32 v95, v95, |v95|\n\t"
      "v_cvt_pk_bf16_f32 v76, v76, v77\n\t"
      "v_mfma_f32_16x16x32_bf16 v[108:111], a[0:3], a[76:79], v[108:111]\n\t"
      "s_waitcnt lgkmcnt(6)\n\t"
      "v_cvt_pk_bf16_f32 v77, v78, v79\n\t"
      "v_cvt_pk_bf16_f32 v78, v92, v93\n\t"
      "v_mfma_f32_16x16x32_bf16 v[124:127], a[4:7], a[76:79], v[124:127]\n\t"
      "ds_read_b64_tr_b16 a[20:21], v5 offset:30720\n\t"
      "v_cvt_pk_bf16_f32 v79, v94, v95\n\t"
      "v_mfma_f32_16x16x32_bf16 v[120:123], a[4:7], a[72:75], v[120:123]\n\t"
      "ds_read_b64_tr_b16 a[22:23], v5 offset:37888\n\t"
      "v_pk_max_i16 v76, v76, 0\n\t"
      "v_mfma_f32_16x16x32_bf16 v[116:119], a[4:7], a[68:71], v[116:119]\n\t"
      "v_pk_max_i16 v77, v77, 0\n\t"
      "v_pk_max_i16 v78, v78, 0\n\t"
      "v_mfma_f32_16x16x32_bf16 v[112:115], a[4:7], a[64:67], v[112:115]\n\t"
      "s_waitcnt lgkmcnt(6)\n\t"
      "v_pk_max_i16 v79, v79, 0\n\t"
      "buffer_store_dwordx4 v[76:79], v29, s[76:79], s30 offen offset:1024 sc1\n\t"
      "v_mfma_f32_16x16x32_bf16 v[96:99], a[8:11], a[80:83], v[96:99]\n\t"
      "ds_read_b64_tr_b16 a[24:25], v4 offset:45056\n\t"
      "v_mul_f32 v224, v224, |v224|\n\t"
      "v_mfma_f32_16x16x32_bf16 v[100:103], a[8:11], a[84:87], v[100:103]\n\t"
      "ds_read_b64_tr_b16 a[26:27], v4 offset:52224\n\t"
      "v_mul_f32 v225, v225, |v225|\n\t"
      "v_mfma_f32_16x16x32_bf16 v[104:107], a[8:11], a[88:91], v[104:107]\n\t"
      "v_mul_f32 v226, v226, |v226|\n\t"
      "v_mul_f32 v227, v227, |v227|\n\t"
      "v_mfma_f32_16x16x32_bf16 v[108:111], a[8:11], a[92:95], v[108:111]\n\t"
      "s_waitcnt lgkmcnt(6)\n\t"
      "v_mul_f32 v240, v240, |v240|\n\t"
      "v_mul_f32 v241, v241, |v241|\n\t"
      "v_mfma_f32_16x16x32_bf16 v[124:127], a[12:15], a[92:95], v[124:127]\n\t"
      "ds_read_b64_tr_b16 a[28:29], v5 offset:45056\n\t"
      "v_mul_f32 v242, v242, |v242|\n\t"
      "v_mfma_f32_16x16x32_bf16 v[120:123], a[12:15], a[88:91], v[120:123]\n\t"
      "ds_read_b64_tr_b16 a[30:31], v5 offset:52224\n\t"
      "v_mul_f32 v243, v243, |v243|\n\t"
      "v_mfma_f32_16x16x32_bf16 v[116:119], a[12:15], a[84:87], v[116:119]\n\t"
      "v_cvt_pk_bf16_f32 v224, v224, v225\n\t"
      "v_cvt_pk_bf16_f32 v225, v226, v227\n\t"
      "v_mfma_f32_16x16x32_bf16 v[112:115], a[12:15], a[80:83], v[112:115]\n\t"
      "s_waitcnt lgkmcnt(6)\n\t"
      "v_cvt_pk_bf16_f32 v226, v240, v241\n\t"
      "v_cvt_pk_bf16_f32 v227, v242, v243\n\t"
      "v_mfma_f32_16x16x32_bf16 v[96:99], a[16:19], a[96:99], v[96:99]\n\t"
      "ds_read_b64_tr_b16 a[0:1], v11 offset:2048\n\t"
      "v_pk_max_i16 v224, v224, 0\n\t"
      "v_mfma_f32_16x16x32_bf16 v[100:103], a[16:19], a[100:103], v[100:103]\n\t"
      "ds_read_b64_tr_b16 a[2:3], v11 offset:9216\n\t"
      "v_pk_max_i16 v225, v225, 0\n\t"
      "v_mfma_f32_16x16x32_bf16 v[104:107], a[16:19], a[104:107], v[104:107]\n\t"
      "v_pk_max_i16 v226, v226, 0\n\t"
      "v_pk_max_i16 v227, v227, 0\n\t"
      "v_mfma_f32_16x16x32_bf16 v[108:111], a[16:19], a[108:111], v[108:111]\n\t"
      "s_waitcnt lgkmcnt(6)\n\t"
      "v_mul_f32 v228, v228, |v228|\n\t"
      "v_mul_f32 v229, v229, |v229|\n\t"
      "v_mfma_f32_16x16x32_bf16 v[124:127], a[20:23], a[108:111], v[124:127]\n\t"
      "ds_read_b64_tr_b16 a[4:5], v13 offset:2048\n\t"
      "v_mul_f32 v230, v230, |v230|\n\t"
      "v_mfma_f32_16x16x32_bf16 v[120:123], a[20:23], a[104:107], v[120:123]\n\t"
      "ds_read_b64_tr_b16 a[6:7], v13 offset:9216\n\t"
      "v_mul_f32 v231, v231, |v231|\n\t"
      "v_mfma_f32_16x16x32_bf16 v[116:119], a[20:23], a[100:103], v[116:119]\n\t"
      "v_mul_f32 v244, v244, |v244|\n\t"
      "v_mul_f32 v245, v245, |v245|\n\t"
      "buffer_store_dwordx4 v[224:227], v22, s[76:79], s31 offen offset:0 sc1\n\t"
      "v_mfma_f32_16x16x32_bf16 v[112:115], a[20:23], a[96:99], v[112:115]\n\t"
      "s_waitcnt lgkmcnt(6)\n\t"
      "v_mul_f32 v246, v246, |v246|\n\t"
      "v_mul_f32 v247, v247, |v247|\n\t"
      "v_mfma_f32_16x16x32_bf16 v[96:99], a[24:27], a[112:115], v[96:99]\n\t"
      "ds_read_b64_tr_b16 a[8:9], v11 offset:16384\n\t"
      "v_cvt_pk_bf16_f32 v228, v228, v229\n\t"
      "v_mfma_f32_16x16x32_bf16 v[100:103], a[24:27], a[116:119], v[100:103]\n\t"
      "ds_read_b64_tr_b16 a[10:11], v11 offset:23552\n\t"
      "v_cvt_pk_bf16_f32 v229, v230, v231\n\t"
      "v_mfma_f32_16x16x32_bf16 v[104:107], a[24:27], a[120:123], v[104:107]\n\t"
      "v_cvt_pk_bf16_f32 v230, v244, v245\n\t"
      "v_cvt_pk_bf16_f32 v231, v246, v247\n\t"
      "v_mfma_f32_16x16x32_bf16 v[108:111], a[24:27], a[124:127], v[108:111]\n\t"
      "s_waitcnt lgkmcnt(6)\n\t"
      "v_pk_max_i16 v228, v228, 0\n\t"
      "v_pk_max_i16 v229, v229, 0\n\t"
      "v_mfma_f32_16x16x32_bf16 v[124:127], a[28:31], a[124:127], v[124:127]\n\t"
      "ds_read_b64_tr_b16 a[12:13], v13 offset:16384\n\t"
      "v_pk_max_i16 v230, v230, 0\n\t"
      "v_mfma_f32_16x16x32_bf16 v[120:123], a[28:31], a[120:123], v[120:123]\n\t"
      "ds_read_b64_tr_b16 a[14:15], v13 offset:23552\n\t"
      "v_pk_max_i16 v231, v231, 0\n\t"
      "v_mfma_f32_16x16x32_bf16 v[116:119], a[28:31], a[116:119], v[116:119]\n\t"
      "v_mul_f32 v232, v232, |v232|\n\t"
      "v_mul_f32 v233, v233, |v233|\n\t"
      "v_mfma_f32_16x16x32_bf16 v[112:115], a[28:31], a[112:115], v[112:115]\n\t"
      "s_waitcnt lgkmcnt(6)\n\t"
      "v_mul_f32 v234, v234, |v234|\n\t"
      "v_mul_f32 v235, v235, |v235|\n\t"
      "v_mfma_f32_16x16x32_bf16 v[96:99], a[0:3], a[128:131], v[96:99]\n\t"
      "ds_read_b64_tr_b16 a[16:17], v6 offset:2048\n\t"
      "v_mul_f32 v248, v248, |v248|\n\t"
      "v_mfma_f32_16x16x32_bf16 v[100:103], a[0:3], a[132:135], v[100:103]\n\t"
      "ds_read_b64_tr_b16 a[18:19], v6 offset:9216\n\t"
      "v_mul_f32 v249, v249, |v249|\n\t"
      "v_mfma_f32_16x16x32_bf16 v[104:107], a[0:3], a[136:139], v[104:107]\n\t"
      "v_mul_f32 v250, v250, |v250|\n\t"
      "v_mul_f32 v251, v251, |v251|\n\t"
      "v_mfma_f32_16x16x32_bf16 v[108:111], a[0:3], a[140:143], v[108:111]\n\t"
      "s_waitcnt lgkmcnt(6)\n\t"
      "v_cvt_pk_bf16_f32 v232, v232, v233\n\t"
      "v_cvt_pk_bf16_f32 v233, v234, v235\n\t"
      "v_mfma_f32_16x16x32_bf16 v[124:127], a[4:7], a[140:143], v[124:127]\n\t"
      "ds_read_b64_tr_b16 a[20:21], v7 offset:2048\n\t"
      "v_cvt_pk_bf16_f32 v234, v248, v249\n\t"
      "v_mfma_f32_16x16x32_bf16 v[120:123], a[4:7], a[136:139], v[120:123]\n\t"
      "ds_read_b64_tr_b16 a[22:23], v7 offset:9216\n\t"
      "v_cvt_pk_bf16_f32 v235, v250, v251\n\t"
      "buffer_store_dwordx4 v[228:231], v22, s[76:79], s31 offen offset:1024 sc1\n\t"
      "v_mfma_f32_16x16x32_bf16 v[116:119], a[4:7], a[132:135], v[116:119]\n\t"
      "v_pk_max_i16 v232, v232, 0\n\t"
      "v_pk_max_i16 v233, v233, 0\n\t"
      "v_mfma_f32_16x16x32_bf16 v[112:115], a[4:7], a[128:131], v[112:115]\n\t"
      "s_waitcnt lgkmcnt(6)\n\t"
      "v_pk_max_i16 v234, v234, 0\n\t"
      "v_pk_max_i16 v235, v235, 0\n\t"
      "v_mfma_f32_16x16x32_bf16 v[96:99], a[8:11], a[144:147], v[96:99]\n\t"
      "ds_read_b64_tr_b16 a[24:25], v6 offset:16384\n\t"
      "v_mul_f32 v236, v236, |v236|\n\t"
      "v_mfma_f32_16x16x32_bf16 v[100:103], a[8:11], a[148:151], v[100:103]\n\t"
      "ds_read_b64_tr_b16 a[26:27], v6 offset:23552\n\t"
      "s_waitcnt vmcnt(2)\n\t"
      "s_barrier\n\t"
      "s_lshl_b32 s5, s32, 24\n\t"
      "s_add_u32 s5, s5, s67\n\t"
      "v_mov_b32 v2, s8\n\t"
      "v_mov_b32 v3, s9\n\t"
      "s_mov_b64 exec, 1\n\t"
      "buffer_store_dwordx2 v[2:3], off, s[96:99], s5 sc1\n\t"
      "s_mov_b64 exec, -1\n\t"
      STAMP(42, 43)
      "v_mfma_f32_16x16x32_bf16 v[104:107], a[8:11], a[152:155], v[104:107]\n\t"
      "v_mul_f32 v237, v237, |v237|\n\t"
      "v_mul_f32 v238, v238, |v238|\n\t"
      "v_mfma_f32_16x16x32_bf16 v[108:111], a[8:11], a[156:159], v[108:111]\n\t"
      "s_waitcnt lgkmcnt(6)\n\t"
      "v_mul_f32 v239, v239, |v239|\n\t"
      "v_mul_f32 v252, v252, |v252|\n\t"
      "v_mfma_f32_16x16x32_bf16 v[124:127], a[12:15], a[156:159], v[124:127]\n\t"
      "ds_read_b64_tr_b16 a[28:29], v7 offset:16384\n\t"
      "v_mul_f32 v253, v253, |v253|\n\t"
      "v_mfma_f32_16x16x32_bf16 v[120:123], a[12:15], a[152:155], v[120:123]\n\t"
      "ds_read_b64_tr_b16 a[30:31], v7 offset:23552\n\t"
      "v_mul_f32 v254, v254, |v254|\n\t"
      "v_mfma_f32_16x16x32_bf16 v[116:119], a[12:15], a[148:151], v[116:119]\n\t"
      "v_mul_f32 v255, v255, |v255|\n\t"
      "v_cvt_pk_bf16_f32 v236, v236, v237\n\t"
      "v_mfma_f32_16x16x32_bf16 v[112:115], a[12:15], a[144:147], v[112:115]\n\t"
      "s_waitcnt lgkmcnt(6)\n\t"
      "v_cvt_pk_bf16_f32 v237, v238, v239\n\t"
      "v_cvt_pk_bf16_f32 v238, v252, v253\n\t"
      "v_mfma_f32_16x16x32_bf16 v[128:131], a[16:19], a[64:67], v[128:131]\n\t"
      "ds_read_b64_tr_b16 a[0:1], v6 offset:30720\n\t"
      "v_cvt_pk_bf16_f32 v239, v254, v255\n\t"
      LSTW(42, "")
      "v_mfma_f32_16x16x32_bf16 v[132:135], a[16:19], a[68:71], v[132:135]\n\t"
      "ds_read_b64_tr_b16 a[2:3], v6 offset:37888\n\t"
      "v_pk_max_i16 v236, v236, 0\n\t"
      "v_mfma_f32_16x16x32_bf16 v[136:139], a[16:19], a[72:75], v[136:139]\n\t"
      "v_pk_max_i16 v237, v237, 0\n\t"
      "v_pk_max_i16 v238, v238, 0\n\t"
      "v_mfma_f32_16x16x32_bf16 v[140:143], a[16:19], a[76:79], v[140:143]\n\t"
      "s_waitcnt lgkmcnt(6)\n\t"
      "v_pk_max_i16 v239, v239, 0\n\t"
      "v_mfma_f32_16x16x32_bf16 v[156:159], a[20:23], a[76:79], v[156:159]\n\t"
      "ds_read_b64_tr_b16 a[4:5], v7 offset:30720\n\t"
      "v_mul_f32 v96, v96, |v96|\n\t"
      "buffer_store_dwordx4 v[232:235], v29, s[76:79], s31 offen offset:0 sc1\n\t"
      "v_mfma_f32_16x16x32_bf16 v[152:155], a[20:23], a[72:75], v[152:155]\n\t"
      "ds_read_b64_tr_b16 a[6:7], v7 offset:37888\n\t"
      "v_mul_f32 v97, v97, |v97|\n\t"
      "v_mfma_f32_16x16x32_bf16 v[148:151], a[20:23], a[68:71], v[148:151]\n\t"
      "v_mul_f32 v98, v98, |v98|\n\t"
      "v_mul_f32 v99, v99, |v99|\n\t"
      "v_mfma_f32_16x16x32_bf16 v[144:147], a[20:23], a[64:67], v[144:147]\n\t"
      "s_waitcnt lgkmcnt(6)\n\t"
      "v_mul_f32 v112, v112, |v112|\n\t"
      "v_mul_f32 v113, v113, |v113|\n\t"
      "v_mfma_f32_16x16x32_bf16 v[128:131], a[24:27], a[80:83], v[128:131]\n\t"
      "ds_read_b64_tr_b16 a[8:9], v6 offset:45056\n\t"
      "v_mul_f32 v114, v114, |v114|\n\t"
      "v_mfma_f32_16x16x32_bf16 v[132:135], a[24:27], a[84:87], v[132:135]\n\t"
      "ds_read_b64_tr_b16 a[10:11], v6 offset:52224\n\t"
      "v_mul_f32 v115, v115, |v115|\n\t"
      "v_mfma_f32_16x16x32_bf16 v[136:139], a[24:27], a[88:91], v[136:139]\n\t"
      "v_cvt_pk_bf16_f32 v96, v96, v97\n\t"
      "v_cvt_pk_bf16_f32 v97, v98, v99\n\t"
      "v_mfma_f32_16x16x32_bf16 v[140:143], a[24:27], a[92:95], v[140:143]\n\t"
      "s_waitcnt lgkmcnt(6)\n\t"
      "v_cvt_pk_bf16_f32 v98, v112, v113\n\t"
      "v_cvt_pk_bf16_f32 v99, v114, v115\n\t"
      "v_mfma_f32_16x16x32_bf16 v[156:159], a[28:31], a[92:95], v[156:159]\n\t"
      "ds_read_b64_tr_b16 a[12:13], v7 offset:45056\n\t"
      "v_pk_max_i16 v96, v96, 0\n\t"
      "v_mfma_f32_16x16x32_bf16 v[152:155], a[28:31], a[88:91], v[152:155]\n\t"
      "ds_read_b64_tr_b16 a[14:15], v7 offset:52224\n\t"
      "buffer_load_dwordx4 a[48:51], v25, s[88:91], 0 offen offset:0\n\t"
      "v_mfma_f32_16x16x32_bf16 v[148:151], a[28:31], a[84:87], v[148:151]\n\t"
      "v_pk_max_i16 v97, v97, 0\n\t"
      "v_pk_max_i16 v98, v98, 0\n\t"
      "v_mfma_f32_16x16x32_bf16 v[144:147], a[28:31], a[80:83], v[144:147]\n\t"
      "s_waitcnt lgkmcnt(6)\n\t"
      "v_pk_max_i16 v99, v99, 0\n\t"
      "v_mul_f32 v100, v100, |v100|\n\t"
      "v_mfma_f32_16x16x32_bf16 v[128:131], a[0:3], a[96:99], v[128:131]\n\t"
      "ds_read_b64_tr_b16 a[16:17], v14 offset:2048\n\t"
      "v_mul_f32 v101, v101, |v101|\n\t"
      "v_mfma_f32_16x16x32_bf16 v[132:135], a[0:3], a[100:103], v[132:135]\n\t"
      "ds_read_b64_tr_b16 a[18:19], v14 offset:9216\n\t"
      "v_mul_f32 v102, v102, |v102|\n\t"
      "v_mfma_f32_16x16x32_bf16 v[136:139], a[0:3], a[104:107], v[136:139]\n\t"
      "v_mul_f32 v103, v103, |v103|\n\t"
      "v_mul_f32 v116, v116, |v116|\n\t"
      "v_mfma_f32_16x16x32_bf16 v[140:143], a[0:3], a[108:111], v[140:143]\n\t"
      "s_waitcnt lgkmcnt(6)\n\t"
      "v_mul_f32 v117, v117, |v117|\n\t"
      "v_mul_f32 v118, v118, |v118|\n\t"
      "buffer_store_dwordx4 v[236:239], v29, s[76:79], s31 offen offset:1024 sc1\n\t"
      "v_mfma_f32_16x16x32_bf16 v[156:159], a[4:7], a[108:111], v[156:159]\n\t"
      "ds_read_b64_tr_b16 a[20:21], v15 offset:2048\n\t"
      "v_mul_f32 v119, v119, |v119|\n\t"
      "v_mfma_f32_16x16x32_bf16 v[152:155], a[4:7], a[104:107], v[152:155]\n\t"
      "ds_read_b64_tr_b16 a[22:23], v15 offset:9216\n\t"
      "v_cvt_pk_bf16_f32 v100, v100, v101\n\t"
      "v_mfma_f32_16x16x32_bf16 v[148:151], a[4:7], a[100:103], v[148:151]\n\t"
      "v_cvt_pk_bf16_f32 v101, v102, v103\n\t"
      "v_cvt_pk_bf16_f32 v102, v116, v117\n\t"
      "v_mfma_f32_16x16x32_bf16 v[144:147], a[4:7], a[96:99], v[144:147]\n\t"
      "s_waitcnt lgkmcnt(6)\n\t"
      "v_cvt_pk_bf16_f32 v103, v118, v119\n\t"
      "v_pk_max_i16 v100, v100, 0\n\t"
      "v_mfma_f32_16x16x32_bf16 v[128:131], a[8:11], a[112:115], v[128:131]\n\t"
      "ds_read_b64_tr_b16 a[24:25], v14 offset:16384\n\t"
      "v_pk_max_i16 v101, v101, 0\n\t"
      "v_mfma_f32_16x16x32_bf16 v[132:135], a[8:11], a[116:119], v[132:135]\n\t"
      "ds_read_b64_tr_b16 a[26:27], v14 offset:23552\n\t"
      "v_pk_max_i16 v102, v102, 0\n\t"
      "v_mfma_f32_16x16x32_bf16 v[136:139], a[8:11], a[120:123], v[136:139]\n\t"
      "v_pk_max_i16 v103, v103, 0\n\t"
      "v_mul_f32 v104, v104, |v104|\n\t"
      "v_mfma_f32_16x16x32_bf16 v[140:143], a[8:11], a[124:127], v[140:143]\n\t"
      "s_waitcnt lgkmcnt(6)\n\t"
      "v_mul_f32 v105, v105, |v105|\n\t"
      "v_mul_f32 v106, v106, |v106|\n\t"
      "v_mfma_f32_16x16x32_bf16 v[156:159], a[12:15], a[124:127], v[156:159]\n\t"
      "ds_read_b64_tr_b16 a[28:29], v15 offset:16384\n\t"
      "buffer_load_dwordx4 a[52:55], v25, s[88:91], 0 offen offset:64\n\t"
      "v_mfma_f32_16x16x32_bf16 v[152:155], a[12:15], a[120:123], v[152:155]\n\t"
      "ds_read_b64_tr_b16 a[30:31], v15 offset:23552\n\t"
      "v_mul_f32 v107, v107, |v107|\n\t"
      "v_mfma_f32_16x16x32_bf16 v[148:151], a[12:15], a[116:119], v[148:151]\n\t"
      "v_mul_f32 v120, v120, |v120|\n\t"
      "v_mul_f32 v121, v121, |v121|\n\t"
      "v_mfma_f32_16x16x32_bf16 v[144:147], a[12:15], a[112:115], v[144:147]\n\t"
      "s_waitcnt lgkmcnt(6)\n\t"
      "v_mul_f32 v122, v122, |v122|\n\t"
      "v_mul_f32 v123, v123, |v123|\n\t"
      "v_mfma_f32_16x16x32_bf16 v[128:131], a[16:19], a[128:131], v[128:131]\n\t"
      "ds_read_b64_tr_b16 a[0:1], v4 offset:4096\n\t"
      "v_cvt_pk_bf16_f32 v104, v104, v105\n\t"
      "v_mfma_f32_16x16x32_bf16 v[132:135], a[16:19], a[132:135], v[132:135]\n\t"
      "ds_read_b64_tr_b16 a[2:3], v4 offset:11264\n\t"
      "v_cvt_pk_bf16_f32 v105, v106, v107\n\t"
      "v_mfma_f32_16x16x32_bf16 v[136:139], a[16:19], a[136:139], v[136:139]\n\t"
      "v_cvt_pk_bf16_f32 v106, v120, v121\n\t"
      "v_cvt_pk_bf16_f32 v107, v122, v123\n\t"
      "s_add_u32 s30, s30, 1966080\n\t"
      "buffer_store_dwordx4 v[96:99], v22, s[76:79], s30 offen offset:0 sc1\n\t"
      "v_mfma_f32_16x16x32_bf16 v[140:143], a[16:19], a[140:143], v[140:143]\n\t"
      "s_waitcnt lgkmcnt(6)\n\t"
      "v_pk_max_i16 v104, v104, 0\n\t"
      "v_pk_max_i16 v105, v105, 0\n\t"
      "v_mfma_f32_16x16x32_bf16 v[156:159], a[20:23], a[140:143], v[156:159]\n\t"
      "ds_read_b64_tr_b16 a[4:5], v5 offset:4096\n\t"
      "v_pk_max_i16 v106, v106, 0\n\t"
      "v_mfma_f32_16x16x32_bf16 v[152:155], a[20:23], a[136:139], v[152:155]\n\t"
      "ds_read_b64_tr_b16 a[6:7], v5 offset:11264\n\t"
      "v_pk_max_i16 v107, v107, 0\n\t"
      "v_mfma_f32_16x16x32_bf16 v[148:151], a[20:23], a[132:135], v[148:151]\n\t"
      "v_mul_f32 v108, v108, |v108|\n\t"
      "v_mul_f32 v109, v109, |v109|\n\t"
      "v_mfma_f32_16x16x32_bf16 v[144:147], a[20:23], a[128:131], v[144:147]\n\t"
      "s_waitcnt lgkmcnt(6)\n\t"
      "v_mul_f32 v110, v110, |v110|\n\t"
      "v_mul_f32 v111, v111, |v111|\n\t"
      "v_mfma_f32_16x16x32_bf16 v[128:131], a[24:27], a[144:147], v[128:131]\n\t"
      "ds_read_b64_tr_b16 a[8:9], v4 offset:18432\n\t"
      "v_mul_f32 v124, v124, |v124|\n\t"
      "v_mfma_f32_16x16x32_bf16 v[132:135], a[24:27], a[148:151], v[132:135]\n\t"
      "ds_read_b64_tr_b16 a[10:11], v4 offset:25600\n\t"
      "v_mul_f32 v125, v125, |v125|\n\t"
      "v_mfma_f32_16x16x32_bf16 v[136:139], a[24:27], a[152:155], v[136:139]\n\t"
      "v_mul_f32 v126, v126, |v126|\n\t"
      "v_mul_f32 v127, v127, |v127|\n\t"
      "v_mfma_f32_16x16x32_bf16 v[140:143], a[24:27], a[156:159], v[140:143]\n\t"
      "s_waitcnt lgkmcnt(6)\n\t"
      "v_cvt_pk_bf16_f32 v108, v108, v109\n\t"
      "v_cvt_pk_bf16_f32 v109, v110, v111\n\t"
      "v_mfma_f32_16x16x32_bf16 v[156:159], a[28:31], a[156:159], v[156:159]\n\t"
      "ds_read_b64_tr_b16 a[12:13], v5 offset:18432\n\t"
      "v_cvt_pk_bf16_f32 v110, v124, v125\n\t"
      "v_mfma_f32_16x16x32_bf16 v[152:155], a[28:31], a[152:155], v[152:155]\n\t"
      "ds_read_b64_tr_b16 a[14:15], v5 offset:25600\n\t"
      "v_cvt_pk_bf16_f32 v111, v126, v127\n\t"
      "v_mfma_f32_16x16x32_bf16 v[148:151], a[28:31], a[148:151], v[148:151]\n\t"
      "s_lshl_b32 s3, s32, 12\n\t"
      "s_add_u32 s3, s3, 147456\n\t"
      "s_add_u32 m0, s3, 0\n\t"
      "s_add_u32 s5, s70, 0\n\t"
      "buffer_load_dwordx4 v17, s[72:75], s5 offen lds\n\t"
      "v_mfma_f32_16x16x32_bf16 v[144:147], a[28:31], a[144:147], v[144:147]\n\t"
      "s_waitcnt lgkmcnt(6)\n\t"
      "v_pk_max_i16 v108, v108, 0\n\t"
      "v_pk_max_i16 v109, v109, 0\n\t"
      "v_mfma_f32_16x16x32_bf16 v[160:163], a[0:3], a[64:67], v[160:163]\n\t"
      "ds_read_b64_tr_b16 a[16:17], v4 offset:32768\n\t"
      "buffer_load_dwordx2 v[2:3], v27, s[96:99], s16 offen offset:2048 sc1\n\t"
      "v_mfma_f32_16x16x32_bf16 v[164:167], a[0:3], a[68:71], v[164:167]\n\t"
      "ds_read_b64_tr_b16 a[18:19], v4 offset:39936\n\t"
      "v_pk_max_i16 v110, v110, 0\n\t"
      "buffer_store_dwordx4 v[100:103], v22, s[76:79], s30 offen offset:1024 sc1\n\t"
      "v_mfma_f32_16x16x32_bf16 v[168:171], a[0:3], a[72:75], v[168:171]\n\t"
      "v_pk_max_i16 v111, v111, 0\n\t"
      "v_mfma_f32_16x16x32_bf16 v[172:175], a[0:3], a[76:79], v[172:175]\n\t"
      "s_waitcnt lgkmcnt(6)\n\t"
      "v_mul_f32 v128, v128, |v128|\n\t"
      "v_mul_f32 v129, v129, |v129|\n\t"
      "v_mfma_f32_16x16x32_bf16 v[188:191], a[4:7], a[76:79], v[188:191]\n\t"
      "ds_read_b64_tr_b16 a[20:21], v5 offset:32768\n\t"
      "v_mul_f32 v130, v130, |v130|\n\t"
      "v_mfma_f32_16x16x32_bf16 v[184:187], a[4:7], a[72:75], v[184:187]\n\t"
      "ds_read_b64_tr_b16 a[22:23], v5 offset:39936\n\t"
      "v_mul_f32 v131, v131, |v131|\n\t"
      "v_mfma_f32_16x16x32_bf16 v[180:183], a[4:7], a[68:71], v[180:183]\n\t"
      "v_mul_f32 v144, v144, |v144|\n\t"
      "v_mul_f32 v145, v145, |v145|\n\t"
      "v_mfma_f32_16x16x32_bf16 v[176:179], a[4:7], a[64:67], v[176:179]\n\t"
      "s_waitcnt lgkmcnt(6)\n\t"
      "v_mul_f32 v146, v146, |v146|\n\t"
      "v_mul_f32 v147, v147, |v147|\n\t"
      "v_mfma_f32_16x16x32_bf16 v[160:163], a[8:11], a[80:83], v[160:163]\n\t"
      "ds_read_b64_tr_b16 a[24:25], v4 offset:47104\n\t"
      "v_cvt_pk_bf16_f32 v128, v128, v129\n\t"
      "v_mfma_f32_16x16x32_bf16 v[164:167], a[8:11], a[84:87], v[164:167]\n\t"
      "ds_read_b64_tr_b16 a[26:27], v4 offset:54272\n\t"
      "v_cvt_pk_bf16_f32 v129, v130, v131\n\t"
      "v_mfma_f32_16x16x32_bf16 v[168:171], a[8:11], a[88:91], v[168:171]\n\t"
      "v_cvt_pk_bf16_f32 v130, v144, v145\n\t"
      "v_cvt_pk_bf16_f32 v131, v146, v147\n\t"
      "v_mfma_f32_16x16x32_bf16 v[172:175], a[8:11], a[92:95], v[172:175]\n\t"
      "s_waitcnt lgkmcnt(6)\n\t"
      "v_pk_max_i16 v128, v128, 0\n\t"
      "v_pk_max_i16 v129, v129, 0\n\t"
      "v_mfma_f32_16x16x32_bf16 v[188:191], a[12:15], a[92:95], v[188:191]\n\t"
      "ds_read_b64_tr_b16 a[28:29], v5 offset:47104\n\t"
      "v_pk_max_i16 v130, v130, 0\n\t"
      "v_mfma_f32_16x16x32_bf16 v[184:187], a[12:15], a[88:91], v[184:187]\n\t"
      "ds_read_b64_tr_b16 a[30:31], v5 offset:54272\n\t"
      "v_pk_max_i16 v131, v131, 0\n\t"
      "v_mfma_f32_16x16x32_bf16 v[180:183], a[12:15], a[84:87], v[180:183]\n\t"
      "s_add_u32 m0, s3, 1024\n\t"
      "s_add_u32 s5, s70, 16384\n\t"
      "buffer_load_dwordx4 v17, s[72:75], s5 offen lds\n\t"
      "v_mfma_f32_16x16x32_bf16 v[176:179], a[12:15], a[80:83], v[176:179]\n\t"
      "s_waitcnt lgkmcnt(6)\n\t"
      "v_mul_f32 v132, v132, |v132|\n\t"
      "v_mul_f32 v133, v133, |v133|\n\t"
      "v_mfma_f32_16x16x32_bf16 v[160:163], a[16:19], a[96:99], v[160:163]\n\t"
      "ds_read_b64_tr_b16 a[0:1], v11 offset:4096\n\t"
      "v_mul_f32 v134, v134, |v134|\n\t"
      "buffer_store_dwordx4 v[104:107], v29, s[76:79], s30 offen offset:0 sc1\n\t"
      "v_mfma_f32_16x16x32_bf16 v[164:167], a[16:19], a[100:103], v[164:167]\n\t"
      "ds_read_b64_tr_b16 a[2:3], v11 offset:11264\n\t"
      "v_mul_f32 v135, v135, |v135|\n\t"
      "v_mfma_f32_16x16x32_bf16 v[168:171], a[16:19], a[104:107], v[168:171]\n\t"
      "v_mul_f32 v148, v148, |v148|\n\t"
      "v_mul_f32 v149, v149, |v149|\n\t"
      "v_mfma_f32_16x16x32_bf16 v[172:175], a[16:19], a[108:111], v[172:175]\n\t"
      "s_waitcnt lgkmcnt(6)\n\t"
      "v_mul_f32 v150, v150, |v150|\n\t"
      "v_mul_f32 v151, v151, |v151|\n\t"
      "v_mfma_f32_16x16x32_bf16 v[188:191], a[20:23], a[108:111], v[188:191]\n\t"
      "ds_read_b64_tr_b16 a[4:5], v13 offset:4096\n\t"
      "v_cvt_pk_bf16_f32 v132, v132, v133\n\t"
      "v_mfma_f32_16x16x32_bf16 v[184:187], a[20:23], a[104:107], v[184:187]\n\t"
      "ds_read_b64_tr_b16 a[6:7], v13 offset:11264\n\t"
      "v_cvt_pk_bf16_f32 v133, v134, v135\n\t"
      "v_mfma_f32_16x16x32_bf16 v[180:183], a[20:23], a[100:103], v[180:183]\n\t"
      "v_cvt_pk_bf16_f32 v134, v148, v149\n\t"
      "v_cvt_pk_bf16_f32 v135, v150, v151\n\t"
      "v_mfma_f32_16x16x32_bf16 v[176:179], a[20:23], a[96:99], v[176:179]\n\t"
      "s_waitcnt lgkmcnt(6)\n\t"
      "v_pk_max_i16 v132, v132, 0\n\t"
      "v_pk_max_i16 v133, v133, 0\n\t"
      "v_mfma_f32_16x16x32_bf16 v[160:163], a[24:27], a[112:115], v[160:163]\n\t"
      "ds_read_b64_tr_b16 a[8:9], v11 offset:18432\n\t"
      "v_pk_max_i16 v134, v134, 0\n\t"
      "v_mfma_f32_16x16x32_bf16 v[164:167], a[24:27], a[116:119], v[164:167]\n\t"
      "ds_read_b64_tr_b16 a[10:11], v11 offset:25600\n\t"
      "v_pk_max_i16 v135, v135, 0\n\t"
      "v_mfma_f32_16x16x32_bf16 v[168:171], a[24:27], a[120:123], v[168:171]\n\t"
      "v_mul_f32 v136, v136, |v136|\n\t"
      "v_mul_f32 v137, v137, |v137|\n\t"
      "v_mfma_f32_16x16x32_bf16 v[172:175], a[24:27], a[124:127], v[172:175]\n\t"
      "s_waitcnt lgkmcnt(6)\n\t"
      "v_mul_f32 v138, v138, |v138|\n\t"
      "v_mul_f32 v139, v139, |v139|\n\t"
      "v_mfma_f32_16x16x32_bf16 v[188:191], a[28:31], a[124:127], v[188:191]\n\t"
      "ds_read_b64_tr_b16 a[12:13], v13 offset:18432\n\t"
      "v_mul_f32 v152, v152, |v152|\n\t"
      "v_mfma_f32_16x16x32_bf16 v[184:187], a[28:31], a[120:123], v[184:187]\n\t"
      "ds_read_b64_tr_b16 a[14:15], v13 offset:25600\n\t"
      "v_mul_f32 v153, v153, |v153|\n\t"
      "v_mfma_f32_16x16x32_bf16 v[180:183], a[28:31], a[116:119], v[180:183]\n\t"
      "s_add_u32 m0, s3, 2048\n\t"
      "s_add_u32 s5, s70, 32768\n\t"
      "buffer_load_dwordx4 v17, s[72:75], s5 offen lds\n\t"
      "v_mfma_f32_16x16x32_bf16 v[176:179], a[28:31], a[112:115], v[176:179]\n\t"
      "s_waitcnt lgkmcnt(6)\n\t"
      "v_mul_f32 v154, v154, |v154|\n\t"
      "v_mul_f32 v155, v155, |v155|\n\t"
      "buffer_store_dwordx4 v[108:111], v29, s[76:79], s30 offen offset:1024 sc1\n\t"
      "v_mfma_f32_16x16x32_bf16 v[160:163], a[0:3], a[128:131], v[160:163]\n\t"
      "ds_read_b64_tr_b16 a[16:17], v6 offset:4096\n\t"
      "v_cvt_pk_bf16_f32 v136, v136, v137\n\t"
      "v_mfma_f32_16x16x32_bf16 v[164:167], a[0:3], a[132:135], v[164:167]\n\t"
      "ds_read_b64_tr_b16 a[18:19], v6 offset:11264\n\t"
      "v_cvt_pk_bf16_f32 v137, v138, v139\n\t"
      "v_mfma_f32_16x16x32_bf16 v[168:171], a[0:3], a[136:139], v[168:171]\n\t"
      "v_cvt_pk_bf16_f32 v138, v152, v153\n\t"
      "v_cvt_pk_bf16_f32 v139, v154, v155\n\t"
      "v_mfma_f32_16x16x32_bf16 v[172:175], a[0:3], a[140:143], v[172:175]\n\t"
      "s_waitcnt lgkmcnt(6)\n\t"
      "v_pk_max_i16 v136, v136, 0\n\t"
      "v_pk_max_i16 v137, v137, 0\n\t"
      "v_mfma_f32_16x16x32_bf16 v[188:191], a[4:7], a[140:143], v[188:191]\n\t"
      "ds_read_b64_tr_b16 a[20:21], v7 offset:4096\n\t"
      "v_pk_max_i16 v138, v138, 0\n\t"
      "v_mfma_f32_16x16x32_bf16 v[184:187], a[4:7], a[136:139], v[184:187]\n\t"
      "ds_read_b64_tr_b16 a[22:23], v7 offset:11264\n\t"
      "v_pk_max_i16 v139, v139, 0\n\t"
      "v_mfma_f32_16x16x32_bf16 v[180:183], a[4:7], a[132:135], v[180:183]\n\t"
      "v_mul_f32 v140, v140, |v140|\n\t"
      "v_mul_f32 v141, v141, |v141|\n\t"
      "v_mfma_f32_16x16x32_bf16 v[176:179], a[4:7], a[128:131], v[176:179]\n\t"
      "s_waitcnt lgkmcnt(6)\n\t"
      "v_mul_f32 v142, v142, |v142|\n\t"
      "v_mul_f32 v143, v143, |v143|\n\t"
      "v_mfma_f32_16x16x32_bf16 v[160:163], a[8:11], a[144:147], v[160:163]\n\t"
      "ds_read_b64_tr_b16 a[24:25], v6 offset:18432\n\t"
      "v_mul_f32 v156, v156, |v156|\n\t"
      "v_mfma_f32_16x16x32_bf16 v[164:167], a[8:11], a[148:151], v[164:167]\n\t"
      "ds_read_b64_tr_b16 a[26:27], v6 offset:25600\n\t"
      "v_mul_f32 v157, v157, |v157|\n\t"
      "v_mfma_f32_16x16x32_bf16 v[168:171], a[8:11], a[152:155], v[168:171]\n\t"
      "v_mul_f32 v158, v158, |v158|\n\t"
      "v_mul_f32 v159, v159, |v159|\n\t"
      "v_mfma_f32_16x16x32_bf16 v[172:175], a[8:11], a[156:159], v[172:175]\n\t"
      "s_waitcnt lgkmcnt(6)\n\t"
      "v_cvt_pk_bf16_f32 v140, v140, v141\n\t"
      "v_cvt_pk_bf16_f32 v141, v142, v143\n\t"
      "v_mfma_f32_16x16x32_bf16 v[188:191], a[12:15], a[156:159], v[188:191]\n\t"
      "ds_read_b64_tr_b16 a[28:29], v7 offset:18432\n\t"
      "v_cvt_pk_bf16_f32 v142, v156, v157\n\t"
      "v_mfma_f32_16x16x32_bf16 v[184:187], a[12:15], a[152:155], v[184:187]\n\t"
      "ds_read_b64_tr_b16 a[30:31], v7 offset:25600\n\t"
      "v_cvt_pk_bf16_f32 v143, v158, v159\n\t"
      "v_mfma_f32_16x16x32_bf16 v[180:183], a[12:15], a[148:151], v[180:183]\n\t"
      "s_add_u32 m0, s3, 3072\n\t"
      "s_add_u32 s5, s70, 49152\n\t"
      "buffer_load_dwordx4 v17, s[72:75], s5 offen lds\n\t"
      "s_add_u32 s30, s30, 131072\n\t"
      "v_mfma_f32_16x16x32_bf16 v[176:179], a[12:15], a[144:147], v[176:179]\n\t"
      "s_waitcnt lgkmcnt(6)\n\t"
      "v_pk_max_i16 v140, v140, 0\n\t"
      "v_pk_max_i16 v141, v141, 0\n\t"
      "buffer_store_dwordx4 v[128:131], v22, s[76:79], s30 offen offset:0 sc1\n\t"
      "v_mfma_f32_16x16x32_bf16 v[192:195], a[16:19], a[64:67], v[192:195]\n\t"
      "ds_read_b64_tr_b16 a[0:1], v6 offset:32768\n\t"
      "v_pk_max_i16 v142, v142, 0\n\t"
      "v_mfma_f32_16x16x32_bf16 v[196:199], a[16:19], a[68:71], v[196:199]\n\t"
      "ds_read_b64_tr_b16 a[2:3], v6 offset:39936\n\t"
      "v_pk_max_i16 v143, v143, 0\n\t"
      "v_mfma_f32_16x16x32_bf16 v[200:203], a[16:19], a[72:75], v[200:203]\n\t"
      "v_mul_f32 v160, v160, |v160|\n\t"
      "v_mul_f32 v161, v161, |v161|\n\t"
      "v_mfma_f32_16x16x32_bf16 v[204:207], a[16:19], a[76:79], v[204:207]\n\t"
      "s_waitcnt lgkmcnt(6)\n\t"
      "v_mul_f32 v162, v162, |v162|\n\t"
      "v_mul_f32 v163, v163, |v163|\n\t"
      "v_mfma_f32_16x16x32_bf16 v[220:223], a[20:23], a[76:79], v[220:223]\n\t"
      "ds_read_b64_tr_b16 a[4:5], v7 offset:32768\n\t"
      "v_mul_f32 v176, v176, |v176|\n\t"
      "v_mfma_f32_16x16x32_bf16 v[216:219], a[20:23], a[72:75], v[216:219]\n\t"
      "ds_read_b64_tr_b16 a[6:7], v7 offset:39936\n\t"
      "v_mul_f32 v177, v177, |v177|\n\t"
      "v_mfma_f32_16x16x32_bf16 v[212:215], a[20:23], a[68:71], v[212:215]\n\t"
      "v_mul_f32 v178, v178, |v178|\n\t"
      "v_mul_f32 v179, v179, |v179|\n\t"
      "v_mfma_f32_16x16x32_bf16 v[208:211], a[20:23], a[64:67], v[208:211]\n\t"
      "s_waitcnt lgkmcnt(6)\n\t"
      "v_cvt_pk_bf16_f32 v160, v160, v161\n\t"
      "v_cvt_pk_bf16_f32 v161, v162, v163\n\t"
      "v_mfma_f32_16x16x32_bf16 v[192:195], a[24:27], a[80:83], v[192:195]\n\t"
      "ds_read_b64_tr_b16 a[8:9], v6 offset:47104\n\t"
      "v_cvt_pk_bf16_f32 v162, v176, v177\n\t"
      "v_mfma_f32_16x16x32_bf16 v[196:199], a[24:27], a[84:87], v[196:199]\n\t"
      "ds_read_b64_tr_b16 a[10:11], v6 offset:54272\n\t"
      "v_cvt_pk_bf16_f32 v163, v178, v179\n\t"
      "v_mfma_f32_16x16x32_bf16 v[200:203], a[24:27], a[88:91], v[200:203]\n\t"
      "v_pk_max_i16 v160, v160, 0\n\t"
      "v_pk_max_i16 v161, v161, 0\n\t"
      "v_mfma_f32_16x16x32_bf16 v[204:207], a[24:27], a[92:95], v[204:207]\n\t"
      "s_waitcnt lgkmcnt(6)\n\t"
      "v_pk_max_i16 v162, v162, 0\n\t"
      "v_pk_max_i16 v163, v163, 0\n\t"
      "v_mfma_f32_16x16x32_bf16 v[220:223], a[28:31], a[92:95], v[220:223]\n\t"
      "ds_read_b64_tr_b16 a[12:13], v7 offset:47104\n\t"
      "v_mul_f32 v164, v164, |v164|\n\t"
      "v_mfma_f32_16x16x32_bf16 v[216:219], a[28:31], a[88:91], v[216:219]\n\t"
      "ds_read_b64_tr_b16 a[14:15], v7 offset:54272\n\t"
      "v_mul_f32 v165, v165, |v165|\n\t"
      "v_mfma_f32_16x16x32_bf16 v[212:215], a[28:31], a[84:87], v[212:215]\n\t"
      "v_mul_f32 v166, v166, |v166|\n\t"
      "v_mul_f32 v167, v167, |v167|\n\t"
      "buffer_store_dwordx4 v[132:135], v22, s[76:79], s30 offen offset:1024 sc1\n\t"
      "v_mfma_f32_16x16x32_bf16 v[208:211], a[28:31], a[80:83], v[208:211]\n\t"
      "s_waitcnt lgkmcnt(6)\n\t"
      "v_mul_f32 v180, v180, |v180|\n\t"
      "v_mul_f32 v181, v181, |v181|\n\t"
      "v_mfma_f32_16x16x32_bf16 v[192:195], a[0:3], a[96:99], v[192:195]\n\t"
      "ds_read_b64_tr_b16 a[16:17], v14 offset:4096\n\t"
      "v_mul_f32 v182, v182, |v182|\n\t"
      "v_mfma_f32_16x16x32_bf16 v[196:199], a[0:3], a[100:103], v[196:199]\n\t"
      "ds_read_b64_tr_b16 a[18:19], v14 offset:11264\n\t"
      "v_mul_f32 v183, v183, |v183|\n\t"
      "v_mfma_f32_16x16x32_bf16 v[200:203], a[0:3], a[104:107], v[200:203]\n\t"
      "v_cvt_pk_bf16_f32 v164, v164, v165\n\t"
      "v_cvt_pk_bf16_f32 v165, v166, v167\n\t"
      "v_mfma_f32_16x16x32_bf16 v[204:207], a[0:3], a[108:111], v[204:207]\n\t"
      "s_waitcnt lgkmcnt(6)\n\t"
      "v_cvt_pk_bf16_f32 v166, v180, v181\n\t"
      "v_cvt_pk_bf16_f32 v167, v182, v183\n\t"
      "v_mfma_f32_16x16x32_bf16 v[220:223], a[4:7], a[108:111], v[220:223]\n\t"
      "ds_read_b64_tr_b16 a[20:21], v15 offset:4096\n\t"
      "v_pk_max_i16 v164, v164, 0\n\t"
      "v_mfma_f32_16x16x32_bf16 v[216:219], a[4:7], a[104:107], v[216:219]\n\t"
      "ds_read_b64_tr_b16 a[22:23], v15 offset:11264\n\t"
      "v_pk_max_i16 v165, v165, 0\n\t"
      "v_mfma_f32_16x16x32_bf16 v[212:215], a[4:7], a[100:103], v[212:215]\n\t"
      "v_pk_max_i16 v166, v166, 0\n\t"
      "v_pk_max_i16 v167, v167, 0\n\t"
      "v_mfma_f32_16x16x32_bf16 v[208:211], a[4:7], a[96:99], v[208:211]\n\t"
      "s_waitcnt lgkmcnt(6)\n\t"
      "v_mul_f32 v168, v168, |v168|\n\t"
      "v_mul_f32 v169, v169, |v169|\n\t"
      "v_mfma_f32_16x16x32_bf16 v[192:195], a[8:11], a[112:115], v[192:195]\n\t"
      "ds_read_b64_tr_b16 a[24:25], v14 offset:18432\n\t"
      "v_mul_f32 v170, v170, |v170|\n\t"
      "v_mfma_f32_16x16x32_bf16 v[196:199], a[8:11], a[116:119], v[196:199]\n\t"
      "ds_read_b64_tr_b16 a[26:27], v14 offset:25600\n\t"
      "v_mul_f32 v171, v171, |v171|\n\t"
      "v_mfma_f32_16x16x32_bf16 v[200:203], a[8:11], a[120:123], v[200:203]\n\t"
      "v_mul_f32 v184, v184, |v184|\n\t"
      "v_mul_f32 v185, v185, |v185|\n\t"
      "v_mfma_f32_16x16x32_bf16 v[204:207], a[8:11], a[124:127], v[204:207]\n\t"
      "s_waitcnt lgkmcnt(6)\n\t"
      "v_mul_f32 v186, v186, |v186|\n\t"
      "v_mul_f32 v187, v187, |v187|\n\t"
      "v_mfma_f32_16x16x32_bf16 v[220:223], a[12:15], a[124:127], v[220:223]\n\t"
      "ds_read_b64_tr_b16 a[28:29], v15 offset:18432\n\t"
      "v_cvt_pk_bf16_f32 v168, v168, v169\n\t"
      "v_mfma_f32_16x16x32_bf16 v[216:219], a[12:15], a[120:123], v[216:219]\n\t"
      "ds_read_b64_tr_b16 a[30:31], v15 offset:25600\n\t"
      "v_cvt_pk_bf16_f32 v169, v170, v171\n\t"
      "buffer_store_dwordx4 v[136:139], v29, s[76:79], s30 offen offset:0 sc1\n\t"
      "v_mfma_f32_16x16x32_bf16 v[212:215], a[12:15], a[116:119], v[212:215]\n\t"
      "v_cvt_pk_bf16_f32 v170, v184, v185\n\t"
      "v_cvt_pk_bf16_f32 v171, v186, v187\n\t"
      "v_mfma_f32_16x16x32_bf16 v[208:211], a[12:15], a[112:115], v[208:211]\n\t"
      "s_waitcnt lgkmcnt(6)\n\t"
      "v_pk_max_i16 v168, v168, 0\n\t"
      "v_pk_max_i16 v169, v169, 0\n\t"
      "v_mfma_f32_16x16x32_bf16 v[192:195], a[16:19], a[128:131], v[192:195]\n\t"
      "v_pk_max_i16 v170, v170, 0\n\t"
      "v_pk_max_i16 v171, v171, 0\n\t"
      "v_mfma_f32_16x16x32_bf16 v[196:199], a[16:19], a[132:135], v[196:199]\n\t"
      "v_mul_f32 v172, v172, |v172|\n\t"
      "v_mul_f32 v173, v173, |v173|\n\t"
      "v_mfma_f32_16x16x32_bf16 v[200:203], a[16:19], a[136:139], v[200:203]\n\t"
      "v_mul_f32 v174, v174, |v174|\n\t"
      "v_mul_f32 v175, v175, |v175|\n\t"
      "ds_read_b64_tr_b16 a[160:161], v18 offset:2048\n\t"
      "v_mfma_f32_16x16x32_bf16 v[204:207], a[16:19], a[140:143], v[204:207]\n\t"
      "s_waitcnt lgkmcnt(5)\n\t"
      "v_mul_f32 v188, v188, |v188|\n\t"
      "v_mul_f32 v189, v189, |v189|\n\t"
      "ds_read_b64_tr_b16 a[162:163], v18 offset:6144\n\t"
      "v_mfma_f32_16x16x32_bf16 v[220:223], a[20:23], a[140:143], v[220:223]\n\t"
      "v_mul_f32 v190, v190, |v190|\n\t"
      "v_mul_f32 v191, v191, |v191|\n\t"
      "ds_read_b64_tr_b16 a[164:165], v19 offset:2048\n\t"
      "v_mfma_f32_16x16x32_bf16 v[216:219], a[20:23], a[136:139], v[216:219]\n\t"
      "v_cvt_pk_bf16_f32 v172, v172, v173\n\t"
      "v_cvt_pk_bf16_f32 v173, v174, v175\n\t"
      "ds_read_b64_tr_b16 a[166:167], v19 offset:6144\n\t"
      "v_mfma_f32_16x16x32_bf16 v[212:215], a[20:23], a[132:135], v[212:215]\n\t"
      "v_cvt_pk_bf16_f32 v174, v188, v189\n\t"
      "v_cvt_pk_bf16_f32 v175, v190, v191\n\t"
      "ds_read_b64_tr_b16 a[168:169], v20 offset:2048\n\t"
      "v_mfma_f32_16x16x32_bf16 v[208:211], a[20:23], a[128:131], v[208:211]\n\t"
      "s_waitcnt lgkmcnt(7)\n\t"
      "v_pk_max_i16 v172, v172, 0\n\t"
      "v_pk_max_i16 v173, v173, 0\n\t"
      "ds_read_b64_tr_b16 a[170:171], v20 offset:6144\n\t"
      "v_mfma_f32_16x16x32_bf16 v[192:195], a[24:27], a[144:147], v[192:195]\n\t"
      "v_pk_max_i16 v174, v174, 0\n\t"
      "v_pk_max_i16 v175, v175, 0\n\t"
      "ds_read_b64_tr_b16 a[172:173], v21 offset:2048\n\t"
      "v_mfma_f32_16x16x32_bf16 v[196:199], a[24:27], a[148:151], v[196:199]\n\t"
      "ds_read_b64_tr_b16 a[174:175], v21 offset:6144\n\t"
      "v_mfma_f32_16x16x32_bf16 v[200:203], a[24:27], a[152:155], v[200:203]\n\t"
      "v_mfma_f32_16x16x32_bf16 v[204:207], a[24:27], a[156:159], v[204:207]\n\t"
      "s_waitcnt lgkmcnt(8)\n\t"
      "v_mfma_f32_16x16x32_bf16 v[220:223], a[28:31], a[156:159], v[220:223]\n\t"
      "buffer_store_dwordx4 v[140:143], v29, s[76:79], s30 offen offset:1024 sc1\n\t"
      "v_mfma_f32_16x16x32_bf16 v[216:219], a[28:31], a[152:155], v[216:219]\n\t"
      "v_mfma_f32_16x16x32_bf16 v[212:215], a[28:31], a[148:151], v[212:215]\n\t"
      "v_mfma_f32_16x16x32_bf16 v[208:211], a[28:31], a[144:147], v[208:211]\n\t"
      "s_barrier\n\t"
      "s_mov_b32 m0, s24\n\t"
      "s_nop 0\n\t"
      "buffer_load_dwordx4 v1, s[36:39], s20 offen lds\n\t"
      "buffer_load_dwordx4 v1, s[36:39], s21 offen offset:1024 lds\n\t"
      "buffer_load_dwordx4 v1, s[36:39], s22 offen offset:2048 lds\n\t"
      "buffer_load_dwordx4 v1, s[36:39], s23 offen offset:3072 lds\n\t"
      "s_add_u32 s24, s24, 16384\n\t"
      "s_and_b32 s24, s24, 0x1ffff\n\t"
      "v_readlane_b32 s54, v23, 3\n\t"
      "s_bitcmp1_b32 s54, 0\n\t"
      "s_cselect_b64 s[36:37], s[80:81], s[84:85]\n\t"
      "s_cselect_b32 s38, s82, s86\n\t"
      "s_and_b32 s54, s54, -2\n\t"
      "v_add_u32 v1, s54, v17\n\t"
      STAMP(24, 25)
      "s_waitcnt lgkmcnt(6)\n\t"
      "v_mfma_f32_16x16x32_bf16 v[48:51], a[160:163], v[32:35], 0\n\t"
      "ds_read_b64_tr_b16 a[176:177], v18 offset:0\n\t"
      "v_mfma_f32_16x16x32_bf16 v[52:55], a[160:163], v[36:39], 0\n\t"
      "ds_read_b64_tr_b16 a[178:179], v18 offset:4096\n\t"
      "v_mul_f32 v204, v204, |v204|\n\t"
      "v_mul_f32 v205, v205, |v205|\n\t"
      "v_mul_f32 v206, v206, |v206|\n\t"
      "v_mfma_f32_16x16x32_bf16 v[56:59], a[160:163], v[40:43], 0\n\t"
      "v_mul_f32 v207, v207, |v207|\n\t"
      "v_mul_f32 v220, v220, |v220|\n\t"
      "v_mul_f32 v221, v221, |v221|\n\t"
      "v_mul_f32 v222, v222, |v222|\n\t"
      "v_mfma_f32_16x16x32_bf16 v[60:63], a[160:163], v[44:47], 0\n\t"
      "s_waitcnt lgkmcnt(6)\n\t"
      "s_mov_b32 m0, s24\n\t"
      "v_mul_f32 v223, v223, |v223|\n\t"
      "v_cvt_pk_bf16_f32 v204, v204, v205\n\t"
      "v_cvt_pk_bf16_f32 v205, v206, v207\n\t"
      "v_mfma_f32_16x16x32_bf16 v[92:95], a[164:167], v[44:47], 0\n\t"
      "ds_read_b64_tr_b16 a[180:181], v19 offset:0\n\t"
      "buffer_load_dwordx4 v1, s[36:39], s20 offen lds\n\t"
      "v_cvt_pk_bf16_f32 v206, v220, v221\n\t"
      LSTW(24, "")
      "v_cvt_pk_bf16_f32 v207, v222, v223\n\t"
      "v_mfma_f32_16x16x32_bf16 v[88:91], a[164:167], v[40:43], 0\n\t"
      "ds_read_b64_tr_b16 a[182:183], v19 offset:4096\n\t"
      "v_pk_max_i16 v204, v204, 0\n\t"
      "v_pk_max_i16 v205, v205, 0\n\t"
      "v_pk_max_i16 v206, v206, 0\n\t"
      "v_mfma_f32_16x16x32_bf16 v[84:87], a[164:167], v[36:39], 0\n\t"
      "v_pk_max_i16 v207, v207, 0\n\t"
      "v_mul_f32 v200, v200, |v200|\n\t"
      "v_mul_f32 v201, v201, |v201|\n\t"
      "v_mul_f32 v202, v202, |v202|\n\t"
      "v_mfma_f32_16x16x32_bf16 v[80:83], a[164:167], v[32:35], 0\n\t"
      "s_waitcnt lgkmcnt(6)\n\t"
      "v_mul_f32 v203, v203, |v203|\n\t"
      "v_mul_f32 v216, v216, |v216|\n\t"
      "v_mul_f32 v217, v217, |v217|\n\t"
      "v_mul_f32 v218, v218, |v218|\n\t"
      "v_mfma_f32_16x16x32_bf16 v[240:243], a[168:171], v[32:35], 0\n\t"
      "ds_read_b64_tr_b16 a[184:185], v20 offset:0\n\t"
      "v_mul_f32 v219, v219, |v219|\n\t"
      "v_cvt_pk_bf16_f32 v200, v200, v201\n\t"
      "v_cvt_pk_bf16_f32 v201, v202, v203\n\t"
      "v_mfma_f32_16x16x32_bf16 v[244:247], a[168:171], v[36:39], 0\n\t"
      "ds_read_b64_tr_b16 a[186:187], v20 offset:4096\n\t"
      "v_cvt_pk_bf16_f32 v202, v216, v217\n\t"
      "v_cvt_pk_bf16_f32 v203, v218, v219\n\t"
      "v_pk_max_i16 v200, v200, 0\n\t"
      "v_mfma_f32_16x16x32_bf16 v[248:251], a[168:171], v[40:43], 0\n\t"
      "v_pk_max_i16 v201, v201, 0\n\t"
      "v_pk_max_i16 v202, v202, 0\n\t"
      "v_pk_max_i16 v203, v203, 0\n\t"
      "v_mul_f32 v196, v196, |v196|\n\t"
      "v_mfma_f32_16x16x32_bf16 v[252:255], a[168:171], v[44:47], 0\n\t"
      "s_waitcnt lgkmcnt(6)\n\t"
      "v_mul_f32 v197, v197, |v197|\n\t"
      "v_mul_f32 v198, v198, |v198|\n\t"
      "v_mul_f32 v199, v199, |v199|\n\t"
      "v_mul_f32 v212, v212, |v212|\n\t"
      "v_mfma_f32_16x16x32_bf16 v[236:239], a[172:175], v[44:47], 0\n\t"
      "ds_read_b64_tr_b16 a[188:189], v21 offset:0\n\t"
      "v_mul_f32 v213, v213, |v213|\n\t"
      "v_mul_f32 v214, v214, |v214|\n\t"
      "v_mul_f32 v215, v215, |v215|\n\t"
      "v_mfma_f32_16x16x32_bf16 v[232:235], a[172:175], v[40:43], 0\n\t"
      "ds_read_b64_tr_b16 a[190:191], v21 offset:4096\n\t"
      "buffer_load_dwordx4 v1, s[36:39], s21 offen offset:1024 lds\n\t"
      "v_cvt_pk_bf16_f32 v196, v196, v197\n\t"
      "v_cvt_pk_bf16_f32 v197, v198, v199\n\t"
      "v_mfma_f32_16x16x32_bf16 v[228:231], a[172:175], v[36:39], 0\n\t"
      "v_cvt_pk_bf16_f32 v198, v212, v213\n\t"
      "v_cvt_pk_bf16_f32 v199, v214, v215\n\t"
      "v_pk_max_i16 v196, v196, 0\n\t"
      "v_pk_max_i16 v197, v197, 0\n\t"
      "v_mfma_f32_16x16x32_bf16 v[224:227], a[172:175], v[32:35], 0\n\t"
      "s_waitcnt lgkmcnt(6)\n\t"
      "v_pk_max_i16 v198, v198, 0\n\t"
      "v_pk_max_i16 v199, v199, 0\n\t"
      "v_mul_f32 v192, v192, |v192|\n\t"
      "v_mul_f32 v193, v193, |v193|\n\t"
      "v_mfma_f32_16x16x32_bf16 v[144:147], a[176:179], v[32:35], 0\n\t"
      "ds_read_b64_tr_b16 a[160:161], v18 offset:10240\n\t"
      "v_mul_f32 v194, v194, |v194|\n\t"
      "v_mul_f32 v195, v195, |v195|\n\t"
      "v_mul_f32 v208, v208, |v208|\n\t"
      "v_mfma_f32_16x16x32_bf16 v[148:151], a[176:179], v[36:39], 0\n\t"
      "ds_read_b64_tr_b16 a[162:163], v18 offset:14336\n\t"
      "v_mul_f32 v209, v209, |v209|\n\t"
      "v_mul_f32 v210, v210, |v210|\n\t"
      "v_mul_f32 v211, v211, |v211|\n\t"
      "v_mfma_f32_16x16x32_bf16 v[152:155], a[176:179], v[40:43], 0\n\t"
      "v_cvt_pk_bf16_f32 v192, v192, v193\n\t"
      "v_cvt_pk_bf16_f32 v193, v194, v195\n\t"
      "v_cvt_pk_bf16_f32 v194, v208, v209\n\t"
      "v_cvt_pk_bf16_f32 v195, v210, v211\n\t"
      "v_mfma_f32_16x16x32_bf16 v[156:159], a[176:179], v[44:47], 0\n\t"
      "s_waitcnt lgkmcnt(6)\n\t"
      "v_pk_max_i16 v192, v192, 0\n\t"
      "v_pk_max_i16 v193, v193, 0\n\t"
      "v_pk_max_i16 v194, v194, 0\n\t"
      "v_pk_max_i16 v195, v195, 0\n\t"
      "v_mfma_f32_16x16x32_bf16 v[188:191], a[180:183], v[44:47], 0\n\t"
      "s_add_u32 s30, s30, 917504\n\t"
      "ds_read_b64_tr_b16 a[164:165], v19 offset:10240\n\t"
      "buffer_store_dwordx4 v[160:163], v22, s[76:79], s30 offen offset:0 sc1\n\t"
      "v_mfma_f32_16x16x32_bf16 v[184:187], a[180:183], v[40:43], 0\n\t"
      "ds_read_b64_tr_b16 a[166:167], v19 offset:14336\n\t"
      "v_mfma_f32_16x16x32_bf16 v[180:183], a[180:183], v[36:39], 0\n\t"
      "v_mfma_f32_16x16x32_bf16 v[176:179], a[180:183], v[32:35], 0\n\t"
      "s_waitcnt lgkmcnt(6)\n\t"
      "buffer_load_dwordx4 v1, s[36:39], s22 offen offset:2048 lds\n\t"
      "v_mfma_f32_16x16x32_bf16 v[112:115], a[184:187], v[32:35], 0\n\t"
      "ds_read_b64_tr_b16 a[168:169], v20 offset:10240\n\t"
      "v_mfma_f32_16x16x32_bf16 v[116:119], a[184:187], v[36:39], 0\n\t"
      "ds_read_b64_tr_b16 a[170:171], v20 offset:14336\n\t"
      "v_mfma_f32_16x16x32_bf16 v[120:123], a[184:187], v[40:43], 0\n\t"
      "v_mfma_f32_16x16x32_bf16 v[124:127], a[184:187], v[44:47], 0\n\t"
      "s_waitcnt lgkmcnt(6)\n\t"
      "v_mfma_f32_16x16x32_bf16 v[220:223], a[188:191], v[44:47], 0\n\t"
      "ds_read_b64_tr_b16 a[172:173], v21 offset:10240\n\t"
      "v_mfma_f32_16x16x32_bf16 v[216:219], a[188:191], v[40:43], 0\n\t"
      "ds_read_b64_tr_b16 a[174:175], v21 offset:14336\n\t"
      "v_mfma_f32_16x16x32_bf16 v[212:215], a[188:191], v[36:39], 0\n\t"
      "v_mfma_f32_16x16x32_bf16 v[208:211], a[188:191], v[32:35], 0\n\t"
      "s_waitcnt lgkmcnt(6)\n\t"
      "v_mfma_f32_16x16x32_bf16 v[48:51], a[160:163], v[64:67], v[48:51]\n\t"
      "ds_read_b64_tr_b16 a[176:177], v18 offset:8192\n\t"
      "buffer_load_dwordx4 v1, s[36:39], s23 offen offset:3072 lds\n\t"
      "v_mfma_f32_16x16x32_bf16 v[52:55], a[160:163], v[68:71], v[52:55]\n\t"
      "s_add_u32 s24, s24, 16384\n\t"
      "ds_read_b64_tr_b16 a[178:179], v18 offset:12288\n\t"
      "v_readlane_b32 s54, v23, 4\n\t"
      "v_mfma_f32_16x16x32_bf16 v[56:59], a[160:163], v[72:75], v[56:59]\n\t"
      "s_and_b32 s24, s24, 0x1ffff\n\t"
      "buffer_store_dwordx4 v[164:167], v22, s[76:79], s30 offen offset:1024 sc1\n\t"
      "v_subrev_u32 v4, s64, v18\n\t"
      "v_mfma_f32_16x16x32_bf16 v[60:63], a[160:163], v[76:79], v[60:63]\n\t"
      "s_waitcnt lgkmcnt(6)\n\t"
      "s_bitcmp1_b32 s54, 0\n\t"
      "s_cselect_b64 s[36:37], s[80:81], s[84:85]\n\t"
      "v_mfma_f32_16x16x32_bf16 v[92:95], a[164:167], v[76:79], v[92:95]\n\t"
      "s_cselect_b32 s38, s82, s86\n\t"
      "s_and_b32 s54, s54, -2\n\t"
      "ds_read_b64_tr_b16 a[180:181], v19 offset:8192\n\t"
      "v_mfma_f32_16x16x32_bf16 v[88:91], a[164:167], v[72:75], v[88:91]\n\t"
      "ds_read_b64_tr_b16 a[182:183], v19 offset:12288\n\t"
      "v_add_u32 v1, s54, v17\n\t"
      "v_mfma_f32_16x16x32_bf16 v[84:87], a[164:167], v[68:71], v[84:87]\n\t"
      "v_subrev_u32 v5, s64, v19\n\t"
      "v_subrev_u32 v6, s64, v20\n\t"
      "v_mfma_f32_16x16x32_bf16 v[80:83], a[164:167], v[64:67], v[80:83]\n\t"
      "s_waitcnt lgkmcnt(6)\n\t"
      "v_subrev_u32 v7, s64, v21\n\t"
      "v_mfma_f32_16x16x32_bf16 v[240:243], a[168:171], v[64:67], v[240:243]\n\t"
      "ds_read_b64_tr_b16 a[184:185], v20 offset:8192\n\t"
      "v_mfma_f32_16x16x32_bf16 v[244:247], a[168:171], v[68:71], v[244:247]\n\t"
      "ds_read_b64_tr_b16 a[186:187], v20 offset:12288\n\t"
      "v_mfma_f32_16x16x32_bf16 v[248:251], a[168:171], v[72:75], v[248:251]\n\t"
      "v_mfma_f32_16x16x32_bf16 v[252:255], a[168:171], v[76:79], v[252:255]\n\t"
      "s_waitcnt lgkmcnt(6)\n\t"
      "v_mfma_f32_16x16x32_bf16 v[236:239], a[172:175], v[76:79], v[236:239]\n\t"
      "ds_read_b64_tr_b16 a[188:189], v21 offset:8192\n\t"
      "v_mfma_f32_16x16x32_bf16 v[232:235], a[172:175], v[72:75], v[232:235]\n\t"
      "ds_read_b64_tr_b16 a[190:191], v21 offset:12288\n\t"
      "v_mfma_f32_16x16x32_bf16 v[228:231], a[172:175], v[68:71], v[228:231]\n\t"
      "buffer_store_dwordx4 v[168:171], v29, s[76:79], s30 offen offset:0 sc1\n\t"
      "v_mfma_f32_16x16x32_bf16 v[224:227], a[172:175], v[64:67], v[224:227]\n\t"
      "s_waitcnt lgkmcnt(6)\n\t"
      "v_mfma_f32_16x16x32_bf16 v[144:147], a[176:179], v[64:67], v[144:147]\n\t"
      "s_waitcnt vmcnt(33)\n\t"
      "s_barrier\n\t"
      "ds_read_b64_tr_b16 a[160:161], v18 offset:18432\n\t"
      "v_mfma_f32_16x16x32_bf16 v[148:151], a[176:179], v[68:71], v[148:151]\n\t"
      "ds_read_b64_tr_b16 a[162:163], v18 offset:22528\n\t"
      "v_mfma_f32_16x16x32_bf16 v[152:155], a[176:179], v[72:75], v[152:155]\n\t"
      "v_mfma_f32_16x16x32_bf16 v[156:159], a[176:179], v[76:79], v[156:159]\n\t"
      "s_waitcnt lgkmcnt(6)\n\t"
      "v_mfma_f32_16x16x32_bf16 v[188:191], a[180:183], v[76:79], v[188:191]\n\t"
      "ds_read_b64_tr_b16 a[164:165], v19 offset:18432\n\t"
      "v_mfma_f32_16x16x32_bf16 v[184:187], a[180:183], v[72:75], v[184:187]\n\t"
      "ds_read_b64_tr_b16 a[166:167], v19 offset:22528\n\t"
      "v_mfma_f32_16x16x32_bf16 v[180:183], a[180:183], v[68:71], v[180:183]\n\t"
      "v_mfma_f32_16x16x32_bf16 v[176:179], a[180:183], v[64:67], v[176:179]\n\t"
      "s_waitcnt lgkmcnt(6)\n\t"
      "v_mfma_f32_16x16x32_bf16 v[112:115], a[184:187], v[64:67], v[112:115]\n\t"
      "ds_read_b64_tr_b16 a[168:169], v20 offset:18432\n\t"
      "v_mfma_f32_16x16x32_bf16 v[116:119], a[184:187], v[68:71], v[116:119]\n\t"
      "ds_read_b64_tr_b16 a[170:171], v20 offset:22528\n\t"
      "v_mfma_f32_16x16x32_bf16 v[120:123], a[184:187], v[72:75], v[120:123]\n\t"
      "buffer_store_dwordx4 v[172:175], v29, s[76:79], s30 offen offset:1024 sc1\n\t"
      "v_mfma_f32_16x16x32_bf16 v[124:127], a[184:187], v[76:79], v[124:127]\n\t"
      "s_waitcnt lgkmcnt(6)\n\t"
      "v_mfma_f32_16x16x32_bf16 v[220:223], a[188:191], v[76:79], v[220:223]\n\t"
      "ds_read_b64_tr_b16 a[172:173], v21 offset:18432\n\t"
      "v_mfma_f32_16x16x32_bf16 v[216:219], a[188:191], v[72:75], v[216:219]\n\t"
      "ds_read_b64_tr_b16 a[174:175], v21 offset:22528\n\t"
      "v_mfma_f32_16x16x32_bf16 v[212:215], a[188:191], v[68:71], v[212:215]\n\t"
      "v_readlane_b32 s53, v23, 35\n\t"
      "v_mfma_f32_16x16x32_bf16 v[208:211], a[188:191], v[64:67], v[208:211]\n\t"
      "s_waitcnt lgkmcnt(6)\n\t"
      "v_mfma_f32_16x16x32_bf16 v[48:51], a[160:163], v[96:99], v[48:51]\n\t"
      "ds_read_b64_tr_b16 a[176:177], v18 offset:16384\n\t"
      "v_add_u32 v0, s53, v16\n\t"
      "v_mfma_f32_16x16x32_bf16 v[52:55], a[160:163], v[100:103], v[52:55]\n\t"
      "ds_read_b64_tr_b16 a[178:179], v18 offset:20480\n\t"
      "v_mfma_f32_16x16x32_bf16 v[56:59], a[160:163], v[104:107], v[56:59]\n\t"
      "v_mfma_f32_16x16x32_bf16 v[60:63], a[160:163], v[108:111], v[60:63]\n\t"
      "s_waitcnt lgkmcnt(6)\n\t"
      "s_mov_b32 m0, s24\n\t"
      "v_mfma_f32_16x16x32_bf16 v[92:95], a[164:167], v[108:111], v[92:95]\n\t"
      "ds_read_b64_tr_b16 a[180:181], v19 offset:16384\n\t"
      "buffer_load_dwordx4 v1, s[36:39], s20 offen lds\n\t"
      "v_mfma_f32_16x16x32_bf16 v[88:91], a[164:167], v[104:107], v[88:91]\n\t"
      "ds_read_b64_tr_b16 a[182:183], v19 offset:20480\n\t"
      "v_mfma_f32_16x16x32_bf16 v[84:87], a[164:167], v[100:103], v[84:87]\n\t"
      "s_add_u32 s30, s30, 131072\n\t"
      "buffer_store_dwordx4 v[192:195], v22, s[76:79], s30 offen offset:0 sc1\n\t"
      "s_waitcnt vmcnt(24)\n\t"
      STAMP(8, 9)
      "v_cmp_eq_u64 vcc, s[8:9], v[2:3]\n\t"
      "s_nop 0\n\t"
      "s_cmp_eq_u64 vcc, -1\n\t"
      LSTW(8, "s_waitcnt lgkmcnt(0)\n\t")
      "s_cbranch_scc1 Lpd17_%=\n\t"
      "s_mov_b32 s10, 200000\n\t"
      "Lp17_%=:\n\t"
      "buffer_load_dwordx2 v[2:3], v27, s[96:99], s16 offen offset:2048 sc1\n\t"
      "s_waitcnt vmcnt(0)\n\t"
      "v_cmp_eq_u64 vcc, s[8:9], v[2:3]\n\t"
      "s_nop 0\n\t"
      "s_cmp_eq_u64 vcc, -1\n\t"
      "s_cbranch_scc1 Lpd17_%=\n\t"
      "s_sleep 2\n\t"
      "s_sub_u32 s10, s10, 1\n\t"
      "s_cmp_lg_u32 s10, 0\n\t"
      "s_cbranch_scc1 Lp17_%=\n\t"
      "Lpd17_%=:\n\t"
      STAMP(44, 45)
      "v_mfma_f32_16x16x32_bf16 v[80:83], a[164:167], v[96:99], v[80:83]\n\t"
      "s_waitcnt lgkmcnt(6)\n\t"
      "v_mfma_f32_16x16x32_bf16 v[240:243], a[168:171], v[96:99], v[240:243]\n\t"
      "ds_read_b64_tr_b16 a[184:185], v20 offset:16384\n\t"
      "v_and_b32 v40, 1023, v16\n\t"
      "v_mfma_f32_16x16x32_bf16 v[244:247], a[168:171], v[100:103], v[244:247]\n\t"
      "ds_read_b64_tr_b16 a[186:187], v20 offset:20480\n\t"
      "v_lshrrev_b32 v45, 4, v40\n\t"
      "v_mfma_f32_16x16x32_bf16 v[248:251], a[168:171], v[104:107], v[248:251]\n\t"
      "v_and_b32 v44, 3, v45\n\t"
      "v_mfma_f32_16x16x32_bf16 v[252:255], a[168:171], v[108:111], v[252:255]\n\t"
      "s_waitcnt lgkmcnt(6)\n\t"
      "v_or_b32 v38, 2, v44\n\t"
      "v_mfma_f32_16x16x32_bf16 v[236:239], a[172:175], v[108:111], v[236:239]\n\t"
      "ds_read_b64_tr_b16 a[188:189], v21 offset:16384\n\t"
      "v_lshlrev_b32 v38, 8, v38\n\t"
      "v_mfma_f32_16x16x32_bf16 v[232:235], a[172:175], v[104:107], v[232:235]\n\t"
      "ds_read_b64_tr_b16 a[190:191], v21 offset:20480\n\t"
      "buffer_load_dwordx4 v1, s[36:39], s21 offen offset:1024 lds\n\t"
      "v_mfma_f32_16x16x32_bf16 v[228:231], a[172:175], v[100:103], v[228:231]\n\t"
      "v_xor_b32 v38, s19, v38\n\t"
      "v_mfma_f32_16x16x32_bf16 v[224:227], a[172:175], v[96:99], v[224:227]\n\t"
      "s_waitcnt lgkmcnt(6)\n\t"
      LSTW(44, "")
      "v_add_u32 v38, 64, v38\n\t"
      "v_mfma_f32_16x16x32_bf16 v[144:147], a[176:179], v[96:99], v[144:147]\n\t"
      "ds_read_b64_tr_b16 a[160:161], v18 offset:26624\n\t"
      "v_mov_b32 v39, 256\n\t"
      "v_mfma_f32_16x16x32_bf16 v[148:151], a[176:179], v[100:103], v[148:151]\n\t"
      "ds_read_b64_tr_b16 a[162:163], v18 offset:30720\n\t"
      "v_xor_b32 v39, s19, v39\n\t"
      "v_mfma_f32_16x16x32_bf16 v[152:155], a[176:179], v[104:107], v[152:155]\n\t"
      "buffer_store_dwordx4 v[196:199], v22, s[76:79], s30 offen offset:1024 sc1\n\t"
      "v_and_b32 v47, 15, v45\n\t"
      "v_mfma_f32_16x16x32_bf16 v[156:159], a[176:179], v[108:111], v[156:159]\n\t"
      "s_waitcnt lgkmcnt(6)\n\t"
      "v_lshl_add_u32 v26, v47, 7, s55\n\t"
      "v_mfma_f32_16x16x32_bf16 v[188:191], a[180:183], v[108:111], v[188:191]\n\t"
      "ds_read_b64_tr_b16 a[164:165], v19 offset:26624\n\t"
      "v_lshrrev_b32 v46, 4, v45\n\t"
      "v_mfma_f32_16x16x32_bf16 v[184:187], a[180:183], v[104:107], v[184:187]\n\t"
      "ds_read_b64_tr_b16 a[166:167], v19 offset:30720\n\t"
      "v_bfe_u32 v47, v45, 2, 2\n\t"
      "v_mfma_f32_16x16x32_bf16 v[180:183], a[180:183], v[100:103], v[180:183]\n\t"
      "v_cmp_eq_u32 vcc, v46, v47\n\t"
      "v_mfma_f32_16x16x32_bf16 v[176:179], a[180:183], v[96:99], v[176:179]\n\t"
      "s_waitcnt lgkmcnt(6)\n\t"
      "buffer_load_dwordx4 v1, s[36:39], s22 offen offset:2048 lds\n\t"
      "v_and_b32 v46, 1, v44\n\t"
      "v_mfma_f32_16x16x32_bf16 v[112:115], a[184:187], v[96:99], v[112:115]\n\t"
      "ds_read_b64_tr_b16 a[168:169], v20 offset:26624\n\t"
      "v_lshlrev_b32 v46, 4, v46\n\t"
      "v_mfma_f32_16x16x32_bf16 v[116:119], a[184:187], v[100:103], v[116:119]\n\t"
      "ds_read_b64_tr_b16 a[170:171], v20 offset:30720\n\t"
      "v_mov_b32 v47, 0x3f80\n\t"
      "v_mfma_f32_16x16x32_bf16 v[120:123], a[184:187], v[104:107], v[120:123]\n\t"
      "s_add_u32 s64, s64, 32768\n\t"
      "s_and_b32 s64, s64, 0x1ffff\n\t"
      "v_lshlrev_b32 v46, v46, v47\n\t"
      "v_mfma_f32_16x16x32_bf16 v[124:127], a[184:187], v[108:111], v[124:127]\n\t"
      "s_waitcnt lgkmcnt(6)\n\t"
      "v_cndmask_b32 v46, 0, v46, vcc\n\t"
      "v_mfma_f32_16x16x32_bf16 v[220:223], a[188:191], v[108:111], v[220:223]\n\t"
      "ds_read_b64_tr_b16 a[172:173], v21 offset:26624\n\t"
      "v_cmp_gt_u32 vcc, 2, v44\n\t"
      "v_mfma_f32_16x16x32_bf16 v[216:219], a[188:191], v[104:107], v[216:219]\n\t"
      "ds_read_b64_tr_b16 a[174:175], v21 offset:30720\n\t"
      "v_mov_b64 v[32:33], 0\n\t"
      "v_mfma_f32_16x16x32_bf16 v[212:215], a[188:191], v[100:103], v[212:215]\n\t"
      "buffer_store_dwordx4 v[200:203], v29, s[76:79], s30 offen offset:0 sc1\n\t"
      "v_mov_b64 v[36:37], 0\n\t"
      "v_mfma_f32_16x16x32_bf16 v[208:211], a[188:191], v[96:99], v[208:211]\n\t"
      "s_waitcnt lgkmcnt(6)\n\t"
      "v_cndmask_b32 v34, 0, v46, vcc\n\t"
      "v_mfma_f32_16x16x32_bf16 v[48:51], a[160:163], v[128:131], v[48:51]\n\t"
      "ds_read_b64_tr_b16 a[176:177], v18 offset:24576\n\t"
      "buffer_load_dwordx4 v1, s[36:39], s23 offen offset:3072 lds\n\t"
      "v_mfma_f32_16x16x32_bf16 v[52:55], a[160:163], v[132:135], v[52:55]\n\t"
      "s_add_u32 s24, s24, 16384\n\t"
      "ds_read_b64_tr_b16 a[178:179], v18 offset:28672\n\t"
      "v_readlane_b32 s54, v23, 5\n\t"
      "v_mfma_f32_16x16x32_bf16 v[56:59], a[160:163], v[136:139], v[56:59]\n\t"
      "s_and_b32 s24, s24, 0x1ffff\n\t"
      "buffer_load_dwordx4 a[224:227], v0, s[76:79], s34 offen sc1\n\t"
      "v_add_u32 v18, s64, v4\n\t"
      "v_mfma_f32_16x16x32_bf16 v[60:63], a[160:163], v[140:143], v[60:63]\n\t"
      "s_waitcnt lgkmcnt(6)\n\t"
      "s_bitcmp1_b32 s54, 0\n\t"
      "s_cselect_b64 s[36:37], s[80:81], s[84:85]\n\t"
      "v_mfma_f32_16x16x32_bf16 v[92:95], a[164:167], v[140:143], v[92:95]\n\t"
      "s_cselect_b32 s38, s82, s86\n\t"
      "s_and_b32 s54, s54, -2\n\t"
      "ds_read_b64_tr_b16 a[180:181], v19 offset:24576\n\t"
      "v_mfma_f32_16x16x32_bf16 v[88:91], a[164:167], v[136:139], v[88:91]\n\t"
      "ds_read_b64_tr_b16 a[182:183], v19 offset:28672\n\t"
      "v_add_u32 v1, s54, v17\n\t"
      "v_mfma_f32_16x16x32_bf16 v[84:87], a[164:167], v[132:135], v[84:87]\n\t"
      "v_add_u32 v19, s64, v5\n\t"
      "v_sub_u32 v35, v46, v34\n\t"
      "v_mfma_f32_16x16x32_bf16 v[80:83], a[164:167], v[128:131], v[80:83]\n\t"
      "s_waitcnt lgkmcnt(6)\n\t"
      "v_and_b32 v46, 1, v45\n\t"
      "v_mfma_f32_16x16x32_bf16 v[240:243], a[168:171], v[128:131], v[240:243]\n\t"
      "ds_read_b64_tr_b16 a[184:185], v20 offset:24576\n\t"
      "v_lshlrev_b32 v46, 4, v46\n\t"
      "v_mfma_f32_16x16x32_bf16 v[244:247], a[168:171], v[132:135], v[244:247]\n\t"
      "ds_read_b64_tr_b16 a[186:187], v20 offset:28672\n\t"
      "v_mov_b32 v47, 0x3f80\n\t"
      "v_mfma_f32_16x16x32_bf16 v[248:251], a[168:171], v[136:139], v[248:251]\n\t"
      "buffer_load_dwordx4 a[228:231], v0, s[76:79], s34 offen offset:1024 sc1\n\t"
      "v_add_u32 v20, s64, v6\n\t"
      "v_mfma_f32_16x16x32_bf16 v[252:255], a[168:171], v[140:143], v[252:255]\n\t"
      "s_waitcnt lgkmcnt(6)\n\t"
      "buffer_store_dwordx4 v[204:207], v29, s[76:79], s30 offen offset:1024 sc1\n\t"
      "v_lshlrev_b32 v46, v46, v47\n\t"
      "v_mfma_f32_16x16x32_bf16 v[236:239], a[172:175], v[140:143], v[236:239]\n\t"
      "ds_read_b64_tr_b16 a[188:189], v21 offset:24576\n\t"
      "v_lshrrev_b32 v47, 5, v45\n\t"
      "v_mfma_f32_16x16x32_bf16 v[232:235], a[172:175], v[136:139], v[232:235]\n\t"
      "ds_read_b64_tr_b16 a[190:191], v21 offset:28672\n\t"
      "v_bfe_u32 v44, v45, 3, 1\n\t"
      "v_mfma_f32_16x16x32_bf16 v[228:231], a[172:175], v[132:135], v[228:231]\n\t"
      "v_add_u32 v21, s64, v7\n\t"
      "v_cmp_eq_u32 vcc, v47, v44\n\t"
      "v_mfma_f32_16x16x32_bf16 v[224:227], a[172:175], v[128:131], v[224:227]\n\t"
      "s_waitcnt lgkmcnt(6)\n\t"
      "v_bfe_u32 v47, v45, 4, 1\n\t"
      "v_mfma_f32_16x16x32_bf16 v[144:147], a[176:179], v[128:131], v[144:147]\n\t"
      "s_waitcnt vmcnt(18)\n\t"
      "s_barrier\n\t"
      "ds_read_b64_tr_b16 a[160:161], v18 offset:2048\n\t"
      "v_mfma_f32_16x16x32_bf16 v[148:151], a[176:179], v[132:135], v[148:151]\n\t"
      "ds_read_b64_tr_b16 a[162:163], v18 offset:6144\n\t"
      "v_bfe_u32 v44, v45, 1, 2\n\t"
      "v_mfma_f32_16x16x32_bf16 v[152:155], a[176:179], v[136:139], v[152:155]\n\t"
      "buffer_load_dwordx4 a[232:235], v0, s[76:79], s35 offen sc1\n\t"
      "v_cndmask_b32 v46, 0, v46, vcc\n\t"
      "v_mfma_f32_16x16x32_bf16 v[156:159], a[176:179], v[140:143], v[156:159]\n\t"
      "s_waitcnt lgkmcnt(6)\n\t"
      "v_lshl_or_b32 v44, v47, 2, v44\n\t"
      "v_mfma_f32_16x16x32_bf16 v[188:191], a[180:183], v[140:143], v[188:191]\n\t"
      "ds_read_b64_tr_b16 a[164:165], v19 offset:2048\n\t"
      "v_cmp_eq_u32 vcc, 0, v44\n\t"
      "v_mfma_f32_16x16x32_bf16 v[184:187], a[180:183], v[136:139], v[184:187]\n\t"
      "ds_read_b64_tr_b16 a[166:167], v19 offset:6144\n\t"
      "v_cndmask_b32 v64, 0, v46, vcc\n\t"
      "v_mfma_f32_16x16x32_bf16 v[180:183], a[180:183], v[132:135], v[180:183]\n\t"
      "v_cmp_eq_u32 vcc, 1, v44\n\t"
      "v_mfma_f32_16x16x32_bf16 v[176:179], a[180:183], v[128:131], v[176:179]\n\t"
      "s_waitcnt lgkmcnt(6)\n\t"
      "v_cndmask_b32 v65, 0, v46, vcc\n\t"
      "v_mfma_f32_16x16x32_bf16 v[112:115], a[184:187], v[128:131], v[112:115]\n\t"
      "ds_read_b64_tr_b16 a[168:169], v20 offset:2048\n\t"
      "v_cmp_eq_u32 vcc, 2, v44\n\t"
      "v_mfma_f32_16x16x32_bf16 v[116:119], a[184:187], v[132:135], v[116:119]\n\t"
      "ds_read_b64_tr_b16 a[170:171], v20 offset:6144\n\t"
      "v_cndmask_b32 v66, 0, v46, vcc\n\t"
      "v_mfma_f32_16x16x32_bf16 v[120:123], a[184:187], v[136:139], v[120:123]\n\t"
      "buffer_load_dwordx4 a[236:239], v0, s[76:79], s35 offen offset:1024 sc1\n\t"
      "v_cmp_eq_u32 vcc, 3, v44\n\t"
      "v_mfma_f32_16x16x32_bf16 v[124:127], a[184:187], v[140:143], v[124:127]\n\t"
      "s_waitcnt lgkmcnt(6)\n\t"
      "v_add_u32 v0, s51, v0\n\t"
      "v_cndmask_b32 v67, 0, v46, vcc\n\t"
      "v_mfma_f32_16x16x32_bf16 v[220:223], a[188:191], v[140:143], v[220:223]\n\t"
      "ds_read_b64_tr_b16 a[172:173], v21 offset:2048\n\t"
      "v_cmp_eq_u32 vcc, 4, v44\n\t"
      "v_mfma_f32_16x16x32_bf16 v[216:219], a[188:191], v[136:139], v[216:219]\n\t"
      "ds_read_b64_tr_b16 a[174:175], v21 offset:6144\n\t"
      "v_cndmask_b32 v68, 0, v46, vcc\n\t"
      "v_mfma_f32_16x16x32_bf16 v[212:215], a[188:191], v[132:135], v[212:215]\n\t"
      "v_cmp_eq_u32 vcc, 5, v44\n\t"
      "v_mfma_f32_16x16x32_bf16 v[208:211], a[188:191], v[128:131], v[208:211]\n\t"
      "s_waitcnt lgkmcnt(6)\n\t"
      "v_cndmask_b32 v69, 0, v46, vcc\n\t"
      "v_mfma_f32_16x16x32_bf16 v[48:51], a[160:163], v[160:163], v[48:51]\n\t"
      "ds_read_b64_tr_b16 a[176:177], v18 offset:0\n\t"
      "v_cmp_eq_u32 vcc, 6, v44\n\t"
      "v_mfma_f32_16x16x32_bf16 v[52:55], a[160:163], v[164:167], v[52:55]\n\t"
      "ds_read_b64_tr_b16 a[178:179], v18 offset:4096\n\t"
      "v_cndmask_b32 v70, 0, v46, vcc\n\t"
      "v_mfma_f32_16x16x32_bf16 v[56:59], a[160:163], v[168:171], v[56:59]\n\t"
      "buffer_load_dwordx4 a[240:243], v0, s[76:79], s34 offen sc1\n\t"
      "v_cmp_eq_u32 vcc, 7, v44\n\t"
      "v_mfma_f32_16x16x32_bf16 v[60:63], a[160:163], v[172:175], v[60:63]\n\t"
      "s_waitcnt lgkmcnt(6)\n\t"
      "s_mov_b32 m0, s24\n\t"
      "v_cndmask_b32 v71, 0, v46, vcc\n\t"
      "v_mfma_f32_16x16x32_bf16 v[92:95], a[164:167], v[172:175], v[92:95]\n\t"
      "ds_read_b64_tr_b16 a[180:181], v19 offset:0\n\t"
      "buffer_load_dwordx4 v1, s[36:39], s20 offen lds\n\t"
      "s_lshl_b32 s3, s32, 12\n\t"
      "v_mfma_f32_16x16x32_bf16 v[88:91], a[164:167], v[168:171], v[88:91]\n\t"
      "ds_read_b64_tr_b16 a[182:183], v19 offset:4096\n\t"
      "s_add_u32 s3, s3, 147456\n\t"
      "v_mfma_f32_16x16x32_bf16 v[84:87], a[164:167], v[164:167], v[84:87]\n\t"
      "v_add_u32 v78, s3, v40\n\t"
      "v_mfma_f32_16x16x32_bf16 v[80:83], a[164:167], v[160:163], v[80:83]\n\t"
      "s_waitcnt lgkmcnt(6)\n\t"
      "v_and_b32 v46, 15, v45\n\t"
      "v_mfma_f32_16x16x32_bf16 v[240:243], a[168:171], v[160:163], v[240:243]\n\t"
      "ds_read_b64_tr_b16 a[184:185], v20 offset:0\n\t"
      "v_lshl_add_u32 v46, v46, 7, s3\n\t"
      "v_mfma_f32_16x16x32_bf16 v[244:247], a[168:171], v[164:167], v[244:247]\n\t"
      "ds_read_b64_tr_b16 a[186:187], v20 offset:4096\n\t"
      "v_and_b32 v47, 6, v45\n\t"
      "v_mfma_f32_16x16x32_bf16 v[248:251], a[168:171], v[168:171], v[248:251]\n\t"
      "buffer_load_dwordx4 a[244:247], v0, s[76:79], s34 offen offset:1024 sc1\n\t"
      "v_lshl_or_b32 v46, v47, 4, v46\n\t"
      "v_mfma_f32_16x16x32_bf16 v[252:255], a[168:171], v[172:175], v[252:255]\n\t"
      "s_waitcnt lgkmcnt(6)\n\t"
      "v_bfe_u32 v47, v45, 5, 1\n\t"
      "v_mfma_f32_16x16x32_bf16 v[236:239], a[172:175], v[172:175], v[236:239]\n\t"
      "ds_read_b64_tr_b16 a[188:189], v21 offset:0\n\t"
      "v_lshlrev_b32 v47, 4, v47\n\t"
      "v_mfma_f32_16x16x32_bf16 v[232:235], a[172:175], v[168:171], v[232:235]\n\t"
      "ds_read_b64_tr_b16 a[190:191], v21 offset:4096\n\t"
      "buffer_load_dwordx4 v1, s[36:39], s21 offen offset:1024 lds\n\t"
      "v_mfma_f32_16x16x32_bf16 v[228:231], a[172:175], v[164:167], v[228:231]\n\t"
      "v_bfe_u32 v44, v45, 4, 1\n\t"
      "v_mfma_f32_16x16x32_bf16 v[224:227], a[172:175], v[160:163], v[224:227]\n\t"
      "s_waitcnt lgkmcnt(6)\n\t"
      "v_lshl_or_b32 v72, v44, 5, v47\n\t"
      "v_mfma_f32_16x16x32_bf16 v[144:147], a[176:179], v[160:163], v[144:147]\n\t"
      "ds_read_b64_tr_b16 a[160:161], v18 offset:10240\n\t"
      "v_xor_b32 v72, v72, v46\n\t"
      "v_mfma_f32_16x16x32_bf16 v[148:151], a[176:179], v[164:167], v[148:151]\n\t"
      "ds_read_b64_tr_b16 a[162:163], v18 offset:14336\n\t"
      "v_xor_b32 v73, 64, v72\n\t"
      "v_mfma_f32_16x16x32_bf16 v[152:155], a[176:179], v[168:171], v[152:155]\n\t"
      "buffer_load_dwordx4 a[248:251], v0, s[76:79], s35 offen sc1\n\t"
      "v_lshl_or_b32 v74, v44, 3, v47\n\t"
      "v_mfma_f32_16x16x32_bf16 v[156:159], a[176:179], v[172:175], v[156:159]\n\t"
      "s_waitcnt lgkmcnt(6)\n\t"
      "v_xor_b32 v74, v74, v46\n\t"
      "v_mfma_f32_16x16x32_bf16 v[188:191], a[180:183], v[172:175], v[188:191]\n\t"
      "ds_read_b64_tr_b16 a[164:165], v19 offset:10240\n\t"
      "v_xor_b32 v75, 32, v74\n\t"
      "v_mfma_f32_16x16x32_bf16 v[184:187], a[180:183], v[168:171], v[184:187]\n\t"
      "ds_read_b64_tr_b16 a[166:167], v19 offset:14336\n\t"
      "v_xor_b32 v76, 64, v74\n\t"
      "v_mfma_f32_16x16x32_bf16 v[180:183], a[180:183], v[164:167], v[180:183]\n\t"
      "v_xor_b32 v77, 0x60, v74\n\t"
      "v_mfma_f32_16x16x32_bf16 v[176:179], a[180:183], v[160:163], v[176:179]\n\t"
      "s_waitcnt lgkmcnt(6)\n\t"
      "buffer_load_dwordx4 v1, s[36:39], s22 offen offset:2048 lds\n\t"
      "v_mfma_f32_16x16x32_bf16 v[112:115], a[184:187], v[160:163], v[112:115]\n\t"
      "ds_read_b64_tr_b16 a[168:169], v20 offset:10240\n\t"
      "v_mfma_f32_16x16x32_bf16 v[116:119], a[184:187], v[164:167], v[116:119]\n\t"
      "ds_read_b64_tr_b16 a[170:171], v20 offset:14336\n\t"
      "v_mfma_f32_16x16x32_bf16 v[120:123], a[184:187], v[168:171], v[120:123]\n\t"
      "buffer_load_dwordx4 a[252:255], v0, s[76:79], s35 offen offset:1024 sc1\n\t"
      "v_mfma_f32_16x16x32_bf16 v[124:127], a[184:187], v[172:175], v[124:127]\n\t"
      "s_waitcnt lgkmcnt(6)\n\t"
      "v_readlane_b32 s53, v23, 36\n\t"
      "v_mfma_f32_16x16x32_bf16 v[220:223], a[188:191], v[172:175], v[220:223]\n\t"
      "ds_read_b64_tr_b16 a[172:173], v21 offset:10240\n\t"
      "v_mfma_f32_16x16x32_bf16 v[216:219], a[188:191], v[168:171], v[216:219]\n\t"
      "ds_read_b64_tr_b16 a[174:175], v21 offset:14336\n\t"
      "v_add_u32 v0, s53, v16\n\t"
      "v_mfma_f32_16x16x32_bf16 v[212:215], a[188:191], v[164:167], v[212:215]\n\t"
      "s_getpc_b64 s[40:41]\n\t"
      "Lpc21_%=:\n\t"
      "s_add_u32 s40, s40, Llast_%=-Lpc21_%=\n\t"
      "s_addc_u32 s41, s41, 0\n\t"
      "v_mfma_f32_16x16x32_bf16 v[208:211], a[188:191], v[160:163], v[208:211]\n\t"
      "s_waitcnt lgkmcnt(6)\n\t"
      "s_and_b32 s41, s41, 0xffff\n\t"
      "v_mfma_f32_16x16x32_bf16 v[48:51], a[160:163], v[192:195], v[48:51]\n\t"
      "ds_read_b64_tr_b16 a[176:177], v18 offset:8192\n\t"
      "buffer_load_dwordx4 v1, s[36:39], s23 offen offset:3072 lds\n\t"
      "s_mov_b32 s42, Lend_%=-Llast_%=\n\t"
      "v_mfma_f32_16x16x32_bf16 v[52:55], a[160:163], v[196:199], v[52:55]\n\t"
      "s_add_u32 s24, s24, 16384\n\t"
      "ds_read_b64_tr_b16 a[178:179], v18 offset:12288\n\t"
      "v_readlane_b32 s54, v23, 6\n\t"
      "v_mfma_f32_16x16x32_bf16 v[56:59], a[160:163], v[200:203], v[56:59]\n\t"
      "s_and_b32 s24, s24, 0x1ffff\n\t"
      "buffer_load_dwordx4 a[192:195], v0, s[76:79], s34 offen sc1\n\t"
      "s_lshr_b32 s3, s2, 3\n\t"
      "v_mfma_f32_16x16x32_bf16 v[60:63], a[160:163], v[204:207], v[60:63]\n\t"
      "s_waitcnt lgkmcnt(6)\n\t"
      "s_bitcmp1_b32 s54, 0\n\t"
      "s_cselect_b64 s[36:37], s[80:81], s[84:85]\n\t"
      "v_mfma_f32_16x16x32_bf16 v[92:95], a[164:167], v[204:207], v[92:95]\n\t"
      "s_cselect_b32 s38, s82, s86\n\t"
      "s_and_b32 s54, s54, -2\n\t"
      "ds_read_b64_tr_b16 a[180:181], v19 offset:8192\n\t"
      "v_mfma_f32_16x16x32_bf16 v[88:91], a[164:167], v[200:203], v[88:91]\n\t"
      "ds_read_b64_tr_b16 a[182:183], v19 offset:12288\n\t"
      "v_add_u32 v1, s54, v17\n\t"
      "s_lshl2_add_u32 s3, s3, s32\n\t"
      "v_mfma_f32_16x16x32_bf16 v[84:87], a[164:167], v[196:199], v[84:87]\n\t"
      "s_lshl_b32 s3, s3, 7\n\t"
      "v_mfma_f32_16x16x32_bf16 v[80:83], a[164:167], v[192:195], v[80:83]\n\t"
      "s_waitcnt lgkmcnt(6)\n\t"
      "v_lshlrev_b32 v41, 10, v40\n\t"
      "v_mfma_f32_16x16x32_bf16 v[240:243], a[168:171], v[192:195], v[240:243]\n\t"
      "ds_read_b64_tr_b16 a[184:185], v20 offset:8192\n\t"
      "v_add_u32 v41, s3, v41\n\t"
      "v_mfma_f32_16x16x32_bf16 v[244:247], a[168:171], v[196:199], v[244:247]\n\t"
      "ds_read_b64_tr_b16 a[186:187], v20 offset:12288\n\t"
      "v_mfma_f32_16x16x32_bf16 v[248:251], a[168:171], v[200:203], v[248:251]\n\t"
      "buffer_load_dwordx4 a[196:199], v0, s[76:79], s34 offen offset:1024 sc1\n\t"
      "buffer_load_dwordx2 v[2:3], v27, s[96:99], 0 offen sc1\n\t"
      "v_mfma_f32_16x16x32_bf16 v[252:255], a[168:171], v[204:207], v[252:255]\n\t"
      "s_waitcnt lgkmcnt(6)\n\t"
      "v_mfma_f32_16x16x32_bf16 v[236:239], a[172:175], v[204:207], v[236:239]\n\t"
      "ds_read_b64_tr_b16 a[188:189], v21 offset:8192\n\t"
      "v_mfma_f32_16x16x32_bf16 v[232:235], a[172:175], v[200:203], v[232:235]\n\t"
      "ds_read_b64_tr_b16 a[190:191], v21 offset:12288\n\t"
      "v_mfma_f32_16x16x32_bf16 v[228:231], a[172:175], v[196:199], v[228:231]\n\t"
      "v_mfma_f32_16x16x32_bf16 v[224:227], a[172:175], v[192:195], v[224:227]\n\t"
      "s_waitcnt lgkmcnt(6)\n\t"
      "v_mfma_f32_16x16x32_bf16 v[144:147], a[176:179], v[192:195], v[144:147]\n\t"
      "s_waitcnt vmcnt(13)\n\t"
      "s_waitcnt vmcnt(26)\n\t"
      "s_barrier\n\t"
      "s_lshl_b32 s5, s32, 24\n\t"
      "s_add_u32 s5, s5, s27\n\t"
      "v_mov_b32 v12, s8\n\t"
      "v_mov_b32 v13, s9\n\t"
      "s_mov_b64 exec, 1\n\t"
      "buffer_store_dwordx2 v[12:13], off, s[96:99], s5 sc1\n\t"
      "s_mov_b64 exec, -1\n\t"
      STAMP(22, 23)
      "ds_read_b64_tr_b16 a[160:161], v18 offset:18432\n\t"
      "v_mfma_f32_16x16x32_bf16 v[148:151], a[176:179], v[196:199], v[148:151]\n\t"
      "ds_read_b64_tr_b16 a[162:163], v18 offset:22528\n\t"
      "v_mfma_f32_16x16x32_bf16 v[152:155], a[176:179], v[200:203], v[152:155]\n\t"
      "buffer_load_dwordx4 a[200:203], v0, s[76:79], s35 offen sc1\n\t"
      "v_mfma_f32_16x16x32_bf16 v[156:159], a[176:179], v[204:207], v[156:159]\n\t"
      "s_waitcnt lgkmcnt(6)\n\t"
      "v_mfma_f32_16x16x32_bf16 v[188:191], a[180:183], v[204:207], v[188:191]\n\t"
      "ds_read_b64_tr_b16 a[164:165], v19 offset:18432\n\t"
      "v_mfma_f32_16x16x32_bf16 v[184:187], a[180:183], v[200:203], v[184:187]\n\t"
      "ds_read_b64_tr_b16 a[166:167], v19 offset:22528\n\t"
      "v_mfma_f32_16x16x32_bf16 v[180:183], a[180:183], v[196:199], v[180:183]\n\t"
      "v_mfma_f32_16x16x32_bf16 v[176:179], a[180:183], v[192:195], v[176:179]\n\t"
      "s_waitcnt lgkmcnt(6)\n\t"
      "v_mfma_f32_16x16x32_bf16 v[112:115], a[184:187], v[192:195], v[112:115]\n\t"
      "ds_read_b64_tr_b16 a[168:169], v20 offset:18432\n\t"
      "v_mfma_f32_16x16x32_bf16 v[116:119], a[184:187], v[196:199], v[116:119]\n\t"
      "ds_read_b64_tr_b16 a[170:171], v20 offset:22528\n\t"
      "v_mfma_f32_16x16x32_bf16 v[120:123], a[184:187], v[200:203], v[120:123]\n\t"
      "buffer_load_dwordx4 a[204:207], v0, s[76:79], s35 offen offset:1024 sc1\n\t"
      "v_mfma_f32_16x16x32_bf16 v[124:127], a[184:187], v[204:207], v[124:127]\n\t"
      "s_waitcnt lgkmcnt(6)\n\t"
      "v_add_u32 v0, s51, v0\n\t"
      LSTW(22, "")
      "v_mfma_f32_16x16x32_bf16 v[220:223], a[188:191], v[204:207], v[220:223]\n\t"
      "ds_read_b64_tr_b16 a[172:173], v21 offset:18432\n\t"
      "v_mfma_f32_16x16x32_bf16 v[216:219], a[188:191], v[200:203], v[216:219]\n\t"
      "ds_read_b64_tr_b16 a[174:175], v21 offset:22528\n\t"
      "v_mfma_f32_16x16x32_bf16 v[212:215], a[188:191], v[196:199], v[212:215]\n\t"
      "s_waitcnt vmcnt(14)\n\t"
      "v_mfma_f32_16x16x32_bf16 v[208:211], a[188:191], v[192:195], v[208:211]\n\t"
      "s_waitcnt lgkmcnt(6)\n\t"
      "v_mfma_f32_16x16x32_bf16 v[48:51], a[160:163], a[224:227], v[48:51]\n\t"
      "ds_read_b64_tr_b16 a[176:177], v18 offset:16384\n\t"
      "v_mfma_f32_16x16x32_bf16 v[52:55], a[160:163], a[228:231], v[52:55]\n\t"
      "ds_read_b64_tr_b16 a[178:179], v18 offset:20480\n\t"
      "v_mfma_f32_16x16x32_bf16 v[56:59], a[160:163], a[232:235], v[56:59]\n\t"
      "buffer_load_dwordx4 a[208:211], v0, s[76:79], s34 offen sc1\n\t"
      "v_mfma_f32_16x16x32_bf16 v[60:63], a[160:163], a[236:239], v[60:63]\n\t"
      "s_waitcnt lgkmcnt(6)\n\t"
      "s_mov_b32 m0, s24\n\t"
      "v_mfma_f32_16x16x32_bf16 v[92:95], a[164:167], a[236:239], v[92:95]\n\t"
      "ds_read_b64_tr_b16 a[180:181], v19 offset:16384\n\t"
      "buffer_load_dwordx4 v1, s[36:39], s20 offen lds\n\t"
      "v_mfma_f32_16x16x32_bf16 v[88:91], a[164:167], a[232:235], v[88:91]\n\t"
      "ds_read_b64_tr_b16 a[182:183], v19 offset:20480\n\t"
      "v_mfma_f32_16x16x32_bf16 v[84:87], a[164:167], a[228:231], v[84:87]\n\t"
      "v_mfma_f32_16x16x32_bf16 v[80:83], a[164:167], a[224:227], v[80:83]\n\t"
      "s_waitcnt lgkmcnt(6)\n\t"
      "v_mfma_f32_16x16x32_bf16 v[240:243], a[168:171], a[224:227], v[240:243]\n\t"
      "ds_read_b64_tr_b16 a[184:185], v20 offset:16384\n\t"
      "v_mfma_f32_16x16x32_bf16 v[244:247], a[168:171], a[228:231], v[244:247]\n\t"
      "ds_read_b64_tr_b16 a[186:187], v20 offset:20480\n\t"
      "v_mfma_f32_16x16x32_bf16 v[248:251], a[168:171], a[232:235], v[248:251]\n\t"
      "buffer_load_dwordx4 a[212:215], v0, s[76:79], s34 offen offset:1024 sc1\n\t"
      "v_mfma_f32_16x16x32_bf16 v[252:255], a[168:171], a[236:239], v[252:255]\n\t"
      "s_waitcnt lgkmcnt(6)\n\t"
      "v_mfma_f32_16x16x32_bf16 v[236:239], a[172:175], a[236:239], v[236:239]\n\t"
      "ds_read_b64_tr_b16 a[188:189], v21 offset:16384\n\t"
      "v_mfma_f32_16x16x32_bf16 v[232:235], a[172:175], a[232:235], v[232:235]\n\t"
      "ds_read_b64_tr_b16 a[190:191], v21 offset:20480\n\t"
      "buffer_load_dwordx4 v1, s[36:39], s21 offen offset:1024 lds\n\t"
      "v_mfma_f32_16x16x32_bf16 v[228:231], a[172:175], a[228:231], v[228:231]\n\t"
      "v_mfma_f32_16x16x32_bf16 v[224:227], a[172:175], a[224:227], v[224:227]\n\t"
      "s_waitcnt lgkmcnt(6)\n\t"
      "v_mfma_f32_16x16x32_bf16 v[144:147], a[176:179], a[224:227], v[144:147]\n\t"
      "ds_read_b64_tr_b16 a[160:161], v18 offset:26624\n\t"
      "v_mfma_f32_16x16x32_bf16 v[148:151], a[176:179], a[228:231], v[148:151]\n\t"
      "ds_read_b64_tr_b16 a[162:163], v18 offset:30720\n\t"
      "v_mfma_f32_16x16x32_bf16 v[152:155], a[176:179], a[232:235], v[152:155]\n\t"
      "buffer_load_dwordx4 a[216:219], v0, s[76:79], s35 offen sc1\n\t"
      "v_mfma_f32_16x16x32_bf16 v[156:159], a[176:179], a[236:239], v[156:159]\n\t"
      "s_waitcnt lgkmcnt(6)\n\t"
      "v_mfma_f32_16x16x32_bf16 v[188:191], a[180:183], a[236:239], v[188:191]\n\t"
      "ds_read_b64_tr_b16 a[164:165], v19 offset:26624\n\t"
      "v_mfma_f32_16x16x32_bf16 v[184:187], a[180:183], a[232:235], v[184:187]\n\t"
      "ds_read_b64_tr_b16 a[166:167], v19 offset:30720\n\t"
      "v_mfma_f32_16x16x32_bf16 v[180:183], a[180:183], a[228:231], v[180:183]\n\t"
      "v_mfma_f32_16x16x32_bf16 v[176:179], a[180:183], a[224:227], v[176:179]\n\t"
      "s_waitcnt lgkmcnt(6)\n\t"
      "buffer_load_dwordx4 v1, s[36:39], s22 offen offset:2048 lds\n\t"
      "v_mfma_f32_16x16x32_bf16 v[112:115], a[184:187], a[224:227], v[112:115]\n\t"
      "ds_read_b64_tr_b16 a[168:169], v20 offset:26624\n\t"
      "v_mfma_f32_16x16x32_bf16 v[116:119], a[184:187], a[228:231], v[116:119]\n\t"
      "ds_read_b64_tr_b16 a[170:171], v20 offset:30720\n\t"
      "v_mfma_f32_16x16x32_bf16 v[120:123], a[184:187], a[232:235], v[120:123]\n\t"
      "s_add_u32 s64, s64, 32768\n\t"
      "s_and_b32 s64, s64, 0x1ffff\n\t"
      "buffer_load_dwordx4 a[220:223], v0, s[76:79], s35 offen offset:1024 sc1\n\t"
      "v_mfma_f32_16x16x32_bf16 v[124:127], a[184:187], a[236:239], v[124:127]\n\t"
      "s_waitcnt lgkmcnt(6)\n\t"
      "v_readlane_b32 s53, v23, 37\n\t"
      "v_mfma_f32_16x16x32_bf16 v[220:223], a[188:191], a[236:239], v[220:223]\n\t"
      "ds_read_b64_tr_b16 a[172:173], v21 offset:26624\n\t"
      "v_mfma_f32_16x16x32_bf16 v[216:219], a[188:191], a[232:235], v[216:219]\n\t"
      "ds_read_b64_tr_b16 a[174:175], v21 offset:30720\n\t"
      "v_add_u32 v0, s53, v16\n\t"
      "v_mfma_f32_16x16x32_bf16 v[212:215], a[188:191], a[228:231], v[212:215]\n\t"
      "s_waitcnt vmcnt(14)\n\t"
      "v_mfma_f32_16x16x32_bf16 v[208:211], a[188:191], a[224:227], v[208:211]\n\t"
      "s_waitcnt lgkmcnt(6)\n\t"
      "v_mfma_f32_16x16x32_bf16 v[48:51], a[160:163], a[240:243], v[48:51]\n\t"
      "ds_read_b64_tr_b16 a[176:177], v18 offset:24576\n\t"
      "buffer_load_dwordx4 v1, s[36:39], s23 offen offset:3072 lds\n\t"
      "v_mfma_f32_16x16x32_bf16 v[52:55], a[160:163], a[244:247], v[52:55]\n\t"
      "s_add_u32 s24, s24, 16384\n\t"
      "ds_read_b64_tr_b16 a[178:179], v18 offset:28672\n\t"
      "v_readlane_b32 s54, v23, 7\n\t"
      "v_mfma_f32_16x16x32_bf16 v[56:59], a[160:163], a[248:251], v[56:59]\n\t"
      "s_and_b32 s24, s24, 0x1ffff\n\t"
      "buffer_load_dwordx4 a[224:227], v0, s[76:79], s34 offen sc1\n\t"
      "v_add_u32 v18, s64, v4\n\t"
      "v_mfma_f32_16x16x32_bf16 v[60:63], a[160:163], a[252:255], v[60:63]\n\t"
      "s_waitcnt lgkmcnt(6)\n\t"
      "s_bitcmp1_b32 s54, 0\n\t"
      "s_cselect_b64 s[36:37], s[80:81], s[84:85]\n\t"
      "v_mfma_f32_16x16x32_bf16 v[92:95], a[164:167], a[252:255], v[92:95]\n\t"
      "s_cselect_b32 s38, s82, s86\n\t"
      "s_and_b32 s54, s54, -2\n\t"
      "ds_read_b64_tr_b16 a[180:181], v19 offset:24576\n\t"
      "v_mfma_f32_16x16x32_bf16 v[88:91], a[164:167], a[248:251], v[88:91]\n\t"
      "ds_read_b64_tr_b16 a[182:183], v19 offset:28672\n\t"
      "v_add_u32 v1, s54, v17\n\t"
      "v_mfma_f32_16x16x32_bf16 v[84:87], a[164:167], a[244:247], v[84:87]\n\t"
      "v_add_u32 v19, s64, v5\n\t"
      "v_mfma_f32_16x16x32_bf16 v[80:83], a[164:167], a[240:243], v[80:83]\n\t"
      "s_waitcnt lgkmcnt(6)\n\t"
      "v_mfma_f32_16x16x32_bf16 v[240:243], a[168:171], a[240:243], v[240:243]\n\t"
      "ds_read_b64_tr_b16 a[184:185], v20 offset:24576\n\t"
      "v_mfma_f32_16x16x32_bf16 v[244:247], a[168:171], a[244:247], v[244:247]\n\t"
      "ds_read_b64_tr_b16 a[186:187], v20 offset:28672\n\t"
      "v_mfma_f32_16x16x32_bf16 v[248:251], a[168:171], a[248:251], v[248:251]\n\t"
      "buffer_load_dwordx4 a[228:231], v0, s[76:79], s34 offen offset:1024 sc1\n\t"
      "v_add_u32 v20, s64, v6\n\t"
      "v_mfma_f32_16x16x32_bf16 v[252:255], a[168:171], a[252:255], v[252:255]\n\t"
      "s_waitcnt lgkmcnt(6)\n\t"
      "v_mfma_f32_16x16x32_bf16 v[236:239], a[172:175], a[252:255], v[236:239]\n\t"
      "ds_read_b64_tr_b16 a[188:189], v21 offset:24576\n\t"
      "v_mfma_f32_16x16x32_bf16 v[232:235], a[172:175], a[248:251], v[232:235]\n\t"
      "ds_read_b64_tr_b16 a[190:191], v21 offset:28672\n\t"
      "v_mfma_f32_16x16x32_bf16 v[228:231], a[172:175], a[244:247], v[228:231]\n\t"
      "v_add_u32 v21, s64, v7\n\t"
      "v_mfma_f32_16x16x32_bf16 v[224:227], a[172:175], a[240:243], v[224:227]\n\t"
      "s_waitcnt lgkmcnt(6)\n\t"
      "v_mfma_f32_16x16x32_bf16 v[144:147], a[176:179], a[240:243], v[144:147]\n\t"
      "s_waitcnt vmcnt(29)\n\t"
      "s_barrier\n\t"
      "ds_read_b64_tr_b16 a[160:161], v18 offset:2048\n\t"
      "v_mfma_f32_16x16x32_bf16 v[148:151], a[176:179], a[244:247], v[148:151]\n\t"
      "ds_read_b64_tr_b16 a[162:163], v18 offset:6144\n\t"
      "v_mfma_f32_16x16x32_bf16 v[152:155], a[176:179], a[248:251], v[152:155]\n\t"
      "buffer_load_dwordx4 a[232:235], v0, s[76:79], s35 offen sc1\n\t"
      "v_mfma_f32_16x16x32_bf16 v[156:159], a[176:179], a[252:255], v[156:159]\n\t"
      "s_waitcnt lgkmcnt(6)\n\t"
      "v_mfma_f32_16x16x32_bf16 v[188:191], a[180:183], a[252:255], v[188:191]\n\t"
      "ds_read_b64_tr_b16 a[164:165], v19 offset:2048\n\t"
      "v_mfma_f32_16x16x32_bf16 v[184:187], a[180:183], a[248:251], v[184:187]\n\t"
      "ds_read_b64_tr_b16 a[166:167], v19 offset:6144\n\t"
      "v_mfma_f32_16x16x32_bf16 v[180:183], a[180:183], a[244:247], v[180:183]\n\t"
      "s_waitcnt vmcnt(14)\n\t"
      "v_cmp_eq_u64 vcc, s[8:9], v[2:3]\n\t"
      "s_nop 0\n\t"
      "s_cmp_eq_u64 vcc, -1\n\t"
      "s_cbranch_scc1 Lpd16_%=\n\t"
      "s_mov_b32 s10, 200000\n\t"
      "Lp16_%=:\n\t"
      "buffer_load_dwordx2 v[2:3], v27, s[96:99], 0 offen offset:0 sc1\n\t"
      "s_waitcnt vmcnt(0)\n\t"
      "v_cmp_eq_u64 vcc, s[8:9], v[2:3]\n\t"
      "s_nop 0\n\t"
      "s_cmp_eq_u64 vcc, -1\n\t"
      "s_cbranch_scc1 Lpd16_%=\n\t"
      "s_sleep 2\n\t"
      "s_sub_u32 s10, s10, 1\n\t"
      "s_cmp_lg_u32 s10, 0\n\t"
      "s_cbranch_scc1 Lp16_%=\n\t"
      "Lpd16_%=:\n\t"
      STAMP(10, 11)
      "v_mfma_f32_16x16x32_bf16 v[176:179], a[180:183], a[240:243], v[176:179]\n\t"
      "s_waitcnt lgkmcnt(6)\n\t"
      "v_mfma_f32_16x16x32_bf16 v[112:115], a[184:187], a[240:243], v[112:115]\n\t"
      "ds_read_b64_tr_b16 a[168:169], v20 offset:2048\n\t"
      "v_mfma_f32_16x16x32_bf16 v[116:119], a[184:187], a[244:247], v[116:119]\n\t"
      "ds_read_b64_tr_b16 a[170:171], v20 offset:6144\n\t"
      "v_mfma_f32_16x16x32_bf16 v[120:123], a[184:187], a[248:251], v[120:123]\n\t"
      "buffer_load_dwordx4 a[236:239], v0, s[76:79], s35 offen offset:1024 sc1\n\t"
      "v_mfma_f32_16x16x32_bf16 v[124:127], a[184:187], a[252:255], v[124:127]\n\t"
      "s_waitcnt lgkmcnt(6)\n\t"
      "v_add_u32 v0, s51, v0\n\t"
      "v_mfma_f32_16x16x32_bf16 v[220:223], a[188:191], a[252:255], v[220:223]\n\t"
      "ds_read_b64_tr_b16 a[172:173], v21 offset:2048\n\t"
      "v_mfma_f32_16x16x32_bf16 v[216:219], a[188:191], a[248:251], v[216:219]\n\t"
      "ds_read_b64_tr_b16 a[174:175], v21 offset:6144\n\t"
      "v_mfma_f32_16x16x32_bf16 v[212:215], a[188:191], a[244:247], v[212:215]\n\t"
      "s_waitcnt vmcnt(12)\n\t"
      "v_mfma_f32_16x16x32_bf16 v[208:211], a[188:191], a[240:243], v[208:211]\n\t"
      "s_waitcnt lgkmcnt(6)\n\t"
      LSTW(10, "s_waitcnt lgkmcnt(0)\n\t")
      "s_branch Lg2_%=\n\t"
      "Lqkv_%=:\n\t"
      "s_mov_b32 s64, 1\n\t"
      "s_mov_b32 s66, -98304\n\t"
      "s_mov_b32 s67, 24576\n\t"
      "s_mov_b32 s68, 7\n\t"
      "s_add_u32 s60, s36, 33554432\n\t"
      "s_addc_u32 s61, s37, 0\n\t"
      "s_mov_b32 s62, 16777216\n\t"
      "v_mov_b32 v14, 0\n\t"
      "v_mov_b32 v11, s80\n\t"
      "v_mov_b32 v13, s81\n\t"
      "s_cmp_lg_u32 s58, 0\n\t"
      "s_cselect_b32 s3, 2, 1\n\t"
      "s_cselect_b32 s5, 4, 6\n\t"
      "s_lshl_b32 s3, s3, 20\n\t"
      "s_lshl_b32 s5, s5, 20\n\t"
      "s_add_u32 s10, s40, s3\n\t"
      "s_addc_u32 s11, s41, 0\n\t"
      "v_writelane_b32 v11, s10, 0\n\t"
      "v_writelane_b32 v13, s11, 0\n\t"
      "v_writelane_b32 v14, s5, 0\n\t"
      "s_add_u32 s10, s10, 1048576\n\t"
      "s_addc_u32 s11, s11, 0\n\t"
      "s_sub_u32 s5, s5, 1048576\n\t"
      "v_writelane_b32 v11, s10, 1\n\t"
      "v_writelane_b32 v13, s11, 1\n\t"
      "v_writelane_b32 v14, s5, 1\n\t"
      "s_add_u32 s10, s10, 1048576\n\t"
      "s_addc_u32 s11, s11, 0\n\t"
      "s_sub_u32 s5, s5, 1048576\n\t"
      "v_writelane_b32 v11, s10, 2\n\t"
      "v_writelane_b32 v13, s11, 2\n\t"
      "v_writelane_b32 v14, s5, 2\n\t"
      "s_add_u32 s10, s10, 1048576\n\t"
      "s_addc_u32 s11, s11, 0\n\t"
      "s_sub_u32 s5, s5, 1048576\n\t"
      "v_writelane_b32 v11, s10, 3\n\t"
      "v_writelane_b32 v13, s11, 3\n\t"
      "v_writelane_b32 v14, s5, 3\n\t"
      "s_add_u32 s10, s10, 1048576\n\t"
      "s_addc_u32 s11, s11, 0\n\t"
      "s_sub_u32 s5, s5, 1048576\n\t"
      "v_writelane_b32 v11, s10, 4\n\t"
      "v_writelane_b32 v13, s11, 4\n\t"
      "v_writelane_b32 v14, s5, 4\n\t"
      "v_writelane_b32 v14, s82, 5\n\t"
      "s_add_u32 s10, s84, 0\n\t"
      "s_addc_u32 s11, s85, 0\n\t"
      "s_mov_b32 s5, 7340032\n\t"
      "v_writelane_b32 v11, s10, 6\n\t"
      "v_writelane_b32 v13, s11, 6\n\t"
      "v_writelane_b32 v14, s5, 6\n\t"
      "s_add_u32 s10, s84, 2097152\n\t"
      "s_addc_u32 s11, s85, 0\n\t"
      "s_mov_b32 s5, 5242880\n\t"
      "v_writelane_b32 v11, s10, 7\n\t"
      "v_writelane_b32 v13, s11, 7\n\t"
      "v_writelane_b32 v14, s5, 7\n\t"
      "s_add_u32 s10, s84, 4194304\n\t"
      "s_addc_u32 s11, s85, 0\n\t"
      "s_mov_b32 s5, 3145728\n\t"
      "v_writelane_b32 v11, s10, 8\n\t"
      "v_writelane_b32 v13, s11, 8\n\t"
      "v_writelane_b32 v14, s5, 8\n\t"
      "s_add_u32 s10, s84, 6291456\n\t"
      "s_addc_u32 s11, s85, 0\n\t"
      "s_mov_b32 s5, 1048576\n\t"
      "v_writelane_b32 v11, s10, 9\n\t"
      "v_writelane_b32 v13, s11, 9\n\t"
      "v_writelane_b32 v14, s5, 9\n\t"
      "s_lshl2_add_u32 s3, s4, s32\n\t"
      "s_lshl_b32 s3, s3, 11\n\t"
      "s_lshl2_add_u32 s5, s2, s32\n\t"
      "s_lshl_b32 s5, s5, 11\n\t"
      "s_sub_u32 s5, s5, s3\n\t"
      "s_sub_u32 s10, s12, s5\n\t"
      "s_subb_u32 s11, s13, 0\n\t"
      "s_add_u32 s5, s5, s14\n\t"
      "v_writelane_b32 v11, s10, 10\n\t"
      "v_writelane_b32 v13, s11, 10\n\t"
      "v_writelane_b32 v14, s5, 10\n\t"
      "s_mov_b32 s14, 0\n\t"
      "s_mov_b32 s19, 0\n\t"
      "s_add_u32 s57, s53, 4096\n\t"
      "s_sub_u32 s40, s40, 4096\n\t"
      "s_subb_u32 s41, s41, 0\n\t"
      "s_add_u32 s42, s42, 4096\n\t"
      "v_mov_b64 v[32:33], 0\n\t"
      "v_mov_b64 v[34:35], 0\n\t"
      "v_mov_b64 v[36:37], 0\n\t"
      "v_mov_b64 v[38:39], 0\n\t"
      "v_mov_b64 v[40:41], 0\n\t"
      "v_mov_b64 v[42:43], 0\n\t"
      "v_mov_b64 v[44:45], 0\n\t"
      "v_mov_b64 v[46:47], 0\n\t"
      "v_mov_b64 v[48:49], 0\n\t"
      "v_mov_b64 v[50:51], 0\n\t"
      "v_mov_b64 v[52:53], 0\n\t"
      "v_mov_b64 v[54:55], 0\n\t"
      "v_mov_b64 v[56:57], 0\n\t"
      "v_mov_b64 v[58:59], 0\n\t"
      "v_mov_b64 v[60:61], 0\n\t"
      "v_mov_b64 v[62:63], 0\n\t"
      "v_mov_b64 v[64:65], 0\n\t"
      "v_mov_b64 v[66:67], 0\n\t"
      "v_mov_b64 v[68:69], 0\n\t"
      "v_mov_b64 v[70:71], 0\n\t"
      "v_mov_b64 v[72:73], 0\n\t"
      "v_mov_b64 v[74:75], 0\n\t"
      "v_mov_b64 v[76:77], 0\n\t"
      "v_mov_b64 v[78:79], 0\n\t"
      "v_mov_b64 v[80:81], 0\n\t"
      "v_mov_b64 v[82:83], 0\n\t"
      "v_mov_b64 v[84:85], 0\n\t"
      "v_mov_b64 v[86:87], 0\n\t"
      "v_mov_b64 v[88:89], 0\n\t"
      "v_mov_b64 v[90:91], 0\n\t"
      "v_mov_b64 v[92:93], 0\n\t"
      "v_mov_b64 v[94:95], 0\n\t"
      "v_mov_b64 v[96:97], 0\n\t"
      "v_mov_b64 v[98:99], 0\n\t"
      "v_mov_b64 v[100:101], 0\n\t"
      "v_mov_b64 v[102:103], 0\n\t"
      "v_mov_b64 v[104:105], 0\n\t"
      "v_mov_b64 v[106:107], 0\n\t"
      "v_mov_b64 v[108:109], 0\n\t"
      "v_mov_b64 v[110:111], 0\n\t"
      "v_mov_b64 v[112:113], 0\n\t"
      "v_mov_b64 v[114:115], 0\n\t"
      "v_mov_b64 v[116:117], 0\n\t"
      "v_mov_b64 v[118:119], 0\n\t"
      "v_mov_b64 v[120:121], 0\n\t"
      "v_mov_b64 v[122:123], 0\n\t"
      "v_mov_b64 v[124:125], 0\n\t"
      "v_mov_b64 v[126:127], 0\n\t"
      "v_mov_b64 v[128:129], 0\n\t"
      "v_mov_b64 v[130:131], 0\n\t"
      "v_mov_b64 v[132:133], 0\n\t"
      "v_mov_b64 v[134:135], 0\n\t"
      "v_mov_b64 v[136:137], 0\n\t"
      "v_mov_b64 v[138:139], 0\n\t"
      "v_mov_b64 v[140:141], 0\n\t"
      "v_mov_b64 v[142:143], 0\n\t"
      "v_mov_b64 v[144:145], 0\n\t"
      "v_mov_b64 v[146:147], 0\n\t"
      "v_mov_b64 v[148:149], 0\n\t"
      "v_mov_b64 v[150:151], 0\n\t"
      "v_mov_b64 v[152:153], 0\n\t"
      "v_mov_b64 v[154:155], 0\n\t"
      "v_mov_b64 v[156:157], 0\n\t"
      "v_mov_b64 v[158:159], 0\n\t"
      "v_mov_b64 v[160:161], 0\n\t"
      "v_mov_b64 v[162:163], 0\n\t"
      "v_mov_b64 v[164:165], 0\n\t"
      "v_mov_b64 v[166:167], 0\n\t"
      "v_mov_b64 v[168:169], 0\n\t"
      "v_mov_b64 v[170:171], 0\n\t"
      "v_mov_b64 v[172:173], 0\n\t"
      "v_mov_b64 v[174:175], 0\n\t"
      "v_mov_b64 v[176:177], 0\n\t"
      "v_mov_b64 v[178:179], 0\n\t"
      "v_mov_b64 v[180:181], 0\n\t"
      "v_mov_b64 v[182:183], 0\n\t"
      "v_mov_b64 v[184:185], 0\n\t"
      "v_mov_b64 v[186:187], 0\n\t"
      "v_mov_b64 v[188:189], 0\n\t"
      "v_mov_b64 v[190:191], 0\n\t"
      "v_mov_b64 v[192:193], 0\n\t"
      "v_mov_b64 v[194:195], 0\n\t"
      "v_mov_b64 v[196:197], 0\n\t"
      "v_mov_b64 v[198:199], 0\n\t"
      "v_mov_b64 v[200:201], 0\n\t"
      "v_mov_b64 v[202:203], 0\n\t"
      "s_waitcnt vmcnt(0)\n\t"
      "s_barrier\n\t"
      "s_cmp_lg_u32 s32, 0\n\t"
      "s_cbranch_scc1 Lf24_%=\n\t"
      "v_mov_b32 v248, s8\n\t"
      "v_mov_b32 v249, s9\n\t"
      "s_mov_b64 exec, 1\n\t"
      "buffer_store_dwordx2 v[248:249], off, s[96:99], s59 sc1\n\t"
      "s_mov_b64 exec, -1\n\t"
      "Lf24_%=:\n\t"
      STAMP(2, 3)
      "s_mov_b32 m0, s53\n\t"
      "s_nop 0\n\t"
      "buffer_load_dwordx4 v2, s[40:43], s44 offen lds\n\t"
      "buffer_load_dwordx4 v2, s[40:43], s45 offen offset:1024 lds\n\t"
      "buffer_load_dwordx4 v2, s[40:43], s46 offen offset:2048 lds\n\t"
      "buffer_load_dwordx4 v2, s[40:43], s47 offen offset:3072 lds\n\t"
      "s_mov_b32 m0, s57\n\t"
      "s_nop 0\n\t"
      "buffer_load_dwordx4 v2, s[40:43], s48 offen lds\n\t"
      "buffer_load_dwordx4 v2, s[40:43], s49 offen offset:1024 lds\n\t"
      "s_add_u32 s53, s53, 24576\n\t"
      "s_cmp_eq_u32 s53, s55\n\t"
      "v_add_u32 v2, s52, v2\n\t"
      "s_cselect_b32 s53, s54, s53\n\t"
      "s_add_u32 s57, s53, 4096\n\t"
      "s_mov_b32 m0, s53\n\t"
      "s_nop 0\n\t"
      "buffer_load_dwordx4 v2, s[40:43], s44 offen lds\n\t"
      "buffer_load_dwordx4 v2, s[40:43], s45 offen offset:1024 lds\n\t"
      "buffer_load_dwordx4 v2, s[40:43], s46 offen offset:2048 lds\n\t"
      "buffer_load_dwordx4 v2, s[40:43], s47 offen offset:3072 lds\n\t"
      "s_mov_b32 m0, s57\n\t"
      "s_nop 0\n\t"
      LSTW(2, "")
      "buffer_load_dwordx4 v2, s[40:43], s48 offen lds\n\t"
      "buffer_load_dwordx4 v2, s[40:43], s49 offen offset:1024 lds\n\t"
      "s_add_u32 s53, s53, 24576\n\t"
      "s_cmp_eq_u32 s53, s55\n\t"
      "v_add_u32 v2, s52, v2\n\t"
      "s_cselect_b32 s53, s54, s53\n\t"
      "s_add_u32 s57, s53, 4096\n\t"
      "s_cmp_lg_u32 s32, 0\n\t"
      "s_cbranch_scc1 Lw25_%=\n\t"
      "s_mov_b32 s10, 200000\n\t"
      "Lp26_%=:\n\t"
      "buffer_load_dwordx2 v[248:249], v26, s[96:99], 0 offen offset:0 sc1\n\t"
      "s_waitcnt vmcnt(0)\n\t"
      "v_cmp_eq_u64 vcc, s[8:9], v[248:249]\n\t"
      "s_nop 0\n\t"
      "s_cmp_eq_u64 vcc, -1\n\t"
      "s_cbranch_scc1 Lpd26_%=\n\t"
      "s_sleep 2\n\t"
      "s_sub_u32 s10, s10, 1\n\t"
      "s_cmp_lg_u32 s10, 0\n\t"
      "s_cbranch_scc1 Lp26_%=\n\t"
      "Lpd26_%=:\n\t"
      "Lw25_%=:\n\t"
      "s_waitcnt vmcnt(6)\n\t"
      "s_barrier\n\t"
      STAMP(4, 5)
      "buffer_load_dwordx4 a[64:67], v0, s[36:39], 0 offen sc1\n\t"
      "buffer_load_dword v12, off, s[12:15], 0\n\t"
      "buffer_load_dwordx4 a[68:71], v0, s[36:39], 0 offen offset:1024 sc1\n\t"
      "buffer_load_dwordx4 a[72:75], v0, s[36:39], 0 offen offset:2048 sc1\n\t"
      "buffer_load_dwordx4 a[76:79], v0, s[36:39], 0 offen offset:3072 sc1\n\t"
      "buffer_load_dwordx4 a[80:83], v1, s[36:39], 0 offen sc1\n\t"
      "v_add_u32 v0, s51, v0\n\t"
      "v_add_u32 v1, s51, v1\n\t"
      "buffer_load_dwordx4 a[84:87], v0, s[36:39], 0 offen sc1\n\t"
      "buffer_load_dword v12, off, s[12:15], 0\n\t"
      "buffer_load_dwordx4 a[88:91], v0, s[36:39], 0 offen offset:1024 sc1\n\t"
      "buffer_load_dword v12, off, s[12:15], 0\n\t"
      "buffer_load_dwordx4 a[92:95], v0, s[36:39], 0 offen offset:2048 sc1\n\t"
      "buffer_load_dword v12, off, s[12:15], 0\n\t"
      "buffer_load_dwordx4 a[96:99], v0, s[36:39], 0 offen offset:3072 sc1\n\t"
      "buffer_load_dword v12, off, s[12:15], 0\n\t"
      "buffer_load_dwordx4 a[100:103], v1, s[36:39], 0 offen sc1\n\t"
      "v_add_u32 v0, s51, v0\n\t"
      "v_add_u32 v1, s51, v1\n\t"
      "buffer_load_dword v12, off, s[12:15], 0\n\t"
      "buffer_load_dwordx4 a[104:107], v0, s[36:39], 0 offen sc1\n\t"
      "buffer_load_dword v12, off, s[12:15], 0\n\t"
      "buffer_load_dwordx4 a[108:111], v0, s[36:39], 0 offen offset:1024 sc1\n\t"
      LSTW(4, "")
      "buffer_load_dwordx4 a[112:115], v0, s[36:39], 0 offen offset:2048 sc1\n\t"
      "buffer_load_dwordx4 a[116:119], v0, s[36:39], 0 offen offset:3072 sc1\n\t"
      "buffer_load_dwordx4 a[120:123], v1, s[36:39], 0 offen sc1\n\t"
      "v_add_u32 v0, s51, v0\n\t"
      "v_add_u32 v1, s51, v1\n\t"
      "ds_read_b64_tr_b16 a[0:1], v4 offset:0\n\t"
      "ds_read_b64_tr_b16 a[2:3], v4 offset:6144\n\t"
      "ds_read_b64_tr_b16 a[4:5], v5 offset:0\n\t"
      "ds_read_b64_tr_b16 a[6:7], v5 offset:6144\n\t"
      "ds_read_b64_tr_b16 a[8:9], v6 offset:0\n\t"
      "ds_read_b64_tr_b16 a[10:11], v6 offset:6144\n\t"
      "ds_read_b64_tr_b16 a[12:13], v7 offset:0\n\t"
      "ds_read_b64_tr_b16 a[14:15], v7 offset:6144\n\t"
      "v_mov_b64 v[204:205], 0\n\t"
      "v_mov_b64 v[206:207], 0\n\t"
      "v_mov_b64 v[208:209], 0\n\t"
      "v_mov_b64 v[210:211], 0\n\t"
      "v_mov_b64 v[212:213], 0\n\t"
      "v_mov_b64 v[214:215], 0\n\t"
      "v_mov_b64 v[216:217], 0\n\t"
      "v_mov_b64 v[218:219], 0\n\t"
      "v_mov_b64 v[220:221], 0\n\t"
      "v_mov_b64 v[222:223], 0\n\t"
      "v_mov_b64 v[224:225], 0\n\t"
      "v_mov_b64 v[226:227], 0\n\t"
      "v_mov_b64 v[228:229], 0\n\t"
      "v_mov_b64 v[230:231], 0\n\t"
      STAMP(20, 21)
      "s_waitcnt vmcnt(16)\n\t"
      "s_waitcnt lgkmcnt(6)\n\t"
      LSTW(20, "s_waitcnt lgkmcnt(0)\n\t")
      "Lg1q_%=:\n\t"
      "v_mfma_f32_16x16x32_bf16 v[32:35], a[0:3], a[64:67], v[32:35]\n\t"
      "ds_read_b64_tr_b16 a[16:17], v8 offset:2048\n\t"
      "v_mfma_f32_16x16x32_bf16 v[36:39], a[0:3], a[68:71], v[36:39]\n\t"
      "ds_read_b64_tr_b16 a[18:19], v8 offset:8192\n\t"
      "v_mfma_f32_16x16x32_bf16 v[40:43], a[0:3], a[72:75], v[40:43]\n\t"
      "ds_read_b64_tr_b16 a[20:21], v4 offset:2048\n\t"
      "buffer_load_dwordx4 a[124:127], v0, s[36:39], 0 offen sc1\n\t"
      "v_mfma_f32_16x16x32_bf16 v[44:47], a[0:3], a[76:79], v[44:47]\n\t"
      "s_waitcnt lgkmcnt(7)\n\t"
      "s_mov_b32 m0, s53\n\t"
      "ds_read_b64_tr_b16 a[22:23], v4 offset:8192\n\t"
      "v_mfma_f32_16x16x32_bf16 v[60:63], a[4:7], a[76:79], v[60:63]\n\t"
      "buffer_load_dwordx4 v2, s[40:43], s44 offen lds\n\t"
      "v_mfma_f32_16x16x32_bf16 v[56:59], a[4:7], a[72:75], v[56:59]\n\t"
      "ds_read_b64_tr_b16 a[24:25], v5 offset:2048\n\t"
      "v_mfma_f32_16x16x32_bf16 v[52:55], a[4:7], a[68:71], v[52:55]\n\t"
      "ds_read_b64_tr_b16 a[26:27], v5 offset:8192\n\t"
      "v_mfma_f32_16x16x32_bf16 v[48:51], a[4:7], a[64:67], v[48:51]\n\t"
      "s_waitcnt lgkmcnt(8)\n\t"
      "v_mfma_f32_16x16x32_bf16 v[64:67], a[8:11], a[64:67], v[64:67]\n\t"
      "v_mfma_f32_16x16x32_bf16 v[68:71], a[8:11], a[68:71], v[68:71]\n\t"
      "ds_read_b64_tr_b16 a[28:29], v6 offset:2048\n\t"
      "v_mfma_f32_16x16x32_bf16 v[72:75], a[8:11], a[72:75], v[72:75]\n\t"
      "ds_read_b64_tr_b16 a[30:31], v6 offset:8192\n\t"
      "v_mfma_f32_16x16x32_bf16 v[76:79], a[8:11], a[76:79], v[76:79]\n\t"
      "s_waitcnt lgkmcnt(8)\n\t"
      "v_mfma_f32_16x16x32_bf16 v[92:95], a[12:15], a[76:79], v[92:95]\n\t"
      "buffer_load_dwordx4 a[128:131], v0, s[36:39], 0 offen offset:1024 sc1\n\t"
      "v_mfma_f32_16x16x32_bf16 v[88:91], a[12:15], a[72:75], v[88:91]\n\t"
      "ds_read_b64_tr_b16 a[0:1], v7 offset:2048\n\t"
      "v_mfma_f32_16x16x32_bf16 v[84:87], a[12:15], a[68:71], v[84:87]\n\t"
      "ds_read_b64_tr_b16 a[2:3], v7 offset:8192\n\t"
      "v_mfma_f32_16x16x32_bf16 v[80:83], a[12:15], a[64:67], v[80:83]\n\t"
      "s_waitcnt lgkmcnt(8)\n\t"
      "buffer_load_dwordx4 v2, s[40:43], s45 offen offset:1024 lds\n\t"
      "v_mfma_f32_16x16x32_bf16 v[224:227], a[16:19], a[80:83], v[224:227]\n\t"
      "s_waitcnt lgkmcnt(6)\n\t"
      "v_mfma_f32_16x16x32_bf16 v[96:99], a[20:23], a[64:67], v[96:99]\n\t"
      "ds_read_b64_tr_b16 a[4:5], v4 offset:4096\n\t"
      "v_mfma_f32_16x16x32_bf16 v[100:103], a[20:23], a[68:71], v[100:103]\n\t"
      "ds_read_b64_tr_b16 a[6:7], v4 offset:10240\n\t"
      "v_mfma_f32_16x16x32_bf16 v[104:107], a[20:23], a[72:75], v[104:107]\n\t"
      "v_mfma_f32_16x16x32_bf16 v[108:111], a[20:23], a[76:79], v[108:111]\n\t"
      "s_waitcnt lgkmcnt(6)\n\t"
      "v_mfma_f32_16x16x32_bf16 v[124:127], a[24:27], a[76:79], v[124:127]\n\t"
      "ds_read_b64_tr_b16 a[8:9], v8 offset:4096\n\t"
      "v_mfma_f32_16x16x32_bf16 v[120:123], a[24:27], a[72:75], v[120:123]\n\t"
      "ds_read_b64_tr_b16 a[10:11], v8 offset:10240\n\t"
      "buffer_load_dwordx4 a[132:135], v0, s[36:39], 0 offen offset:2048 sc1\n\t"
      "v_mfma_f32_16x16x32_bf16 v[116:119], a[24:27], a[68:71], v[116:119]\n\t"
      "ds_read_b64_tr_b16 a[12:13], v5 offset:4096\n\t"
      "v_mfma_f32_16x16x32_bf16 v[112:115], a[24:27], a[64:67], v[112:115]\n\t"
      "s_waitcnt lgkmcnt(7)\n\t"
      "ds_read_b64_tr_b16 a[14:15], v5 offset:10240\n\t"
      "v_mfma_f32_16x16x32_bf16 v[128:131], a[28:31], a[64:67], v[128:131]\n\t"
      "v_mfma_f32_16x16x32_bf16 v[132:135], a[28:31], a[68:71], v[132:135]\n\t"
      "ds_read_b64_tr_b16 a[16:17], v6 offset:4096\n\t"
      "buffer_load_dwordx4 v2, s[40:43], s46 offen offset:2048 lds\n\t"
      "v_mfma_f32_16x16x32_bf16 v[136:139], a[28:31], a[72:75], v[136:139]\n\t"
      "ds_read_b64_tr_b16 a[18:19], v6 offset:10240\n\t"
      "v_mfma_f32_16x16x32_bf16 v[140:143], a[28:31], a[76:79], v[140:143]\n\t"
      "s_waitcnt lgkmcnt(8)\n\t"
      "v_mfma_f32_16x16x32_bf16 v[156:159], a[0:3], a[76:79], v[156:159]\n\t"
      "v_mfma_f32_16x16x32_bf16 v[152:155], a[0:3], a[72:75], v[152:155]\n\t"
      "ds_read_b64_tr_b16 a[20:21], v7 offset:4096\n\t"
      "v_mfma_f32_16x16x32_bf16 v[148:151], a[0:3], a[68:71], v[148:151]\n\t"
      "ds_read_b64_tr_b16 a[22:23], v7 offset:10240\n\t"
      "v_mfma_f32_16x16x32_bf16 v[144:147], a[0:3], a[64:67], v[144:147]\n\t"
      "s_waitcnt lgkmcnt(8)\n\t"
      "buffer_load_dwordx4 a[136:139], v0, s[36:39], 0 offen offset:3072 sc1\n\t"
      "v_mfma_f32_16x16x32_bf16 v[160:163], a[64:67], a[4:7], v[160:163]\n\t"
      "v_mfma_f32_16x16x32_bf16 v[164:167], a[68:71], a[4:7], v[164:167]\n\t"
      "ds_read_b64_tr_b16 a[24:25], v4 offset:12288\n\t"
      "v_mfma_f32_16x16x32_bf16 v[168:171], a[72:75], a[4:7], v[168:171]\n\t"
      "ds_read_b64_tr_b16 a[26:27], v4 offset:18432\n\t"
      "v_mfma_f32_16x16x32_bf16 v[172:175], a[76:79], a[4:7], v[172:175]\n\t"
      "s_waitcnt lgkmcnt(8)\n\t"
      "v_mfma_f32_16x16x32_bf16 v[228:231], a[80:83], a[8:11], v[228:231]\n\t"
      "s_waitcnt lgkmcnt(6)\n\t"
      "v_mfma_f32_16x16x32_bf16 v[188:191], a[76:79], a[12:15], v[188:191]\n\t"
      "ds_read_b64_tr_b16 a[28:29], v5 offset:12288\n\t"
      "buffer_load_dwordx4 v2, s[40:43], s47 offen offset:3072 lds\n\t"
      "v_mfma_f32_16x16x32_bf16 v[184:187], a[72:75], a[12:15], v[184:187]\n\t"
      "ds_read_b64_tr_b16 a[30:31], v5 offset:18432\n\t"
      "v_mfma_f32_16x16x32_bf16 v[180:183], a[68:71], a[12:15], v[180:183]\n\t"
      "v_mfma_f32_16x16x32_bf16 v[176:179], a[64:67], a[12:15], v[176:179]\n\t"
      "s_waitcnt lgkmcnt(6)\n\t"
      "v_mfma_f32_16x16x32_bf16 v[192:195], a[64:67], a[16:19], v[192:195]\n\t"
      "ds_read_b64_tr_b16 a[0:1], v6 offset:12288\n\t"
      "buffer_load_dwordx4 a[140:143], v1, s[36:39], 0 offen sc1\n\t"
      "v_mfma_f32_16x16x32_bf16 v[196:199], a[68:71], a[16:19], v[196:199]\n\t"
      "ds_read_b64_tr_b16 a[2:3], v6 offset:18432\n\t"
      "v_add_u32 v0, s51, v0\n\t"
      "v_mfma_f32_16x16x32_bf16 v[200:203], a[72:75], a[16:19], v[200:203]\n\t"
      "v_add_u32 v1, s51, v1\n\t"
      "v_mfma_f32_16x16x32_bf16 v[204:207], a[76:79], a[16:19], v[204:207]\n\t"
      "s_waitcnt lgkmcnt(6)\n\t"
      "v_mfma_f32_16x16x32_bf16 v[220:223], a[76:79], a[20:23], v[220:223]\n\t"
      "ds_read_b64_tr_b16 a[4:5], v7 offset:12288\n\t"
      "v_mfma_f32_16x16x32_bf16 v[216:219], a[72:75], a[20:23], v[216:219]\n\t"
      "ds_read_b64_tr_b16 a[6:7], v7 offset:18432\n\t"
      "v_mfma_f32_16x16x32_bf16 v[212:215], a[68:71], a[20:23], v[212:215]\n\t"
      "s_waitcnt vmcnt(16)\n\t"
      "s_mov_b32 m0, s57\n\t"
      "v_mfma_f32_16x16x32_bf16 v[208:211], a[64:67], a[20:23], v[208:211]\n\t"
      "s_waitcnt lgkmcnt(6)\n\t"
      "buffer_load_dwordx4 v2, s[40:43], s48 offen lds\n\t"
      "v_mfma_f32_16x16x32_bf16 v[32:35], a[24:27], a[84:87], v[32:35]\n\t"
      "ds_read_b64_tr_b16 a[8:9], v8 offset:14336\n\t"
      "v_mfma_f32_16x16x32_bf16 v[36:39], a[24:27], a[88:91], v[36:39]\n\t"
      "ds_read_b64_tr_b16 a[10:11], v8 offset:20480\n\t"
      "v_mfma_f32_16x16x32_bf16 v[40:43], a[24:27], a[92:95], v[40:43]\n\t"
      "ds_read_b64_tr_b16 a[12:13], v4 offset:14336\n\t"
      "buffer_load_dwordx4 a[64:67], v0, s[36:39], 0 offen sc1\n\t"
      "v_mfma_f32_16x16x32_bf16 v[44:47], a[24:27], a[96:99], v[44:47]\n\t"
      "s_waitcnt lgkmcnt(7)\n\t"
      "ds_read_b64_tr_b16 a[14:15], v4 offset:20480\n\t"
      "v_mfma_f32_16x16x32_bf16 v[60:63], a[28:31], a[96:99], v[60:63]\n\t"
      "v_mfma_f32_16x16x32_bf16 v[56:59], a[28:31], a[92:95], v[56:59]\n\t"
      "ds_read_b64_tr_b16 a[16:17], v5 offset:14336\n\t"
      "v_mfma_f32_16x16x32_bf16 v[52:55], a[28:31], a[88:91], v[52:55]\n\t"
      "ds_read_b64_tr_b16 a[18:19], v5 offset:20480\n\t"
      "v_mfma_f32_16x16x32_bf16 v[48:51], a[28:31], a[84:87], v[48:51]\n\t"
      "s_waitcnt lgkmcnt(8)\n\t"
      "v_mfma_f32_16x16x32_bf16 v[64:67], a[0:3], a[84:87], v[64:67]\n\t"
      "v_mfma_f32_16x16x32_bf16 v[68:71], a[0:3], a[88:91], v[68:71]\n\t"
      "ds_read_b64_tr_b16 a[20:21], v6 offset:14336\n\t"
      "v_mfma_f32_16x16x32_bf16 v[72:75], a[0:3], a[92:95], v[72:75]\n\t"
      "ds_read_b64_tr_b16 a[22:23], v6 offset:20480\n\t"
      "buffer_load_dwordx4 v2, s[40:43], s49 offen offset:1024 lds\n\t"
      "v_mfma_f32_16x16x32_bf16 v[76:79], a[0:3], a[96:99], v[76:79]\n\t"
      "s_waitcnt lgkmcnt(8)\n\t"
      "s_add_u32 s53, s53, 24576\n\t"
      "v_add_u32 v2, s52, v2\n\t"
      "v_mfma_f32_16x16x32_bf16 v[92:95], a[4:7], a[96:99], v[92:95]\n\t"
      "s_cmp_eq_u32 s53, s55\n\t"
      "s_cselect_b32 s53, s54, s53\n\t"
      "buffer_load_dwordx4 a[68:71], v0, s[36:39], 0 offen offset:1024 sc1\n\t"
      "v_mfma_f32_16x16x32_bf16 v[88:91], a[4:7], a[92:95], v[88:91]\n\t"
      "s_add_u32 s57, s53, 4096\n\t"
      "s_add_u32 s64, s64, 1\n\t"
      "ds_read_b64_tr_b16 a[24:25], v7 offset:14336\n\t"
      "v_mfma_f32_16x16x32_bf16 v[84:87], a[4:7], a[88:91], v[84:87]\n\t"
      "s_cmp_eq_u32 s64, 5\n\t"
      "s_cselect_b32 s65, s66, s67\n\t"
      "ds_read_b64_tr_b16 a[26:27], v7 offset:20480\n\t"
      "v_mfma_f32_16x16x32_bf16 v[80:83], a[4:7], a[84:87], v[80:83]\n\t"
      "s_waitcnt lgkmcnt(8)\n\t"
      "s_cselect_b32 s64, 0, s64\n\t"
      "v_mfma_f32_16x16x32_bf16 v[224:227], a[8:11], a[100:103], v[224:227]\n\t"
      "s_waitcnt lgkmcnt(6)\n\t"
      "v_mfma_f32_16x16x32_bf16 v[96:99], a[12:15], a[84:87], v[96:99]\n\t"
      "ds_read_b64_tr_b16 a[28:29], v4 offset:16384\n\t"
      "v_mfma_f32_16x16x32_bf16 v[100:103], a[12:15], a[88:91], v[100:103]\n\t"
      "ds_read_b64_tr_b16 a[30:31], v4 offset:22528\n\t"
      "v_mfma_f32_16x16x32_bf16 v[104:107], a[12:15], a[92:95], v[104:107]\n\t"
      "v_add_u32 v4, s65, v4\n\t"
      "v_mfma_f32_16x16x32_bf16 v[108:111], a[12:15], a[96:99], v[108:111]\n\t"
      "s_waitcnt lgkmcnt(6)\n\t"
      "v_mfma_f32_16x16x32_bf16 v[124:127], a[16:19], a[96:99], v[124:127]\n\t"
      "ds_read_b64_tr_b16 a[0:1], v8 offset:16384\n\t"
      "v_mfma_f32_16x16x32_bf16 v[120:123], a[16:19], a[92:95], v[120:123]\n\t"
      "ds_read_b64_tr_b16 a[2:3], v8 offset:22528\n\t"
      "buffer_load_dwordx4 a[72:75], v0, s[36:39], 0 offen offset:2048 sc1\n\t"
      "v_mfma_f32_16x16x32_bf16 v[116:119], a[16:19], a[88:91], v[116:119]\n\t"
      "ds_read_b64_tr_b16 a[4:5], v5 offset:16384\n\t"
      "v_add_u32 v8, s65, v8\n\t"
      "v_mfma_f32_16x16x32_bf16 v[112:115], a[16:19], a[84:87], v[112:115]\n\t"
      "s_waitcnt lgkmcnt(7)\n\t"
      "ds_read_b64_tr_b16 a[6:7], v5 offset:22528\n\t"
      "v_mfma_f32_16x16x32_bf16 v[128:131], a[20:23], a[84:87], v[128:131]\n\t"
      "v_add_u32 v5, s65, v5\n\t"
      "v_mfma_f32_16x16x32_bf16 v[132:135], a[20:23], a[88:91], v[132:135]\n\t"
      "ds_read_b64_tr_b16 a[8:9], v6 offset:16384\n\t"
      "v_mfma_f32_16x16x32_bf16 v[136:139], a[20:23], a[92:95], v[136:139]\n\t"
      "ds_read_b64_tr_b16 a[10:11], v6 offset:22528\n\t"
      "v_mfma_f32_16x16x32_bf16 v[140:143], a[20:23], a[96:99], v[140:143]\n\t"
      "s_waitcnt lgkmcnt(8)\n\t"
      "v_add_u32 v6, s65, v6\n\t"
      "v_readlane_b32 s12, v11, s19\n\t"
      "v_mfma_f32_16x16x32_bf16 v[156:159], a[24:27], a[96:99], v[156:159]\n\t"
      "v_readlane_b32 s13, v13, s19\n\t"
      "v_readlane_b32 s14, v14, s19\n\t"
      "v_mfma_f32_16x16x32_bf16 v[152:155], a[24:27], a[92:95], v[152:155]\n\t"
      "s_add_u32 s19, s19, 1\n\t"
      "ds_read_b64_tr_b16 a[12:13], v7 offset:16384\n\t"
      "v_mfma_f32_16x16x32_bf16 v[148:151], a[24:27], a[88:91], v[148:151]\n\t"
      "ds_read_b64_tr_b16 a[14:15], v7 offset:22528\n\t"
      "v_mfma_f32_16x16x32_bf16 v[144:147], a[24:27], a[84:87], v[144:147]\n\t"
      "s_waitcnt lgkmcnt(8)\n\t"
      "buffer_load_dwordx4 a[76:79], v0, s[36:39], 0 offen offset:3072 sc1\n\t"
      "v_add_u32 v7, s65, v7\n\t"
      "v_mfma_f32_16x16x32_bf16 v[160:163], a[84:87], a[28:31], v[160:163]\n\t"
      "v_mfma_f32_16x16x32_bf16 v[164:167], a[88:91], a[28:31], v[164:167]\n\t"
      "s_waitcnt vmcnt(20)\n\t"
      "s_barrier\n\t"
      "ds_read_b64_tr_b16 a[16:17], v4 offset:0\n\t"
      "v_mfma_f32_16x16x32_bf16 v[168:171], a[92:95], a[28:31], v[168:171]\n\t"
      "ds_read_b64_tr_b16 a[18:19], v4 offset:6144\n\t"
      "v_mfma_f32_16x16x32_bf16 v[172:175], a[96:99], a[28:31], v[172:175]\n\t"
      "s_waitcnt lgkmcnt(8)\n\t"
      "buffer_load_dword v12, v28, s[12:15], 0 offen\n\t"
      "v_mfma_f32_16x16x32_bf16 v[228:231], a[100:103], a[0:3], v[228:231]\n\t"
      "s_waitcnt lgkmcnt(6)\n\t"
      "v_mfma_f32_16x16x32_bf16 v[188:191], a[96:99], a[4:7], v[188:191]\n\t"
      "ds_read_b64_tr_b16 a[20:21], v5 offset:0\n\t"
      "v_mfma_f32_16x16x32_bf16 v[184:187], a[92:95], a[4:7], v[184:187]\n\t"
      "ds_read_b64_tr_b16 a[22:23], v5 offset:6144\n\t"
      "v_mfma_f32_16x16x32_bf16 v[180:183], a[88:91], a[4:7], v[180:183]\n\t"
      "v_mfma_f32_16x16x32_bf16 v[176:179], a[84:87], a[4:7], v[176:179]\n\t"
      "s_waitcnt lgkmcnt(6)\n\t"
      "v_mfma_f32_16x16x32_bf16 v[192:195], a[84:87], a[8:11], v[192:195]\n\t"
      "ds_read_b64_tr_b16 a[24:25], v6 offset:0\n\t"
      "buffer_load_dwordx4 a[80:83], v1, s[36:39], 0 offen sc1\n\t"
      "v_mfma_f32_16x16x32_bf16 v[196:199], a[88:91], a[8:11], v[196:199]\n\t"
      "ds_read_b64_tr_b16 a[26:27], v6 offset:6144\n\t"
      "v_add_u32 v0, s51, v0\n\t"
      "v_mfma_f32_16x16x32_bf16 v[200:203], a[92:95], a[8:11], v[200:203]\n\t"
      "v_add_u32 v1, s51, v1\n\t"
      "v_mfma_f32_16x16x32_bf16 v[204:207], a[96:99], a[8:11], v[204:207]\n\t"
      "s_waitcnt lgkmcnt(6)\n\t"
      "v_mfma_f32_16x16x32_bf16 v[220:223], a[96:99], a[12:15], v[220:223]\n\t"
      "ds_read_b64_tr_b16 a[28:29], v7 offset:0\n\t"
      "v_mfma_f32_16x16x32_bf16 v[216:219], a[92:95], a[12:15], v[216:219]\n\t"
      "ds_read_b64_tr_b16 a[30:31], v7 offset:6144\n\t"
      "v_mfma_f32_16x16x32_bf16 v[212:215], a[88:91], a[12:15], v[212:215]\n\t"
      "s_waitcnt vmcnt(17)\n\t"
      "v_mfma_f32_16x16x32_bf16 v[208:211], a[84:87], a[12:15], v[208:211]\n\t"
      "s_waitcnt lgkmcnt(6)\n\t"
      "v_mfma_f32_16x16x32_bf16 v[32:35], a[16:19], a[104:107], v[32:35]\n\t"
      "ds_read_b64_tr_b16 a[0:1], v8 offset:2048\n\t"
      "v_mfma_f32_16x16x32_bf16 v[36:39], a[16:19], a[108:111], v[36:39]\n\t"
      "ds_read_b64_tr_b16 a[2:3], v8 offset:8192\n\t"
      "v_mfma_f32_16x16x32_bf16 v[40:43], a[16:19], a[112:115], v[40:43]\n\t"
      "ds_read_b64_tr_b16 a[4:5], v4 offset:2048\n\t"
      "buffer_load_dwordx4 a[84:87], v0, s[36:39], 0 offen sc1\n\t"
      "v_mfma_f32_16x16x32_bf16 v[44:47], a[16:19], a[116:119], v[44:47]\n\t"
      "s_waitcnt lgkmcnt(7)\n\t"
      "s_mov_b32 m0, s53\n\t"
      "ds_read_b64_tr_b16 a[6:7], v4 offset:8192\n\t"
      "v_mfma_f32_16x16x32_bf16 v[60:63], a[20:23], a[116:119], v[60:63]\n\t"
      "buffer_load_dwordx4 v2, s[40:43], s44 offen lds\n\t"
      "v_mfma_f32_16x16x32_bf16 v[56:59], a[20:23], a[112:115], v[56:59]\n\t"
      "ds_read_b64_tr_b16 a[8:9], v5 offset:2048\n\t"
      "v_mfma_f32_16x16x32_bf16 v[52:55], a[20:23], a[108:111], v[52:55]\n\t"
      "ds_read_b64_tr_b16 a[10:11], v5 offset:8192\n\t"
      "v_mfma_f32_16x16x32_bf16 v[48:51], a[20:23], a[104:107], v[48:51]\n\t"
      "s_waitcnt lgkmcnt(8)\n\t"
      "v_mfma_f32_16x16x32_bf16 v[64:67], a[24:27], a[104:107], v[64:67]\n\t"
      "v_mfma_f32_16x16x32_bf16 v[68:71], a[24:27], a[108:111], v[68:71]\n\t"
      "ds_read_b64_tr_b16 a[12:13], v6 offset:2048\n\t"
      "v_mfma_f32_16x16x32_bf16 v[72:75], a[24:27], a[112:115], v[72:75]\n\t"
      "ds_read_b64_tr_b16 a[14:15], v6 offset:8192\n\t"
      "v_mfma_f32_16x16x32_bf16 v[76:79], a[24:27], a[116:119], v[76:79]\n\t"
      "s_waitcnt lgkmcnt(8)\n\t"
      "v_mfma_f32_16x16x32_bf16 v[92:95], a[28:31], a[116:119], v[92:95]\n\t"
      "buffer_load_dwordx4 a[88:91], v0, s[36:39], 0 offen offset:1024 sc1\n\t"
      "v_mfma_f32_16x16x32_bf16 v[88:91], a[28:31], a[112:115], v[88:91]\n\t"
      "ds_read_b64_tr_b16 a[16:17], v7 offset:2048\n\t"
      "v_mfma_f32_16x16x32_bf16 v[84:87], a[28:31], a[108:111], v[84:87]\n\t"
      "ds_read_b64_tr_b16 a[18:19], v7 offset:8192\n\t"
      "v_mfma_f32_16x16x32_bf16 v[80:83], a[28:31], a[104:107], v[80:83]\n\t"
      "s_waitcnt lgkmcnt(8)\n\t"
      "buffer_load_dwordx4 v2, s[40:43], s45 offen offset:1024 lds\n\t"
      "v_mfma_f32_16x16x32_bf16 v[224:227], a[0:3], a[120:123], v[224:227]\n\t"
      "s_waitcnt lgkmcnt(6)\n\t"
      "v_mfma_f32_16x16x32_bf16 v[96:99], a[4:7], a[104:107], v[96:99]\n\t"
      "ds_read_b64_tr_b16 a[20:21], v4 offset:4096\n\t"
      "v_mfma_f32_16x16x32_bf16 v[100:103], a[4:7], a[108:111], v[100:103]\n\t"
      "ds_read_b64_tr_b16 a[22:23], v4 offset:10240\n\t"
      "v_mfma_f32_16x16x32_bf16 v[104:107], a[4:7], a[112:115], v[104:107]\n\t"
      "v_mfma_f32_16x16x32_bf16 v[108:111], a[4:7], a[116:119], v[108:111]\n\t"
      "s_waitcnt lgkmcnt(6)\n\t"
      "v_mfma_f32_16x16x32_bf16 v[124:127], a[8:11], a[116:119], v[124:127]\n\t"
      "ds_read_b64_tr_b16 a[24:25], v8 offset:4096\n\t"
      "v_mfma_f32_16x16x32_bf16 v[120:123], a[8:11], a[112:115], v[120:123]\n\t"
      "ds_read_b64_tr_b16 a[26:27], v8 offset:10240\n\t"
      "buffer_load_dwordx4 a[92:95], v0, s[36:39], 0 offen offset:2048 sc1\n\t"
      "v_mfma_f32_16x16x32_bf16 v[116:119], a[8:11], a[108:111], v[116:119]\n\t"
      "ds_read_b64_tr_b16 a[28:29], v5 offset:4096\n\t"
      "v_mfma_f32_16x16x32_bf16 v[112:115], a[8:11], a[104:107], v[112:115]\n\t"
      "s_waitcnt lgkmcnt(7)\n\t"
      "ds_read_b64_tr_b16 a[30:31], v5 offset:10240\n\t"
      "v_mfma_f32_16x16x32_bf16 v[128:131], a[12:15], a[104:107], v[128:131]\n\t"
      "v_mfma_f32_16x16x32_bf16 v[132:135], a[12:15], a[108:111], v[132:135]\n\t"
      "ds_read_b64_tr_b16 a[0:1], v6 offset:4096\n\t"
      "buffer_load_dwordx4 v2, s[40:43], s46 offen offset:2048 lds\n\t"
      "v_mfma_f32_16x16x32_bf16 v[136:139], a[12:15], a[112:115], v[136:139]\n\t"
      "ds_read_b64_tr_b16 a[2:3], v6 offset:10240\n\t"
      "v_mfma_f32_16x16x32_bf16 v[140:143], a[12:15], a[116:119], v[140:143]\n\t"
      "s_waitcnt lgkmcnt(8)\n\t"
      "v_mfma_f32_16x16x32_bf16 v[156:159], a[16:19], a[116:119], v[156:159]\n\t"
      "v_mfma_f32_16x16x32_bf16 v[152:155], a[16:19], a[112:115], v[152:155]\n\t"
      "ds_read_b64_tr_b16 a[4:5], v7 offset:4096\n\t"
      "v_mfma_f32_16x16x32_bf16 v[148:151], a[16:19], a[108:111], v[148:151]\n\t"
      "ds_read_b64_tr_b16 a[6:7], v7 offset:10240\n\t"
      "v_mfma_f32_16x16x32_bf16 v[144:147], a[16:19], a[104:107], v[144:147]\n\t"
      "s_waitcnt lgkmcnt(8)\n\t"
      "buffer_load_dwordx4 a[96:99], v0, s[36:39], 0 offen offset:3072 sc1\n\t"
      "v_mfma_f32_16x16x32_bf16 v[160:163], a[104:107], a[20:23], v[160:163]\n\t"
      "v_mfma_f32_16x16x32_bf16 v[164:167], a[108:111], a[20:23], v[164:167]\n\t"
      "ds_read_b64_tr_b16 a[8:9], v4 offset:12288\n\t"
      "v_mfma_f32_16x16x32_bf16 v[168:171], a[112:115], a[20:23], v[168:171]\n\t"
      "ds_read_b64_tr_b16 a[10:11], v4 offset:18432\n\t"
      "v_mfma_f32_16x16x32_bf16 v[172:175], a[116:119], a[20:23], v[172:175]\n\t"
      "s_waitcnt lgkmcnt(8)\n\t"
      "v_mfma_f32_16x16x32_bf16 v[228:231], a[120:123], a[24:27], v[228:231]\n\t"
      "s_waitcnt lgkmcnt(6)\n\t"
      "v_mfma_f32_16x16x32_bf16 v[188:191], a[116:119], a[28:31], v[188:191]\n\t"
      "ds_read_b64_tr_b16 a[12:13], v5 offset:12288\n\t"
      "buffer_load_dwordx4 v2, s[40:43], s47 offen offset:3072 lds\n\t"
      "v_mfma_f32_16x16x32_bf16 v[184:187], a[112:115], a[28:31], v[184:187]\n\t"
      "ds_read_b64_tr_b16 a[14:15], v5 offset:18432\n\t"
      "v_mfma_f32_16x16x32_bf16 v[180:183], a[108:111], a[28:31], v[180:183]\n\t"
      "v_mfma_f32_16x16x32_bf16 v[176:179], a[104:107], a[28:31], v[176:179]\n\t"
      "s_waitcnt lgkmcnt(6)\n\t"
      "v_mfma_f32_16x16x32_bf16 v[192:195], a[104:107], a[0:3], v[192:195]\n\t"
      "ds_read_b64_tr_b16 a[16:17], v6 offset:12288\n\t"
      "buffer_load_dwordx4 a[100:103], v1, s[36:39], 0 offen sc1\n\t"
      "v_mfma_f32_16x16x32_bf16 v[196:199], a[108:111], a[0:3], v[196:199]\n\t"
      "ds_read_b64_tr_b16 a[18:19], v6 offset:18432\n\t"
      "v_add_u32 v0, s51, v0\n\t"
      "v_mfma_f32_16x16x32_bf16 v[200:203], a[112:115], a[0:3], v[200:203]\n\t"
      "v_add_u32 v1, s51, v1\n\t"
      "v_mfma_f32_16x16x32_bf16 v[204:207], a[116:119], a[0:3], v[204:207]\n\t"
      "s_waitcnt lgkmcnt(6)\n\t"
      "v_mfma_f32_16x16x32_bf16 v[220:223], a[116:119], a[4:7], v[220:223]\n\t"
      "ds_read_b64_tr_b16 a[20:21], v7 offset:12288\n\t"
      "v_mfma_f32_16x16x32_bf16 v[216:219], a[112:115], a[4:7], v[216:219]\n\t"
      "ds_read_b64_tr_b16 a[22:23], v7 offset:18432\n\t"
      "v_mfma_f32_16x16x32_bf16 v[212:215], a[108:111], a[4:7], v[212:215]\n\t"
      "s_waitcnt vmcnt(17)\n\t"
      "s_mov_b32 m0, s57\n\t"
      "v_mfma_f32_16x16x32_bf16 v[208:211], a[104:107], a[4:7], v[208:211]\n\t"
      "s_waitcnt lgkmcnt(6)\n\t"
      "buffer_load_dwordx4 v2, s[40:43], s48 offen lds\n\t"
      "v_mfma_f32_16x16x32_bf16 v[32:35], a[8:11], a[124:127], v[32:35]\n\t"
      "ds_read_b64_tr_b16 a[24:25], v8 offset:14336\n\t"
      "v_mfma_f32_16x16x32_bf16 v[36:39], a[8:11], a[128:131], v[36:39]\n\t"
      "ds_read_b64_tr_b16 a[26:27], v8 offset:20480\n\t"
      "v_mfma_f32_16x16x32_bf16 v[40:43], a[8:11], a[132:135], v[40:43]\n\t"
      "ds_read_b64_tr_b16 a[28:29], v4 offset:14336\n\t"
      "buffer_load_dwordx4 a[104:107], v0, s[36:39], 0 offen sc1\n\t"
      "v_mfma_f32_16x16x32_bf16 v[44:47], a[8:11], a[136:139], v[44:47]\n\t"
      "s_waitcnt lgkmcnt(7)\n\t"
      "ds_read_b64_tr_b16 a[30:31], v4 offset:20480\n\t"
      "v_mfma_f32_16x16x32_bf16 v[60:63], a[12:15], a[136:139], v[60:63]\n\t"
      "v_mfma_f32_16x16x32_bf16 v[56:59], a[12:15], a[132:135], v[56:59]\n\t"
      "ds_read_b64_tr_b16 a[0:1], v5 offset:14336\n\t"
      "v_mfma_f32_16x16x32_bf16 v[52:55], a[12:15], a[128:131], v[52:55]\n\t"
      "ds_read_b64_tr_b16 a[2:3], v5 offset:20480\n\t"
      "v_mfma_f32_16x16x32_bf16 v[48:51], a[12:15], a[124:127], v[48:51]\n\t"
      "s_waitcnt lgkmcnt(8)\n\t"
      "v_mfma_f32_16x16x32_bf16 v[64:67], a[16:19], a[124:127], v[64:67]\n\t"
      "v_mfma_f32_16x16x32_bf16 v[68:71], a[16:19], a[128:131], v[68:71]\n\t"
      "ds_read_b64_tr_b16 a[4:5], v6 offset:14336\n\t"
      "v_mfma_f32_16x16x32_bf16 v[72:75], a[16:19], a[132:135], v[72:75]\n\t"
      "ds_read_b64_tr_b16 a[6:7], v6 offset:20480\n\t"
      "buffer_load_dwordx4 v2, s[40:43], s49 offen offset:1024 lds\n\t"
      "v_mfma_f32_16x16x32_bf16 v[76:79], a[16:19], a[136:139], v[76:79]\n\t"
      "s_waitcnt lgkmcnt(8)\n\t"
      "s_add_u32 s53, s53, 24576\n\t"
      "v_add_u32 v2, s52, v2\n\t"
      "v_mfma_f32_16x16x32_bf16 v[92:95], a[20:23], a[136:139], v[92:95]\n\t"
      "s_cmp_eq_u32 s53, s55\n\t"
      "s_cselect_b32 s53, s54, s53\n\t"
      "buffer_load_dwordx4 a[108:111], v0, s[36:39], 0 offen offset:1024 sc1\n\t"
      "v_mfma_f32_16x16x32_bf16 v[88:91], a[20:23], a[132:135], v[88:91]\n\t"
      "s_add_u32 s57, s53, 4096\n\t"
      "s_add_u32 s64, s64, 1\n\t"
      "ds_read_b64_tr_b16 a[8:9], v7 offset:14336\n\t"
      "v_mfma_f32_16x16x32_bf16 v[84:87], a[20:23], a[128:131], v[84:87]\n\t"
      "s_cmp_eq_u32 s64, 5\n\t"
      "s_cselect_b32 s65, s66, s67\n\t"
      "ds_read_b64_tr_b16 a[10:11], v7 offset:20480\n\t"
      "v_mfma_f32_16x16x32_bf16 v[80:83], a[20:23], a[124:127], v[80:83]\n\t"
      "s_waitcnt lgkmcnt(8)\n\t"
      "s_cselect_b32 s64, 0, s64\n\t"
      "v_mfma_f32_16x16x32_bf16 v[224:227], a[24:27], a[140:143], v[224:227]\n\t"
      "s_waitcnt lgkmcnt(6)\n\t"
      "v_mfma_f32_16x16x32_bf16 v[96:99], a[28:31], a[124:127], v[96:99]\n\t"
      "ds_read_b64_tr_b16 a[12:13], v4 offset:16384\n\t"
      "v_mfma_f32_16x16x32_bf16 v[100:103], a[28:31], a[128:131], v[100:103]\n\t"
      "ds_read_b64_tr_b16 a[14:15], v4 offset:22528\n\t"
      "v_mfma_f32_16x16x32_bf16 v[104:107], a[28:31], a[132:135], v[104:107]\n\t"
      "v_add_u32 v4, s65, v4\n\t"
      "v_mfma_f32_16x16x32_bf16 v[108:111], a[28:31], a[136:139], v[108:111]\n\t"
      "s_waitcnt lgkmcnt(6)\n\t"
      "v_mfma_f32_16x16x32_bf16 v[124:127], a[0:3], a[136:139], v[124:127]\n\t"
      "ds_read_b64_tr_b16 a[16:17], v8 offset:16384\n\t"
      "v_mfma_f32_16x16x32_bf16 v[120:123], a[0:3], a[132:135], v[120:123]\n\t"
      "ds_read_b64_tr_b16 a[18:19], v8 offset:22528\n\t"
      "buffer_load_dwordx4 a[112:115], v0, s[36:39], 0 offen offset:2048 sc1\n\t"
      "v_mfma_f32_16x16x32_bf16 v[116:119], a[0:3], a[128:131], v[116:119]\n\t"
      "ds_read_b64_tr_b16 a[20:21], v5 offset:16384\n\t"
      "v_add_u32 v8, s65, v8\n\t"
      "v_mfma_f32_16x16x32_bf16 v[112:115], a[0:3], a[124:127], v[112:115]\n\t"
      "s_waitcnt lgkmcnt(7)\n\t"
      "ds_read_b64_tr_b16 a[22:23], v5 offset:22528\n\t"
      "v_mfma_f32_16x16x32_bf16 v[128:131], a[4:7], a[124:127], v[128:131]\n\t"
      "v_add_u32 v5, s65, v5\n\t"
      "v_mfma_f32_16x16x32_bf16 v[132:135], a[4:7], a[128:131], v[132:135]\n\t"
      "ds_read_b64_tr_b16 a[24:25], v6 offset:16384\n\t"
      "v_mfma_f32_16x16x32_bf16 v[136:139], a[4:7], a[132:135], v[136:139]\n\t"
      "ds_read_b64_tr_b16 a[26:27], v6 offset:22528\n\t"
      "v_mfma_f32_16x16x32_bf16 v[140:143], a[4:7], a[136:139], v[140:143]\n\t"
      "s_waitcnt lgkmcnt(8)\n\t"
      "v_add_u32 v6, s65, v6\n\t"
      "v_readlane_b32 s12, v11, s19\n\t"
      "v_mfma_f32_16x16x32_bf16 v[156:159], a[8:11], a[136:139], v[156:159]\n\t"
      "v_readlane_b32 s13, v13, s19\n\t"
      "v_readlane_b32 s14, v14, s19\n\t"
      "v_mfma_f32_16x16x32_bf16 v[152:155], a[8:11], a[132:135], v[152:155]\n\t"
      "s_add_u32 s19, s19, 1\n\t"
      "ds_read_b64_tr_b16 a[28:29], v7 offset:16384\n\t"
      "v_mfma_f32_16x16x32_bf16 v[148:151], a[8:11], a[128:131], v[148:151]\n\t"
      "ds_read_b64_tr_b16 a[30:31], v7 offset:22528\n\t"
      "v_mfma_f32_16x16x32_bf16 v[144:147], a[8:11], a[124:127], v[144:147]\n\t"
      "s_waitcnt lgkmcnt(8)\n\t"
      "buffer_load_dwordx4 a[116:119], v0, s[36:39], 0 offen offset:3072 sc1\n\t"
      "v_add_u32 v7, s65, v7\n\t"
      "v_mfma_f32_16x16x32_bf16 v[160:163], a[124:127], a[12:15], v[160:163]\n\t"
      "v_mfma_f32_16x16x32_bf16 v[164:167], a[128:131], a[12:15], v[164:167]\n\t"
      "s_waitcnt vmcnt(20)\n\t"
      "s_barrier\n\t"
      "ds_read_b64_tr_b16 a[0:1], v4 offset:0\n\t"
      "v_mfma_f32_16x16x32_bf16 v[168:171], a[132:135], a[12:15], v[168:171]\n\t"
      "ds_read_b64_tr_b16 a[2:3], v4 offset:6144\n\t"
      "v_mfma_f32_16x16x32_bf16 v[172:175], a[136:139], a[12:15], v[172:175]\n\t"
      "s_waitcnt lgkmcnt(8)\n\t"
      "buffer_load_dword v12, v28, s[12:15], 0 offen\n\t"
      "v_mfma_f32_16x16x32_bf16 v[228:231], a[140:143], a[16:19], v[228:231]\n\t"
      "s_waitcnt lgkmcnt(6)\n\t"
      "v_mfma_f32_16x16x32_bf16 v[188:191], a[136:139], a[20:23], v[188:191]\n\t"
      "ds_read_b64_tr_b16 a[4:5], v5 offset:0\n\t"
      "v_mfma_f32_16x16x32_bf16 v[184:187], a[132:135], a[20:23], v[184:187]\n\t"
      "ds_read_b64_tr_b16 a[6:7], v5 offset:6144\n\t"
      "v_mfma_f32_16x16x32_bf16 v[180:183], a[128:131], a[20:23], v[180:183]\n\t"
      "v_mfma_f32_16x16x32_bf16 v[176:179], a[124:127], a[20:23], v[176:179]\n\t"
      "s_waitcnt lgkmcnt(6)\n\t"
      "v_mfma_f32_16x16x32_bf16 v[192:195], a[124:127], a[24:27], v[192:195]\n\t"
      "ds_read_b64_tr_b16 a[8:9], v6 offset:0\n\t"
      "buffer_load_dwordx4 a[120:123], v1, s[36:39], 0 offen sc1\n\t"
      "v_mfma_f32_16x16x32_bf16 v[196:199], a[128:131], a[24:27], v[196:199]\n\t"
      "ds_read_b64_tr_b16 a[10:11], v6 offset:6144\n\t"
      "v_add_u32 v0, s51, v0\n\t"
      "v_mfma_f32_16x16x32_bf16 v[200:203], a[132:135], a[24:27], v[200:203]\n\t"
      "v_add_u32 v1, s51, v1\n\t"
      "v_mfma_f32_16x16x32_bf16 v[204:207], a[136:139], a[24:27], v[204:207]\n\t"
      "s_waitcnt lgkmcnt(6)\n\t"
      "v_mfma_f32_16x16x32_bf16 v[220:223], a[136:139], a[28:31], v[220:223]\n\t"
      "ds_read_b64_tr_b16 a[12:13], v7 offset:0\n\t"
      "v_mfma_f32_16x16x32_bf16 v[216:219], a[132:135], a[28:31], v[216:219]\n\t"
      "s_sub_u32 s68, s68, 1\n\t"
      "ds_read_b64_tr_b16 a[14:15], v7 offset:6144\n\t"
      "v_mfma_f32_16x16x32_bf16 v[212:215], a[128:131], a[28:31], v[212:215]\n\t"
      "s_cmp_lg_u32 s68, 0\n\t"
      "s_waitcnt vmcnt(17)\n\t"
      "v_mfma_f32_16x16x32_bf16 v[208:211], a[124:127], a[28:31], v[208:211]\n\t"
      "s_waitcnt lgkmcnt(6)\n\t"
      "s_cbranch_scc1 Lg1q_%=\n\t"
      "v_mfma_f32_16x16x32_bf16 v[32:35], a[0:3], a[64:67], v[32:35]\n\t"
      "ds_read_b64_tr_b16 a[16:17], v4 offset:2048\n\t"
      "v_mfma_f32_16x16x32_bf16 v[36:39], a[0:3], a[68:71], v[36:39]\n\t"
      "ds_read_b64_tr_b16 a[18:19], v4 offset:8192\n\t"
      "v_mfma_f32_16x16x32_bf16 v[40:43], a[0:3], a[72:75], v[40:43]\n\t"
      "buffer_load_dwordx4 a[124:127], v0, s[36:39], 0 offen sc1\n\t"
      "v_mfma_f32_16x16x32_bf16 v[44:47], a[0:3], a[76:79], v[44:47]\n\t"
      "s_waitcnt lgkmcnt(6)\n\t"
      "v_mfma_f32_16x16x32_bf16 v[60:63], a[4:7], a[76:79], v[60:63]\n\t"
      "ds_read_b64_tr_b16 a[20:21], v8 offset:2048\n\t"
      "buffer_load_dwordx4 a[128:131], v0, s[36:39], 0 offen offset:1024 sc1\n\t"
      "v_mfma_f32_16x16x32_bf16 v[56:59], a[4:7], a[72:75], v[56:59]\n\t"
      "ds_read_b64_tr_b16 a[22:23], v8 offset:8192\n\t"
      "v_mfma_f32_16x16x32_bf16 v[52:55], a[4:7], a[68:71], v[52:55]\n\t"
      "ds_read_b64_tr_b16 a[24:25], v5 offset:2048\n\t"
      "buffer_load_dwordx4 a[132:135], v0, s[36:39], 0 offen offset:2048 sc1\n\t"
      "v_mfma_f32_16x16x32_bf16 v[48:51], a[4:7], a[64:67], v[48:51]\n\t"
      "s_waitcnt lgkmcnt(7)\n\t"
      "ds_read_b64_tr_b16 a[26:27], v5 offset:8192\n\t"
      "v_mfma_f32_16x16x32_bf16 v[64:67], a[8:11], a[64:67], v[64:67]\n\t"
      "buffer_load_dwordx4 a[136:139], v0, s[36:39], 0 offen offset:3072 sc1\n\t"
      "v_mfma_f32_16x16x32_bf16 v[68:71], a[8:11], a[68:71], v[68:71]\n\t"
      "ds_read_b64_tr_b16 a[28:29], v4 offset:4096\n\t"
      "v_mfma_f32_16x16x32_bf16 v[72:75], a[8:11], a[72:75], v[72:75]\n\t"
      "ds_read_b64_tr_b16 a[30:31], v4 offset:10240\n\t"
      "buffer_load_dwordx4 a[140:143], v1, s[36:39], 0 offen sc1\n\t"
      "v_mfma_f32_16x16x32_bf16 v[76:79], a[8:11], a[76:79], v[76:79]\n\t"
      "s_waitcnt lgkmcnt(8)\n\t"
      "v_mfma_f32_16x16x32_bf16 v[92:95], a[12:15], a[76:79], v[92:95]\n\t"
      "v_mfma_f32_16x16x32_bf16 v[88:91], a[12:15], a[72:75], v[88:91]\n\t"
      "ds_read_b64_tr_b16 a[0:1], v8 offset:4096\n\t"
      "v_mfma_f32_16x16x32_bf16 v[84:87], a[12:15], a[68:71], v[84:87]\n\t"
      "ds_read_b64_tr_b16 a[2:3], v8 offset:10240\n\t"
      "v_mfma_f32_16x16x32_bf16 v[80:83], a[12:15], a[64:67], v[80:83]\n\t"
      "s_waitcnt lgkmcnt(8)\n\t"
      "ds_read_b64_tr_b16 a[4:5], v5 offset:4096\n\t"
      "v_mfma_f32_16x16x32_bf16 v[96:99], a[16:19], a[64:67], v[96:99]\n\t"
      "ds_read_b64_tr_b16 a[6:7], v5 offset:10240\n\t"
      "v_mfma_f32_16x16x32_bf16 v[100:103], a[16:19], a[68:71], v[100:103]\n\t"
      "v_mfma_f32_16x16x32_bf16 v[104:107], a[16:19], a[72:75], v[104:107]\n\t"
      "ds_read_b64_tr_b16 a[8:9], v4 offset:14336\n\t"
      "v_mfma_f32_16x16x32_bf16 v[108:111], a[16:19], a[76:79], v[108:111]\n\t"
      "s_waitcnt lgkmcnt(9)\n\t"
      "ds_read_b64_tr_b16 a[10:11], v4 offset:20480\n\t"
      "v_mfma_f32_16x16x32_bf16 v[224:227], a[20:23], a[80:83], v[224:227]\n\t"
      "s_waitcnt lgkmcnt(8)\n\t"
      "v_mfma_f32_16x16x32_bf16 v[124:127], a[24:27], a[76:79], v[124:127]\n\t"
      "v_mfma_f32_16x16x32_bf16 v[120:123], a[24:27], a[72:75], v[120:123]\n\t"
      "ds_read_b64_tr_b16 a[12:13], v8 offset:14336\n\t"
      "v_mfma_f32_16x16x32_bf16 v[116:119], a[24:27], a[68:71], v[116:119]\n\t"
      "ds_read_b64_tr_b16 a[14:15], v8 offset:20480\n\t"
      "v_mfma_f32_16x16x32_bf16 v[112:115], a[24:27], a[64:67], v[112:115]\n\t"
      "s_waitcnt lgkmcnt(8)\n\t"
      "ds_read_b64_tr_b16 a[16:17], v5 offset:14336\n\t"
      "v_mfma_f32_16x16x32_bf16 v[160:163], a[64:67], a[28:31], v[160:163]\n\t"
      "ds_read_b64_tr_b16 a[18:19], v5 offset:20480\n\t"
      "v_mfma_f32_16x16x32_bf16 v[164:167], a[68:71], a[28:31], v[164:167]\n\t"
      "v_mfma_f32_16x16x32_bf16 v[168:171], a[72:75], a[28:31], v[168:171]\n\t"
      "ds_read_b64_tr_b16 a[20:21], v4 offset:16384\n\t"
      "v_mfma_f32_16x16x32_bf16 v[172:175], a[76:79], a[28:31], v[172:175]\n\t"
      "s_waitcnt lgkmcnt(9)\n\t"
      "ds_read_b64_tr_b16 a[22:23], v4 offset:22528\n\t"
      "v_mfma_f32_16x16x32_bf16 v[228:231], a[80:83], a[0:3], v[228:231]\n\t"
      "s_waitcnt lgkmcnt(8)\n\t"
      "v_mfma_f32_16x16x32_bf16 v[188:191], a[76:79], a[4:7], v[188:191]\n\t"
      "v_mfma_f32_16x16x32_bf16 v[184:187], a[72:75], a[4:7], v[184:187]\n\t"
      "ds_read_b64_tr_b16 a[24:25], v8 offset:16384\n\t"
      "v_mfma_f32_16x16x32_bf16 v[180:183], a[68:71], a[4:7], v[180:183]\n\t"
      "s_waitcnt vmcnt(13)\n\t"
      "ds_read_b64_tr_b16 a[26:27], v8 offset:22528\n\t"
      "v_mfma_f32_16x16x32_bf16 v[176:179], a[64:67], a[4:7], v[176:179]\n\t"
      "s_waitcnt lgkmcnt(8)\n\t"
      "ds_read_b64_tr_b16 a[28:29], v5 offset:16384\n\t"
      "v_mfma_f32_16x16x32_bf16 v[96:99], a[8:11], a[84:87], v[96:99]\n\t"
      "ds_read_b64_tr_b16 a[30:31], v5 offset:22528\n\t"
      "v_mfma_f32_16x16x32_bf16 v[100:103], a[8:11], a[88:91], v[100:103]\n\t"
      "v_mfma_f32_16x16x32_bf16 v[104:107], a[8:11], a[92:95], v[104:107]\n\t"
      "ds_read_b64_tr_b16 a[0:1], v4 offset:12288\n\t"
      "v_mfma_f32_16x16x32_bf16 v[108:111], a[8:11], a[96:99], v[108:111]\n\t"
      "s_waitcnt lgkmcnt(9)\n\t"
      "ds_read_b64_tr_b16 a[2:3], v4 offset:18432\n\t"
      "v_mfma_f32_16x16x32_bf16 v[224:227], a[12:15], a[100:103], v[224:227]\n\t"
      "s_waitcnt lgkmcnt(8)\n\t"
      "v_mfma_f32_16x16x32_bf16 v[124:127], a[16:19], a[96:99], v[124:127]\n\t"
      "v_mfma_f32_16x16x32_bf16 v[120:123], a[16:19], a[92:95], v[120:123]\n\t"
      "ds_read_b64_tr_b16 a[4:5], v5 offset:12288\n\t"
      "v_mfma_f32_16x16x32_bf16 v[116:119], a[16:19], a[88:91], v[116:119]\n\t"
      "ds_read_b64_tr_b16 a[6:7], v5 offset:18432\n\t"
      "v_mfma_f32_16x16x32_bf16 v[112:115], a[16:19], a[84:87], v[112:115]\n\t"
      "s_waitcnt lgkmcnt(8)\n\t"
      "v_mfma_f32_16x16x32_bf16 v[160:163], a[84:87], a[20:23], v[160:163]\n\t"
      "v_mfma_f32_16x16x32_bf16 v[164:167], a[88:91], a[20:23], v[164:167]\n\t"
      "s_waitcnt vmcnt(10)\n\t"
      "s_barrier\n\t"
      "ds_read_b64_tr_b16 a[8:9], v4 offset:26624\n\t"
      "v_mfma_f32_16x16x32_bf16 v[168:171], a[92:95], a[20:23], v[168:171]\n\t"
      "ds_read_b64_tr_b16 a[10:11], v4 offset:32768\n\t"
      STAMP(6, 7)
      "s_mov_b32 s16, 32768\n\t"
      "s_mov_b32 s64, 98304\n\t"
      "s_and_b32 s55, s27, 0xfffff000\n\t"
      "s_mov_b32 s48, 38\n\t"
      "v_mfma_f32_16x16x32_bf16 v[172:175], a[96:99], a[20:23], v[172:175]\n\t"
      "s_waitcnt lgkmcnt(8)\n\t"
      "s_mov_b32 s50, 8\n\t"
      "s_sub_u32 s80, s80, 4096\n\t"
      "s_subb_u32 s81, s81, 0\n\t"
      "s_add_u32 s82, s82, 4096\n\t"
      "v_mfma_f32_16x16x32_bf16 v[228:231], a[100:103], a[24:27], v[228:231]\n\t"
      "s_waitcnt lgkmcnt(6)\n\t"
      "s_sub_u32 s84, s84, 4096\n\t"
      "s_subb_u32 s85, s85, 0\n\t"
      "s_add_u32 s86, s86, 4096\n\t"
      "s_lshl2_add_u32 s56, s2, s32\n\t"
      "v_mfma_f32_16x16x32_bf16 v[188:191], a[96:99], a[28:31], v[188:191]\n\t"
      "ds_read_b64_tr_b16 a[12:13], v8 offset:26624\n\t"
      "s_lshl_b32 s56, s56, 14\n\t"
      "s_xor_b32 s57, s56, 131072\n\t"
      "s_xor_b32 s58, s56, 266240\n\t"
      "s_xor_b32 s69, s56, 401408\n\t"
      "v_mfma_f32_16x16x32_bf16 v[184:187], a[92:95], a[28:31], v[184:187]\n\t"
      LSTW(6, "")
      "ds_read_b64_tr_b16 a[14:15], v8 offset:32768\n\t"
      "s_add_u32 s67, s27, 32768\n\t"
      "s_add_u32 s19, s59, 65536\n\t"
      "s_add_u32 s18, s19, 32768\n\t"
      "s_mov_b32 s68, 3\n\t"
      "v_mfma_f32_16x16x32_bf16 v[180:183], a[88:91], a[28:31], v[180:183]\n\t"
      "ds_read_b64_tr_b16 a[16:17], v5 offset:26624\n\t"
      "v_readlane_b32 s54, v23, 0\n\t"
      "s_bitcmp1_b32 s54, 0\n\t"
      "s_cselect_b64 s[36:37], s[80:81], s[84:85]\n\t"
      "s_cselect_b32 s38, s82, s86\n\t"
      "v_mfma_f32_16x16x32_bf16 v[176:179], a[84:87], a[28:31], v[176:179]\n\t"
      "s_waitcnt lgkmcnt(7)\n\t"
      "ds_read_b64_tr_b16 a[18:19], v5 offset:32768\n\t"
      "s_and_b32 s54, s54, -2\n\t"
      "v_add_u32 v1, s54, v17\n\t"
      "v_mfma_f32_16x16x32_bf16 v[32:35], a[0:3], a[84:87], v[32:35]\n\t"
      "v_mfma_f32_16x16x32_bf16 v[36:39], a[0:3], a[88:91], v[36:39]\n\t"
      "ds_read_b64_tr_b16 a[20:21], v4 offset:28672\n\t"
      "v_mfma_f32_16x16x32_bf16 v[40:43], a[0:3], a[92:95], v[40:43]\n\t"
      "ds_read_b64_tr_b16 a[22:23], v4 offset:34816\n\t"
      "v_mfma_f32_16x16x32_bf16 v[44:47], a[0:3], a[96:99], v[44:47]\n\t"
      "s_waitcnt lgkmcnt(8)\n\t"
      "v_mfma_f32_16x16x32_bf16 v[60:63], a[4:7], a[96:99], v[60:63]\n\t"
      "v_mfma_f32_16x16x32_bf16 v[56:59], a[4:7], a[92:95], v[56:59]\n\t"
      "ds_read_b64_tr_b16 a[24:25], v8 offset:28672\n\t"
      "s_mov_b32 m0, s24\n\t"
      "s_nop 0\n\t"
      "buffer_load_dwordx4 v1, s[36:39], s20 offen lds\n\t"
      "v_mfma_f32_16x16x32_bf16 v[52:55], a[4:7], a[88:91], v[52:55]\n\t"
      "s_waitcnt vmcnt(6)\n\t"
      "ds_read_b64_tr_b16 a[26:27], v8 offset:34816\n\t"
      "v_mfma_f32_16x16x32_bf16 v[48:51], a[4:7], a[84:87], v[48:51]\n\t"
      "s_waitcnt lgkmcnt(8)\n\t"
      "ds_read_b64_tr_b16 a[28:29], v5 offset:28672\n\t"
      "v_mfma_f32_16x16x32_bf16 v[96:99], a[8:11], a[104:107], v[96:99]\n\t"
      "ds_read_b64_tr_b16 a[30:31], v5 offset:34816\n\t"
      "v_mfma_f32_16x16x32_bf16 v[100:103], a[8:11], a[108:111], v[100:103]\n\t"
      "v_mfma_f32_16x16x32_bf16 v[104:107], a[8:11], a[112:115], v[104:107]\n\t"
      "ds_read_b64_tr_b16 a[0:1], v4 offset:24576\n\t"
      "v_mfma_f32_16x16x32_bf16 v[108:111], a[8:11], a[116:119], v[108:111]\n\t"
      "s_waitcnt lgkmcnt(9)\n\t"
      "ds_read_b64_tr_b16 a[2:3], v4 offset:30720\n\t"
      "buffer_load_dwordx4 v1, s[36:39], s21 offen offset:1024 lds\n\t"
      "v_mfma_f32_16x16x32_bf16 v[224:227], a[12:15], a[120:123], v[224:227]\n\t"
      "s_waitcnt lgkmcnt(8)\n\t"
      "v_mfma_f32_16x16x32_bf16 v[124:127], a[16:19], a[116:119], v[124:127]\n\t"
      "v_mfma_f32_16x16x32_bf16 v[120:123], a[16:19], a[112:115], v[120:123]\n\t"
      "ds_read_b64_tr_b16 a[4:5], v5 offset:24576\n\t"
      "v_mfma_f32_16x16x32_bf16 v[116:119], a[16:19], a[108:111], v[116:119]\n\t"
      "ds_read_b64_tr_b16 a[6:7], v5 offset:30720\n\t"
      "v_mfma_f32_16x16x32_bf16 v[112:115], a[16:19], a[104:107], v[112:115]\n\t"
      "s_waitcnt lgkmcnt(8)\n\t"
      "v_mfma_f32_16x16x32_bf16 v[160:163], a[104:107], a[20:23], v[160:163]\n\t"
      "buffer_load_dwordx4 v1, s[36:39], s22 offen offset:2048 lds\n\t"
      "v_mfma_f32_16x16x32_bf16 v[164:167], a[108:111], a[20:23], v[164:167]\n\t"
      "ds_read_b64_tr_b16 a[8:9], v4 offset:38912\n\t"
      "v_mfma_f32_16x16x32_bf16 v[168:171], a[112:115], a[20:23], v[168:171]\n\t"
      "ds_read_b64_tr_b16 a[10:11], v4 offset:45056\n\t"
      "v_mfma_f32_16x16x32_bf16 v[172:175], a[116:119], a[20:23], v[172:175]\n\t"
      "s_waitcnt lgkmcnt(8)\n\t"
      "v_mfma_f32_16x16x32_bf16 v[228:231], a[120:123], a[24:27], v[228:231]\n\t"
      "s_waitcnt lgkmcnt(6)\n\t"
      "v_mfma_f32_16x16x32_bf16 v[188:191], a[116:119], a[28:31], v[188:191]\n\t"
      "ds_read_b64_tr_b16 a[12:13], v8 offset:38912\n\t"
      "v_mfma_f32_16x16x32_bf16 v[184:187], a[112:115], a[28:31], v[184:187]\n\t"
      "ds_read_b64_tr_b16 a[14:15], v8 offset:45056\n\t"
      "buffer_load_dwordx4 v1, s[36:39], s23 offen offset:3072 lds\n\t"
      "s_add_u32 s24, s24, 16384\n\t"
      "s_and_b32 s24, s24, 0x1ffff\n\t"
      "v_readlane_b32 s54, v23, 1\n\t"
      "v_mfma_f32_16x16x32_bf16 v[180:183], a[108:111], a[28:31], v[180:183]\n\t"
      "ds_read_b64_tr_b16 a[16:17], v5 offset:38912\n\t"
      "s_bitcmp1_b32 s54, 0\n\t"
      "s_cselect_b64 s[36:37], s[80:81], s[84:85]\n\t"
      "s_cselect_b32 s38, s82, s86\n\t"
      "s_and_b32 s54, s54, -2\n\t"
      "v_mfma_f32_16x16x32_bf16 v[176:179], a[104:107], a[28:31], v[176:179]\n\t"
      "s_waitcnt lgkmcnt(7)\n\t"
      "ds_read_b64_tr_b16 a[18:19], v5 offset:45056\n\t"
      "v_add_u32 v1, s54, v17\n\t"
      "v_mfma_f32_16x16x32_bf16 v[32:35], a[0:3], a[104:107], v[32:35]\n\t"
      "v_mfma_f32_16x16x32_bf16 v[36:39], a[0:3], a[108:111], v[36:39]\n\t"
      "ds_read_b64_tr_b16 a[20:21], v4 offset:40960\n\t"
      "v_mfma_f32_16x16x32_bf16 v[40:43], a[0:3], a[112:115], v[40:43]\n\t"
      "ds_read_b64_tr_b16 a[22:23], v4 offset:47104\n\t"
      "v_mfma_f32_16x16x32_bf16 v[44:47], a[0:3], a[116:119], v[44:47]\n\t"
      "s_waitcnt lgkmcnt(8)\n\t"
      "v_mfma_f32_16x16x32_bf16 v[60:63], a[4:7], a[116:119], v[60:63]\n\t"
      "v_mfma_f32_16x16x32_bf16 v[56:59], a[4:7], a[112:115], v[56:59]\n\t"
      "ds_read_b64_tr_b16 a[24:25], v8 offset:40960\n\t"
      "s_mov_b32 m0, s24\n\t"
      "s_nop 0\n\t"
      "buffer_load_dwordx4 v1, s[36:39], s20 offen lds\n\t"
      "v_mfma_f32_16x16x32_bf16 v[52:55], a[4:7], a[108:111], v[52:55]\n\t"
      "s_waitcnt vmcnt(5)\n\t"
      "ds_read_b64_tr_b16 a[26:27], v8 offset:47104\n\t"
      "v_mfma_f32_16x16x32_bf16 v[48:51], a[4:7], a[104:107], v[48:51]\n\t"
      "s_waitcnt lgkmcnt(8)\n\t"
      "ds_read_b64_tr_b16 a[28:29], v5 offset:40960\n\t"
      "v_mfma_f32_16x16x32_bf16 v[96:99], a[8:11], a[124:127], v[96:99]\n\t"
      "ds_read_b64_tr_b16 a[30:31], v5 offset:47104\n\t"
      "v_mfma_f32_16x16x32_bf16 v[100:103], a[8:11], a[128:131], v[100:103]\n\t"
      "v_mfma_f32_16x16x32_bf16 v[104:107], a[8:11], a[132:135], v[104:107]\n\t"
      "ds_read_b64_tr_b16 a[0:1], v4 offset:36864\n\t"
      "v_mfma_f32_16x16x32_bf16 v[108:111], a[8:11], a[136:139], v[108:111]\n\t"
      "s_waitcnt lgkmcnt(9)\n\t"
      "ds_read_b64_tr_b16 a[2:3], v4 offset:43008\n\t"
      "buffer_load_dwordx4 v1, s[36:39], s21 offen offset:1024 lds\n\t"
      "v_mfma_f32_16x16x32_bf16 v[224:227], a[12:15], a[140:143], v[224:227]\n\t"
      "s_waitcnt lgkmcnt(8)\n\t"
      "v_mfma_f32_16x16x32_bf16 v[124:127], a[16:19], a[136:139], v[124:127]\n\t"
      "v_mfma_f32_16x16x32_bf16 v[120:123], a[16:19], a[132:135], v[120:123]\n\t"
      "ds_read_b64_tr_b16 a[4:5], v5 offset:36864\n\t"
      "v_mfma_f32_16x16x32_bf16 v[116:119], a[16:19], a[128:131], v[116:119]\n\t"
      "ds_read_b64_tr_b16 a[6:7], v5 offset:43008\n\t"
      "v_mfma_f32_16x16x32_bf16 v[112:115], a[16:19], a[124:127], v[112:115]\n\t"
      "s_waitcnt lgkmcnt(8)\n\t"
      "v_mfma_f32_16x16x32_bf16 v[160:163], a[124:127], a[20:23], v[160:163]\n\t"
      "buffer_load_dwordx4 v1, s[36:39], s22 offen offset:2048 lds\n\t"
      "v_mfma_f32_16x16x32_bf16 v[164:167], a[128:131], a[20:23], v[164:167]\n\t"
      "ds_read_b64_tr_b16 a[8:9], v6 offset:2048\n\t"
      "v_mfma_f32_16x16x32_bf16 v[168:171], a[132:135], a[20:23], v[168:171]\n\t"
      "ds_read_b64_tr_b16 a[10:11], v6 offset:8192\n\t"
      "v_mfma_f32_16x16x32_bf16 v[172:175], a[136:139], a[20:23], v[172:175]\n\t"
      "s_waitcnt lgkmcnt(8)\n\t"
      "v_mfma_f32_16x16x32_bf16 v[228:231], a[140:143], a[24:27], v[228:231]\n\t"
      "s_waitcnt lgkmcnt(6)\n\t"
      "v_mfma_f32_16x16x32_bf16 v[188:191], a[136:139], a[28:31], v[188:191]\n\t"
      "ds_read_b64_tr_b16 a[12:13], v7 offset:2048\n\t"
      "v_mfma_f32_16x16x32_bf16 v[184:187], a[132:135], a[28:31], v[184:187]\n\t"
      "ds_read_b64_tr_b16 a[14:15], v7 offset:8192\n\t"
      "buffer_load_dwordx4 v1, s[36:39], s23 offen offset:3072 lds\n\t"
      "s_add_u32 s24, s24, 16384\n\t"
      "s_and_b32 s24, s24, 0x1ffff\n\t"
      "v_readlane_b32 s54, v23, 2\n\t"
      "v_mfma_f32_16x16x32_bf16 v[180:183], a[128:131], a[28:31], v[180:183]\n\t"
      "s_bitcmp1_b32 s54, 0\n\t"
      "s_cselect_b64 s[36:37], s[80:81], s[84:85]\n\t"
      "s_cselect_b32 s38, s82, s86\n\t"
      "s_and_b32 s54, s54, -2\n\t"
      "v_add_u32 v1, s54, v17\n\t"
      "v_mfma_f32_16x16x32_bf16 v[176:179], a[124:127], a[28:31], v[176:179]\n\t"
      "s_waitcnt lgkmcnt(6)\n\t"
      "v_mfma_f32_16x16x32_bf16 v[32:35], a[0:3], a[124:127], v[32:35]\n\t"
      "ds_read_b64_tr_b16 a[16:17], v6 offset:4096\n\t"
      "v_mfma_f32_16x16x32_bf16 v[36:39], a[0:3], a[128:131], v[36:39]\n\t"
      "ds_read_b64_tr_b16 a[18:19], v6 offset:10240\n\t"
      "v_mfma_f32_16x16x32_bf16 v[40:43], a[0:3], a[132:135], v[40:43]\n\t"
      "v_mfma_f32_16x16x32_bf16 v[44:47], a[0:3], a[136:139], v[44:47]\n\t"
      "s_waitcnt lgkmcnt(6)\n\t"
      "v_mfma_f32_16x16x32_bf16 v[60:63], a[4:7], a[136:139], v[60:63]\n\t"
      "ds_read_b64_tr_b16 a[20:21], v7 offset:4096\n\t"
      "v_mfma_f32_16x16x32_bf16 v[56:59], a[4:7], a[132:135], v[56:59]\n\t"
      "ds_read_b64_tr_b16 a[22:23], v7 offset:10240\n\t"
      "v_mfma_f32_16x16x32_bf16 v[52:55], a[4:7], a[128:131], v[52:55]\n\t"
      "buffer_load_dwordx4 a[48:51], v25, s[88:91], 0 offen offset:0\n\t"
      "v_mfma_f32_16x16x32_bf16 v[48:51], a[4:7], a[124:127], v[48:51]\n\t"
      "s_waitcnt lgkmcnt(6)\n\t"
      "v_mfma_f32_16x16x32_bf16 v[128:131], a[8:11], a[64:67], v[128:131]\n\t"
      "ds_read_b64_tr_b16 a[24:25], v6 offset:14336\n\t"
      "v_mfma_f32_16x16x32_bf16 v[132:135], a[8:11], a[68:71], v[132:135]\n\t"
      "ds_read_b64_tr_b16 a[26:27], v6 offset:20480\n\t"
      "v_mfma_f32_16x16x32_bf16 v[136:139], a[8:11], a[72:75], v[136:139]\n\t"
      "v_cvt_pk_bf16_f32 v252, v224, v225\n\t"
      "v_cvt_pk_bf16_f32 v253, v226, v227\n\t"
      "v_cvt_pk_bf16_f32 v254, v228, v229\n\t"
      "v_cvt_pk_bf16_f32 v255, v230, v231\n\t"
      "v_mfma_f32_16x16x32_bf16 v[140:143], a[8:11], a[76:79], v[140:143]\n\t"
      "s_waitcnt lgkmcnt(6)\n\t"
      "ds_write_b64 v30, v[252:253]\n\t"
      "ds_write_b64 v30, v[254:255] offset:2048\n\t"
      "v_cvt_pk_bf16_f32 v248, v108, v109\n\t"
      "v_cvt_pk_bf16_f32 v249, v110, v111\n\t"
      "v_mfma_f32_16x16x32_bf16 v[156:159], a[12:15], a[76:79], v[156:159]\n\t"
      "ds_read_b64_tr_b16 a[28:29], v7 offset:14336\n\t"
      "v_cvt_pk_bf16_f32 v250, v124, v125\n\t"
      "v_cvt_pk_bf16_f32 v251, v126, v127\n\t"
      "v_cvt_pk_bf16_f32 v236, v96, v97\n\t"
      "v_mfma_f32_16x16x32_bf16 v[152:155], a[12:15], a[72:75], v[152:155]\n\t"
      "ds_read_b64_tr_b16 a[30:31], v7 offset:20480\n\t"
      "v_cvt_pk_bf16_f32 v237, v98, v99\n\t"
      "v_cvt_pk_bf16_f32 v238, v112, v113\n\t"
      "v_cvt_pk_bf16_f32 v239, v114, v115\n\t"
      "v_mfma_f32_16x16x32_bf16 v[148:151], a[12:15], a[68:71], v[148:151]\n\t"
      "buffer_load_dwordx4 a[52:55], v25, s[88:91], 0 offen offset:64\n\t"
      "v_cvt_pk_bf16_f32 v240, v100, v101\n\t"
      "v_cvt_pk_bf16_f32 v241, v102, v103\n\t"
      "v_cvt_pk_bf16_f32 v242, v116, v117\n\t"
      "v_mfma_f32_16x16x32_bf16 v[144:147], a[12:15], a[64:67], v[144:147]\n\t"
      "s_waitcnt lgkmcnt(8)\n\t"
      "v_cvt_pk_bf16_f32 v243, v118, v119\n\t"
      "v_cvt_pk_bf16_f32 v244, v104, v105\n\t"
      "v_cvt_pk_bf16_f32 v245, v106, v107\n\t"
      "v_cvt_pk_bf16_f32 v246, v120, v121\n\t"
      "v_mfma_f32_16x16x32_bf16 v[192:195], a[64:67], a[16:19], v[192:195]\n\t"
      "ds_read_b64_tr_b16 a[0:1], v6 offset:16384\n\t"
      "v_cvt_pk_bf16_f32 v247, v122, v123\n\t"
      "ds_write_b64 v24, v[248:249] offset:0\n\t"
      "ds_write_b64 v24, v[250:251] offset:512\n\t"
      "v_mfma_f32_16x16x32_bf16 v[196:199], a[68:71], a[16:19], v[196:199]\n\t"
      "ds_read_b64_tr_b16 a[2:3], v6 offset:22528\n\t"
      "v_cvt_pk_bf16_f32 v104, v172, v173\n\t"
      "v_cvt_pk_bf16_f32 v105, v174, v175\n\t"
      "v_cvt_pk_bf16_f32 v114, v188, v189\n\t"
      "v_mfma_f32_16x16x32_bf16 v[200:203], a[72:75], a[16:19], v[200:203]\n\t"
      "v_cvt_pk_bf16_f32 v115, v190, v191\n\t"
      "ds_write_b64 v24, v[104:105] offset:2048\n\t"
      "ds_write_b64 v24, v[114:115] offset:2560\n\t"
      "v_cvt_pk_bf16_f32 v98, v160, v161\n\t"
      "v_mfma_f32_16x16x32_bf16 v[204:207], a[76:79], a[16:19], v[204:207]\n\t"
      "s_waitcnt lgkmcnt(12)\n\t"
      "v_cvt_pk_bf16_f32 v99, v162, v163\n\t"
      "v_cvt_pk_bf16_f32 v108, v176, v177\n\t"
      "v_cvt_pk_bf16_f32 v109, v178, v179\n\t"
      "v_cvt_pk_bf16_f32 v100, v164, v165\n\t"
      "v_mfma_f32_16x16x32_bf16 v[220:223], a[76:79], a[20:23], v[220:223]\n\t"
      "ds_read_b64_tr_b16 a[4:5], v7 offset:16384\n\t"
      "v_cvt_pk_bf16_f32 v101, v166, v167\n\t"
      "v_cvt_pk_bf16_f32 v110, v180, v181\n\t"
      "v_cvt_pk_bf16_f32 v111, v182, v183\n\t"
      "v_mfma_f32_16x16x32_bf16 v[216:219], a[72:75], a[20:23], v[216:219]\n\t"
      "ds_read_b64_tr_b16 a[6:7], v7 offset:22528\n\t"
      "v_cvt_pk_bf16_f32 v102, v168, v169\n\t"
      "v_cvt_pk_bf16_f32 v103, v170, v171\n\t"
      "v_cvt_pk_bf16_f32 v112, v184, v185\n\t"
      "v_mfma_f32_16x16x32_bf16 v[212:215], a[68:71], a[20:23], v[212:215]\n\t"
      "s_lshl_b32 s3, s32, 12\n\t"
      "s_add_u32 s3, s3, 147456\n\t"
      "s_add_u32 m0, s3, 0\n\t"
      "s_add_u32 s5, s70, 0\n\t"
      "buffer_load_dwordx4 v17, s[72:75], s5 offen lds\n\t"
      "v_mfma_f32_16x16x32_bf16 v[208:211], a[64:67], a[20:23], v[208:211]\n\t"
      "s_waitcnt lgkmcnt(12)\n\t"
      "v_cvt_pk_bf16_f32 v113, v186, v187\n\t"
      "s_waitcnt lgkmcnt(2)\n\t"
      "s_barrier\n\t"
      "ds_read_b64 v[232:233], v29 offset:0\n\t"
      "v_mfma_f32_16x16x32_bf16 v[128:131], a[24:27], a[84:87], v[128:131]\n\t"
      "ds_read_b64_tr_b16 a[8:9], v6 offset:12288\n\t"
      "ds_read_b64 v[96:97], v29 offset:2048\n\t"
      "ds_read_b64 v[234:235], v29 offset:512\n\t"
      "ds_read_b64 v[106:107], v29 offset:2560\n\t"
      "v_mfma_f32_16x16x32_bf16 v[132:135], a[24:27], a[88:91], v[132:135]\n\t"
      "ds_read_b64_tr_b16 a[10:11], v6 offset:18432\n\t"
      "v_cvt_pk_bf16_f32 v160, v32, v33\n\t"
      "v_cvt_pk_bf16_f32 v161, v34, v35\n\t"
      "v_cvt_pk_bf16_f32 v162, v48, v49\n\t"
      "v_mfma_f32_16x16x32_bf16 v[136:139], a[24:27], a[92:95], v[136:139]\n\t"
      "v_cvt_pk_bf16_f32 v163, v50, v51\n\t"
      "v_cvt_pk_bf16_f32 v164, v36, v37\n\t"
      "v_cvt_pk_bf16_f32 v165, v38, v39\n\t"
      "v_cvt_pk_bf16_f32 v166, v52, v53\n\t"
      "v_mfma_f32_16x16x32_bf16 v[140:143], a[24:27], a[96:99], v[140:143]\n\t"
      "s_waitcnt lgkmcnt(14)\n\t"
      "v_cvt_pk_bf16_f32 v167, v54, v55\n\t"
      "v_cvt_pk_bf16_f32 v168, v40, v41\n\t"
      "v_cvt_pk_bf16_f32 v169, v42, v43\n\t"
      "v_cvt_pk_bf16_f32 v170, v56, v57\n\t"
      "v_mfma_f32_16x16x32_bf16 v[156:159], a[28:31], a[96:99], v[156:159]\n\t"
      "ds_read_b64_tr_b16 a[12:13], v7 offset:12288\n\t"
      "v_cvt_pk_bf16_f32 v171, v58, v59\n\t"
      "v_cvt_pk_bf16_f32 v172, v44, v45\n\t"
      "v_cvt_pk_bf16_f32 v173, v46, v47\n\t"
      "v_mfma_f32_16x16x32_bf16 v[152:155], a[28:31], a[92:95], v[152:155]\n\t"
      "ds_read_b64_tr_b16 a[14:15], v7 offset:18432\n\t"
      "v_cvt_pk_bf16_f32 v174, v60, v61\n\t"
      "v_cvt_pk_bf16_f32 v175, v62, v63\n\t"
      "s_mov_b32 s10, 0xaaaaaaaa\n\t"
      "s_mov_b32 s11, 0xaaaaaaaa\n\t"
      "v_mfma_f32_16x16x32_bf16 v[148:151], a[28:31], a[88:91], v[148:151]\n\t"
      "s_add_u32 m0, s3, 1024\n\t"
      "s_add_u32 s5, s70, 16384\n\t"
      "buffer_load_dwordx4 v17, s[72:75], s5 offen lds\n\t"
      "s_mov_b32 s12, 0xfff0ffff\n\t"
      "s_mov_b32 s13, 0xf000ff00\n\t"
      "v_mfma_f32_16x16x32_bf16 v[144:147], a[28:31], a[84:87], v[144:147]\n\t"
      "s_waitcnt lgkmcnt(12)\n\t"
      "s_mov_b32 s14, 0xffc0fffc\n\t"
      "s_mov_b32 s15, 0xc000fc00\n\t"
      "s_mov_b32 s17, 0x3e8293ee\n\t"
      "v_mov_b32 v125, 0xffff\n\t"
      "v_mfma_f32_16x16x32_bf16 v[192:195], a[84:87], a[0:3], v[192:195]\n\t"
      "ds_read_b64_tr_b16 a[16:17], v6 offset:26624\n\t"
      "v_mov_b32 v126, 0xffff0000\n\t"
      "v_cndmask_b32 v120, v125, v126, s[10:11]\n\t"
      "v_cndmask_b32 v121, v120, 0, s[12:13]\n\t"
      "v_mfma_f32_16x16x32_bf16 v[196:199], a[88:91], a[0:3], v[196:199]\n\t"
      "ds_read_b64_tr_b16 a[18:19], v6 offset:32768\n\t"
      "v_cndmask_b32 v123, 0, v120, s[12:13]\n\t"
      "v_cndmask_b32 v122, v120, 0, s[14:15]\n\t"
      "v_cndmask_b32 v124, 0, v120, s[14:15]\n\t"
      "v_mfma_f32_16x16x32_bf16 v[200:203], a[92:95], a[0:3], v[200:203]\n\t"
      "v_mov_b32 v116, s28\n\t"
      "v_mov_b32 v117, s28\n\t"
      "v_mov_b32 v118, s28\n\t"
      "v_mov_b32 v119, s28\n\t"
      "v_mfma_f32_16x16x32_bf16 v[204:207], a[96:99], a[0:3], v[204:207]\n\t"
      "s_waitcnt lgkmcnt(10)\n\t"
      "v_mfma_f32_16x16x32_bf16 v[36:39], v[236:239], v[164:167], 0\n\t"
      "v_mfma_f32_16x16x32_bf16 v[220:223], a[96:99], a[4:7], v[220:223]\n\t"
      "ds_read_b64_tr_b16 a[20:21], v7 offset:26624\n\t"
      "v_mfma_f32_16x16x32_bf16 v[52:55], v[240:243], v[164:167], 0\n\t"
      "v_mfma_f32_16x16x32_bf16 v[216:219], a[92:95], a[4:7], v[216:219]\n\t"
      "ds_read_b64_tr_b16 a[22:23], v7 offset:32768\n\t"
      "v_mfma_f32_16x16x32_bf16 v[40:43], v[240:243], v[168:171], 0\n\t"
      "v_mfma_f32_16x16x32_bf16 v[212:215], a[88:91], a[4:7], v[212:215]\n\t"
      "s_add_u32 m0, s3, 2048\n\t"
      "s_add_u32 s5, s70, 32768\n\t"
      "buffer_load_dwordx4 v17, s[72:75], s5 offen lds\n\t"
      "v_mfma_f32_16x16x32_bf16 v[56:59], v[244:247], v[168:171], 0\n\t"
      "v_mfma_f32_16x16x32_bf16 v[208:211], a[84:87], a[4:7], v[208:211]\n\t"
      "s_waitcnt lgkmcnt(6)\n\t"
      "v_mfma_f32_16x16x32_bf16 v[44:47], v[244:247], v[172:175], 0\n\t"
      "v_mfma_f32_16x16x32_bf16 v[64:67], a[8:11], a[84:87], v[64:67]\n\t"
      "ds_read_b64_tr_b16 a[24:25], v6 offset:28672\n\t"
      "v_mfma_f32_16x16x32_bf16 v[60:63], v[248:251], v[172:175], 0\n\t"
      "v_mfma_f32_16x16x32_bf16 v[68:71], a[8:11], a[88:91], v[68:71]\n\t"
      "ds_read_b64_tr_b16 a[26:27], v6 offset:34816\n\t"
      "s_waitcnt lgkmcnt(9)\n\t"
      "v_mfma_f32_16x16x32_bf16 v[32:35], v[232:235], v[160:163], v[116:119]\n\t"
      "v_mfma_f32_16x16x32_bf16 v[72:75], a[8:11], a[92:95], v[72:75]\n\t"
      "v_mfma_f32_16x16x32_bf16 v[48:51], v[236:239], v[160:163], 0\n\t"
      "v_mfma_f32_16x16x32_bf16 v[76:79], a[8:11], a[96:99], v[76:79]\n\t"
      "s_waitcnt lgkmcnt(6)\n\t"
      "v_cndmask_b32 v36, v36, v37, s[10:11]\n\t"
      "v_cndmask_b32 v52, v52, v53, s[10:11]\n\t"
      "v_cndmask_b32 v38, v38, v39, s[10:11]\n\t"
      "v_cndmask_b32 v54, v54, v55, s[10:11]\n\t"
      "v_mfma_f32_16x16x32_bf16 v[92:95], a[12:15], a[96:99], v[92:95]\n\t"
      "ds_read_b64_tr_b16 a[28:29], v7 offset:28672\n\t"
      "v_cndmask_b32 v178, v36, v52, s[12:13]\n\t"
      "v_cndmask_b32 v179, v38, v54, s[14:15]\n\t"
      "v_max_f32 v185, v178, v179\n\t"
      "v_mfma_f32_16x16x32_bf16 v[88:91], a[12:15], a[92:95], v[88:91]\n\t"
      "ds_read_b64_tr_b16 a[30:31], v7 offset:34816\n\t"
      "v_cndmask_b32 v40, v40, v41, s[10:11]\n\t"
      "v_cndmask_b32 v56, v56, v57, s[10:11]\n\t"
      "v_cndmask_b32 v42, v42, v43, s[10:11]\n\t"
      "v_mfma_f32_16x16x32_bf16 v[84:87], a[12:15], a[88:91], v[84:87]\n\t"
      "s_add_u32 m0, s3, 3072\n\t"
      "s_add_u32 s5, s70, 49152\n\t"
      "buffer_load_dwordx4 v17, s[72:75], s5 offen lds\n\t"
      "v_cndmask_b32 v58, v58, v59, s[10:11]\n\t"
      "v_cndmask_b32 v180, v40, v56, s[12:13]\n\t"
      "v_mfma_f32_16x16x32_bf16 v[80:83], a[12:15], a[84:87], v[80:83]\n\t"
      "s_waitcnt lgkmcnt(6)\n\t"
      "v_cndmask_b32 v181, v42, v58, s[14:15]\n\t"
      "v_max_f32 v186, v180, v181\n\t"
      "v_cndmask_b32 v44, v44, v45, s[10:11]\n\t"
      "v_cndmask_b32 v60, v60, v61, s[10:11]\n\t"
      "v_mfma_f32_16x16x32_bf16 v[128:131], a[16:19], a[104:107], v[128:131]\n\t"
      "ds_read_b64_tr_b16 a[0:1], v6 offset:24576\n\t"
      "v_cndmask_b32 v46, v46, v47, s[10:11]\n\t"
      "v_cndmask_b32 v62, v62, v63, s[10:11]\n\t"
      "v_cndmask_b32 v182, v44, v60, s[12:13]\n\t"
      "v_mfma_f32_16x16x32_bf16 v[132:135], a[16:19], a[108:111], v[132:135]\n\t"
      "ds_read_b64_tr_b16 a[2:3], v6 offset:30720\n\t"
      "v_cndmask_b32 v183, v46, v62, s[14:15]\n\t"
      "v_max_f32 v187, v182, v183\n\t"
      "v_cndmask_b32 v32, v32, v33, s[10:11]\n\t"
      "v_mfma_f32_16x16x32_bf16 v[136:139], a[16:19], a[112:115], v[136:139]\n\t"
      "v_cndmask_b32 v48, v48, v49, s[10:11]\n\t"
      "v_cndmask_b32 v34, v34, v35, s[10:11]\n\t"
      "v_cndmask_b32 v50, v50, v51, s[10:11]\n\t"
      "v_cndmask_b32 v176, v32, v48, s[12:13]\n\t"
      "v_mfma_f32_16x16x32_bf16 v[140:143], a[16:19], a[116:119], v[140:143]\n\t"
      "s_waitcnt lgkmcnt(6)\n\t"
      "v_cndmask_b32 v177, v34, v50, s[14:15]\n\t"
      "v_max_f32 v184, v176, v177\n\t"
      "v_permlane16_swap_b32 v186, v187\n\t"
      "s_nop 0\n\t"
      "v_permlane16_swap_b32 v184, v185\n\t"
      "v_mfma_f32_16x16x32_bf16 v[156:159], a[20:23], a[116:119], v[156:159]\n\t"
      "ds_read_b64_tr_b16 a[4:5], v7 offset:24576\n\t"
      "v_max_f32 v186, v186, v187\n\t"
      "v_max_f32 v184, v184, v185\n\t"
      "s_nop 1\n\t"
      "v_permlane32_swap_b32 v184, v186\n\t"
      "v_mfma_f32_16x16x32_bf16 v[152:155], a[20:23], a[112:115], v[152:155]\n\t"
      "ds_read_b64_tr_b16 a[6:7], v7 offset:30720\n\t"
      "v_max_f32 v184, v184, v186\n\t"
      "v_mul_f32 v184, s17, v184\n\t"
      "v_mov_b32 v186, v184\n\t"
      "v_mfma_f32_16x16x32_bf16 v[148:151], a[20:23], a[108:111], v[148:151]\n\t"
      "s_nop 0\n\t"
      "v_permlane32_swap_b32 v184, v186\n\t"
      "v_mov_b32 v185, v184\n\t"
      "v_mov_b32 v187, v186\n\t"
      "s_nop 0\n\t"
      "v_permlane16_swap_b32 v184, v185\n\t"
      "v_mfma_f32_16x16x32_bf16 v[144:147], a[20:23], a[104:107], v[144:147]\n\t"
      "s_waitcnt lgkmcnt(6)\n\t"
      "v_permlane16_swap_b32 v186, v187\n\t"
      "v_fma_f32 v176, v176, s17, -v184\n\t"
      "v_fma_f32 v177, v177, s17, -v184\n\t"
      "v_fma_f32 v178, v178, s17, -v185\n\t"
      "v_mfma_f32_16x16x32_bf16 v[192:195], a[104:107], a[24:27], v[192:195]\n\t"
      "ds_read_b64_tr_b16 a[8:9], v6 offset:38912\n\t"
      "v_fma_f32 v179, v179, s17, -v185\n\t"
      "v_fma_f32 v180, v180, s17, -v186\n\t"
      "v_fma_f32 v181, v181, s17, -v186\n\t"
      "v_mfma_f32_16x16x32_bf16 v[196:199], a[108:111], a[24:27], v[196:199]\n\t"
      "ds_read_b64_tr_b16 a[10:11], v6 offset:45056\n\t"
      "v_fma_f32 v182, v182, s17, -v187\n\t"
      "v_fma_f32 v183, v183, s17, -v187\n\t"
      "v_exp_f32 v176, v176\n\t"
      "v_mfma_f32_16x16x32_bf16 v[200:203], a[112:115], a[24:27], v[200:203]\n\t"
      "v_exp_f32 v177, v177\n\t"
      "v_exp_f32 v178, v178\n\t"
      "v_exp_f32 v179, v179\n\t"
      "v_exp_f32 v180, v180\n\t"
      "v_mfma_f32_16x16x32_bf16 v[204:207], a[116:119], a[24:27], v[204:207]\n\t"
      "s_waitcnt lgkmcnt(6)\n\t"
      "v_exp_f32 v181, v181\n\t"
      "v_exp_f32 v182, v182\n\t"
      "v_exp_f32 v183, v183\n\t"
      "v_add_f32 v184, v176, v177\n\t"
      "v_mfma_f32_16x16x32_bf16 v[220:223], a[116:119], a[28:31], v[220:223]\n\t"
      "ds_read_b64_tr_b16 a[12:13], v7 offset:38912\n\t"
      "v_add_f32 v185, v178, v179\n\t"
      "v_add_f32 v186, v180, v181\n\t"
      "v_add_f32 v187, v182, v183\n\t"
      "v_mfma_f32_16x16x32_bf16 v[216:219], a[112:115], a[28:31], v[216:219]\n\t"
      "ds_read_b64_tr_b16 a[14:15], v7 offset:45056\n\t"
      "v_permlane16_swap_b32 v186, v187\n\t"
      "v_permlane16_swap_b32 v184, v185\n\t"
      "v_add_f32 v186, v186, v187\n\t"
      "v_mfma_f32_16x16x32_bf16 v[212:215], a[108:111], a[28:31], v[212:215]\n\t"
      "v_add_f32 v184, v184, v185\n\t"
      "s_nop 1\n\t"
      "v_permlane32_swap_b32 v184, v186\n\t"
      "v_add_f32 v184, v184, v186\n\t"
      "v_rcp_f32 v184, v184\n\t"
      "v_mfma_f32_16x16x32_bf16 v[208:211], a[104:107], a[28:31], v[208:211]\n\t"
      "s_waitcnt lgkmcnt(6)\n\t"
      "v_mov_b32 v186, v184\n\t"
      "s_nop 1\n\t"
      "v_permlane32_swap_b32 v184, v186\n\t"
      "v_mov_b32 v185, v184\n\t"
      "v_mov_b32 v187, v186\n\t"
      "v_mfma_f32_16x16x32_bf16 v[64:67], a[0:3], a[104:107], v[64:67]\n\t"
      "ds_read_b64_tr_b16 a[16:17], v6 offset:40960\n\t"
      "v_permlane16_swap_b32 v184, v185\n\t"
      "v_permlane16_swap_b32 v186, v187\n\t"
      "v_mul_f32 v176, v176, v184\n\t"
      "v_mfma_f32_16x16x32_bf16 v[68:71], a[0:3], a[108:111], v[68:71]\n\t"
      "ds_read_b64_tr_b16 a[18:19], v6 offset:47104\n\t"
      "v_mul_f32 v177, v177, v184\n\t"
      "v_mul_f32 v178, v178, v185\n\t"
      "v_mul_f32 v179, v179, v185\n\t"
      "v_mfma_f32_16x16x32_bf16 v[72:75], a[0:3], a[112:115], v[72:75]\n\t"
      "v_mul_f32 v180, v180, v186\n\t"
      "v_mul_f32 v181, v181, v186\n\t"
      "v_mul_f32 v182, v182, v187\n\t"
      "v_mul_f32 v183, v183, v187\n\t"
      "v_mfma_f32_16x16x32_bf16 v[76:79], a[0:3], a[116:119], v[76:79]\n\t"
      "s_waitcnt lgkmcnt(6)\n\t"
      "v_cvt_pk_bf16_f32 v176, v176, v176\n\t"
      "v_cvt_pk_bf16_f32 v177, v177, v177\n\t"
      "v_cvt_pk_bf16_f32 v178, v178, v178\n\t"
      "v_cvt_pk_bf16_f32 v179, v179, v179\n\t"
      "v_mfma_f32_16x16x32_bf16 v[92:95], a[4:7], a[116:119], v[92:95]\n\t"
      "ds_read_b64_tr_b16 a[20:21], v7 offset:40960\n\t"
      "v_cvt_pk_bf16_f32 v180, v180, v180\n\t"
      "v_cvt_pk_bf16_f32 v181, v181, v181\n\t"
      "v_cvt_pk_bf16_f32 v182, v182, v182\n\t"
      "v_mfma_f32_16x16x32_bf16 v[88:91], a[4:7], a[112:115], v[88:91]\n\t"
      "ds_read_b64_tr_b16 a[22:23], v7 offset:47104\n\t"
      "v_cvt_pk_bf16_f32 v183, v183, v183\n\t"
      "v_and_b32 v32, v176, v121\n\t"
      "v_and_b32 v34, v176, v123\n\t"
      "v_mfma_f32_16x16x32_bf16 v[84:87], a[4:7], a[108:111], v[84:87]\n\t"
      "v_and_b32 v33, v177, v122\n\t"
      "v_and_b32 v35, v177, v124\n\t"
      "v_and_b32 v36, v178, v121\n\t"
      "v_and_b32 v38, v178, v123\n\t"
      "v_mfma_f32_16x16x32_bf16 v[80:83], a[4:7], a[104:107], v[80:83]\n\t"
      "s_waitcnt lgkmcnt(6)\n\t"
      "v_and_b32 v37, v179, v122\n\t"
      "v_and_b32 v39, v179, v124\n\t"
      "v_and_b32 v40, v180, v121\n\t"
      "v_and_b32 v42, v180, v123\n\t"
      "v_mfma_f32_16x16x32_bf16 v[128:131], a[8:11], a[124:127], v[128:131]\n\t"
      "ds_read_b64_tr_b16 a[24:25], v6 offset:36864\n\t"
      "v_and_b32 v41, v181, v122\n\t"
      "v_and_b32 v43, v181, v124\n\t"
      "v_and_b32 v44, v182, v121\n\t"
      "v_mfma_f32_16x16x32_bf16 v[132:135], a[8:11], a[128:131], v[132:135]\n\t"
      "ds_read_b64_tr_b16 a[26:27], v6 offset:43008\n\t"
      "v_and_b32 v46, v182, v123\n\t"
      "v_and_b32 v45, v183, v122\n\t"
      "v_and_b32 v47, v183, v124\n\t"
      "v_mfma_f32_16x16x32_bf16 v[136:139], a[8:11], a[132:135], v[136:139]\n\t"
      "v_mfma_f32_16x16x32_bf16 v[48:51], v[96:99], v[32:35], 0\n\t"
      "v_mfma_f32_16x16x32_bf16 v[140:143], a[8:11], a[136:139], v[140:143]\n\t"
      "s_waitcnt lgkmcnt(6)\n\t"
      "v_mfma_f32_16x16x32_bf16 v[160:163], v[106:109], v[32:35], 0\n\t"
      "v_mfma_f32_16x16x32_bf16 v[156:159], a[12:15], a[136:139], v[156:159]\n\t"
      "ds_read_b64_tr_b16 a[28:29], v7 offset:36864\n\t"
      "v_mfma_f32_16x16x32_bf16 v[52:55], v[98:101], v[36:39], 0\n\t"
      "v_mfma_f32_16x16x32_bf16 v[152:155], a[12:15], a[132:135], v[152:155]\n\t"
      "ds_read_b64_tr_b16 a[30:31], v7 offset:43008\n\t"
      "v_mfma_f32_16x16x32_bf16 v[164:167], v[108:111], v[36:39], 0\n\t"
      "v_mfma_f32_16x16x32_bf16 v[148:151], a[12:15], a[128:131], v[148:151]\n\t"
      "v_mfma_f32_16x16x32_bf16 v[56:59], v[100:103], v[40:43], 0\n\t"
      "v_mfma_f32_16x16x32_bf16 v[144:147], a[12:15], a[124:127], v[144:147]\n\t"
      "s_waitcnt lgkmcnt(6)\n\t"
      "v_mfma_f32_16x16x32_bf16 v[168:171], v[110:113], v[40:43], 0\n\t"
      "v_mfma_f32_16x16x32_bf16 v[192:195], a[124:127], a[16:19], v[192:195]\n\t"
      "v_mfma_f32_16x16x32_bf16 v[60:63], v[102:105], v[44:47], 0\n\t"
      "v_mfma_f32_16x16x32_bf16 v[196:199], a[128:131], a[16:19], v[196:199]\n\t"
      "v_mfma_f32_16x16x32_bf16 v[172:175], v[112:115], v[44:47], 0\n\t"
      "v_mfma_f32_16x16x32_bf16 v[200:203], a[132:135], a[16:19], v[200:203]\n\t"
      "v_cvt_pk_bf16_f32 v32, v48, v49\n\t"
      "v_cvt_pk_bf16_f32 v33, v50, v51\n\t"
      "v_cvt_pk_bf16_f32 v34, v160, v161\n\t"
      "v_cvt_pk_bf16_f32 v35, v162, v163\n\t"
      "v_mfma_f32_16x16x32_bf16 v[204:207], a[136:139], a[16:19], v[204:207]\n\t"
      "s_waitcnt lgkmcnt(4)\n\t"
      "buffer_store_dwordx4 v[32:35], v22, s[76:79], s30 offen offset:0 sc1\n\t"
      "v_cvt_pk_bf16_f32 v36, v52, v53\n\t"
      "v_cvt_pk_bf16_f32 v37, v54, v55\n\t"
      "v_cvt_pk_bf16_f32 v38, v164, v165\n\t"
      "v_mfma_f32_16x16x32_bf16 v[220:223], a[136:139], a[20:23], v[220:223]\n\t"
      "v_cvt_pk_bf16_f32 v39, v166, v167\n\t"
      "buffer_store_dwordx4 v[36:39], v22, s[76:79], s30 offen offset:1024 sc1\n\t"
      "v_cvt_pk_bf16_f32 v40, v56, v57\n\t"
      "v_cvt_pk_bf16_f32 v41, v58, v59\n\t"
      "v_mfma_f32_16x16x32_bf16 v[216:219], a[132:135], a[20:23], v[216:219]\n\t"
      "v_cvt_pk_bf16_f32 v42, v168, v169\n\t"
      "v_cvt_pk_bf16_f32 v43, v170, v171\n\t"
      "buffer_store_dwordx4 v[40:43], v22, s[76:79], s30 offen offset:2048 sc1\n\t"
      "v_cvt_pk_bf16_f32 v44, v60, v61\n\t"
      "v_mfma_f32_16x16x32_bf16 v[212:215], a[128:131], a[20:23], v[212:215]\n\t"
      "v_cvt_pk_bf16_f32 v45, v62, v63\n\t"
      "v_cvt_pk_bf16_f32 v46, v172, v173\n\t"
      "v_cvt_pk_bf16_f32 v47, v174, v175\n\t"
      "buffer_store_dwordx4 v[44:47], v22, s[76:79], s30 offen offset:3072 sc1\n\t"
      "v_mfma_f32_16x16x32_bf16 v[208:211], a[124:127], a[20:23], v[208:211]\n\t"
      "s_waitcnt lgkmcnt(2)\n\t"
      "v_mfma_f32_16x16x32_bf16 v[64:67], a[24:27], a[124:127], v[64:67]\n\t"
      "v_mfma_f32_16x16x32_bf16 v[68:71], a[24:27], a[128:131], v[68:71]\n\t"
      "v_mfma_f32_16x16x32_bf16 v[72:75], a[24:27], a[132:135], v[72:75]\n\t"
      "v_mfma_f32_16x16x32_bf16 v[76:79], a[24:27], a[136:139], v[76:79]\n\t"
      "s_waitcnt lgkmcnt(0)\n\t"
      "v_mfma_f32_16x16x32_bf16 v[92:95], a[28:31], a[136:139], v[92:95]\n\t"
      "v_mfma_f32_16x16x32_bf16 v[88:91], a[28:31], a[132:135], v[88:91]\n\t"
      "v_mfma_f32_16x16x32_bf16 v[84:87], a[28:31], a[128:131], v[84:87]\n\t"
      "v_mfma_f32_16x16x32_bf16 v[80:83], a[28:31], a[124:127], v[80:83]\n\t"
      "v_cvt_pk_bf16_f32 v248, v140, v141\n\t"
      "v_cvt_pk_bf16_f32 v249, v142, v143\n\t"
      "v_cvt_pk_bf16_f32 v250, v156, v157\n\t"
      "v_cvt_pk_bf16_f32 v251, v158, v159\n\t"
      "v_cvt_pk_bf16_f32 v236, v128, v129\n\t"
      "v_cvt_pk_bf16_f32 v237, v130, v131\n\t"
      "v_cvt_pk_bf16_f32 v238, v144, v145\n\t"
      "v_cvt_pk_bf16_f32 v239, v146, v147\n\t"
      "v_cvt_pk_bf16_f32 v240, v132, v133\n\t"
      "v_cvt_pk_bf16_f32 v241, v134, v135\n\t"
      "v_cvt_pk_bf16_f32 v242, v148, v149\n\t"
      "v_cvt_pk_bf16_f32 v243, v150, v151\n\t"
      "v_cvt_pk_bf16_f32 v244, v136, v137\n\t"
      "v_cvt_pk_bf16_f32 v245, v138, v139\n\t"
      "v_cvt_pk_bf16_f32 v246, v152, v153\n\t"
      "v_cvt_pk_bf16_f32 v247, v154, v155\n\t"
      "ds_write_b64 v24, v[248:249] offset:1024\n\t"
      "ds_write_b64 v24, v[250:251] offset:1536\n\t"
      "v_cvt_pk_bf16_f32 v104, v204, v205\n\t"
      "v_cvt_pk_bf16_f32 v105, v206, v207\n\t"
      "v_cvt_pk_bf16_f32 v114, v220, v221\n\t"
      "v_cvt_pk_bf16_f32 v115, v222, v223\n\t"
      "ds_write_b64 v24, v[104:105] offset:3072\n\t"
      "ds_write_b64 v24, v[114:115] offset:3584\n\t"
      "v_cvt_pk_bf16_f32 v98, v192, v193\n\t"
      "v_cvt_pk_bf16_f32 v99, v194, v195\n\t"
      "v_cvt_pk_bf16_f32 v108, v208, v209\n\t"
      "v_cvt_pk_bf16_f32 v109, v210, v211\n\t"
      "v_cvt_pk_bf16_f32 v100, v196, v197\n\t"
      "v_cvt_pk_bf16_f32 v101, v198, v199\n\t"
      "v_cvt_pk_bf16_f32 v110, v212, v213\n\t"
      "v_cvt_pk_bf16_f32 v111, v214, v215\n\t"
      "v_cvt_pk_bf16_f32 v102, v200, v201\n\t"
      "v_cvt_pk_bf16_f32 v103, v202, v203\n\t"
      "v_cvt_pk_bf16_f32 v112, v216, v217\n\t"
      "v_cvt_pk_bf16_f32 v113, v218, v219\n\t"
      "s_waitcnt lgkmcnt(0)\n\t"
      "s_waitcnt vmcnt(14)\n\t"
      "s_barrier\n\t"
      "ds_read_b64 v[232:233], v29 offset:1024\n\t"
      "ds_read_b64 v[96:97], v29 offset:3072\n\t"
      "ds_read_b64 v[234:235], v29 offset:1536\n\t"
      "ds_read_b64 v[106:107], v29 offset:3584\n\t"
      "s_mov_b32 m0, s24\n\t"
      "s_nop 0\n\t"
      "buffer_load_dwordx4 v1, s[36:39], s20 offen lds\n\t"
      "buffer_load_dwordx4 v1, s[36:39], s21 offen offset:1024 lds\n\t"
      "buffer_load_dwordx4 v1, s[36:39], s22 offen offset:2048 lds\n\t"
      "buffer_load_dwordx4 v1, s[36:39], s23 offen offset:3072 lds\n\t"
      "s_add_u32 s24, s24, 16384\n\t"
      "s_and_b32 s24, s24, 0x1ffff\n\t"
      "v_readlane_b32 s54, v23, 3\n\t"
      "s_bitcmp1_b32 s54, 0\n\t"
      "s_cselect_b64 s[36:37], s[80:81], s[84:85]\n\t"
      "s_cselect_b32 s38, s82, s86\n\t"
      "s_and_b32 s54, s54, -2\n\t"
      "v_add_u32 v1, s54, v17\n\t"
      "v_cvt_pk_bf16_f32 v160, v64, v65\n\t"
      "v_cvt_pk_bf16_f32 v161, v66, v67\n\t"
      "v_cvt_pk_bf16_f32 v162, v80, v81\n\t"
      "v_cvt_pk_bf16_f32 v163, v82, v83\n\t"
      "v_cvt_pk_bf16_f32 v164, v68, v69\n\t"
      "v_cvt_pk_bf16_f32 v165, v70, v71\n\t"
      "v_cvt_pk_bf16_f32 v166, v84, v85\n\t"
      "v_cvt_pk_bf16_f32 v167, v86, v87\n\t"
      "v_cvt_pk_bf16_f32 v168, v72, v73\n\t"
      "v_cvt_pk_bf16_f32 v169, v74, v75\n\t"
      "v_cvt_pk_bf16_f32 v170, v88, v89\n\t"
      "v_cvt_pk_bf16_f32 v171, v90, v91\n\t"
      "v_cvt_pk_bf16_f32 v172, v76, v77\n\t"
      "v_cvt_pk_bf16_f32 v173, v78, v79\n\t"
      "v_cvt_pk_bf16_f32 v174, v92, v93\n\t"
      "v_cvt_pk_bf16_f32 v175, v94, v95\n\t"
      "v_mfma_f32_16x16x32_bf16 v[68:71], v[236:239], v[164:167], 0\n\t"
      "v_mfma_f32_16x16x32_bf16 v[84:87], v[240:243], v[164:167], 0\n\t"
      "v_mfma_f32_16x16x32_bf16 v[72:75], v[240:243], v[168:171], 0\n\t"
      "v_mfma_f32_16x16x32_bf16 v[88:91], v[244:247], v[168:171], 0\n\t"
      "v_mfma_f32_16x16x32_bf16 v[76:79], v[244:247], v[172:175], 0\n\t"
      "v_mfma_f32_16x16x32_bf16 v[92:95], v[248:251], v[172:175], 0\n\t"
      "s_waitcnt lgkmcnt(0)\n\t"
      "v_mfma_f32_16x16x32_bf16 v[64:67], v[232:235], v[160:163], v[116:119]\n\t"
      "v_mfma_f32_16x16x32_bf16 v[80:83], v[236:239], v[160:163], 0\n\t"
      "v_cndmask_b32 v68, v68, v69, s[10:11]\n\t"
      "v_cndmask_b32 v84, v84, v85, s[10:11]\n\t"
      "v_cndmask_b32 v70, v70, v71, s[10:11]\n\t"
      "v_cndmask_b32 v86, v86, v87, s[10:11]\n\t"
      "v_cndmask_b32 v178, v68, v84, s[12:13]\n\t"
      "v_cndmask_b32 v179, v70, v86, s[14:15]\n\t"
      "v_max_f32 v185, v178, v179\n\t"
      "v_cndmask_b32 v72, v72, v73, s[10:11]\n\t"
      "v_cndmask_b32 v88, v88, v89, s[10:11]\n\t"
      "v_cndmask_b32 v74, v74, v75, s[10:11]\n\t"
      "v_cndmask_b32 v90, v90, v91, s[10:11]\n\t"
      "v_cndmask_b32 v180, v72, v88, s[12:13]\n\t"
      "v_cndmask_b32 v181, v74, v90, s[14:15]\n\t"
      "v_max_f32 v186, v180, v181\n\t"
      "v_cndmask_b32 v76, v76, v77, s[10:11]\n\t"
      "v_cndmask_b32 v92, v92, v93, s[10:11]\n\t"
      "v_cndmask_b32 v78, v78, v79, s[10:11]\n\t"
      "v_cndmask_b32 v94, v94, v95, s[10:11]\n\t"
      "v_cndmask_b32 v182, v76, v92, s[12:13]\n\t"
      "v_cndmask_b32 v183, v78, v94, s[14:15]\n\t"
      "v_max_f32 v187, v182, v183\n\t"
      "v_cndmask_b32 v64, v64, v65, s[10:11]\n\t"
      "v_cndmask_b32 v80, v80, v81, s[10:11]\n\t"
      "v_cndmask_b32 v66, v66, v67, s[10:11]\n\t"
      "v_cndmask_b32 v82, v82, v83, s[10:11]\n\t"
      "v_cndmask_b32 v176, v64, v80, s[12:13]\n\t"
      "v_cndmask_b32 v177, v66, v82, s[14:15]\n\t"
      "v_max_f32 v184, v176, v177\n\t"
      "v_permlane16_swap_b32 v186, v187\n\t"
      "s_nop 0\n\t"
      "v_permlane16_swap_b32 v184, v185\n\t"
      "v_max_f32 v186, v186, v187\n\t"
      "v_max_f32 v184, v184, v185\n\t"
      "s_nop 1\n\t"
      "v_permlane32_swap_b32 v184, v186\n\t"
      "v_max_f32 v184, v184, v186\n\t"
      "v_mul_f32 v184, s17, v184\n\t"
      "v_mov_b32 v186, v184\n\t"
      "s_nop 1\n\t"
      "v_permlane32_swap_b32 v184, v186\n\t"
      "v_mov_b32 v185, v184\n\t"
      "v_mov_b32 v187, v186\n\t"
      "s_nop 0\n\t"
      "v_permlane16_swap_b32 v184, v185\n\t"
      "v_permlane16_swap_b32 v186, v187\n\t"
      "v_fma_f32 v176, v176, s17, -v184\n\t"
      "v_fma_f32 v177, v177, s17, -v184\n\t"
      "v_fma_f32 v178, v178, s17, -v185\n\t"
      "v_fma_f32 v179, v179, s17, -v185\n\t"
      "v_fma_f32 v180, v180, s17, -v186\n\t"
      "v_fma_f32 v181, v181, s17, -v186\n\t"
      "v_fma_f32 v182, v182, s17, -v187\n\t"
      "v_fma_f32 v183, v183, s17, -v187\n\t"
      "v_exp_f32 v176, v176\n\t"
      "v_exp_f32 v177, v177\n\t"
      "v_exp_f32 v178, v178\n\t"
      "v_exp_f32 v179, v179\n\t"
      "v_exp_f32 v180, v180\n\t"
      "v_exp_f32 v181, v181\n\t"
      "v_exp_f32 v182, v182\n\t"
      "v_exp_f32 v183, v183\n\t"
      "v_add_f32 v184, v176, v177\n\t"
      "v_add_f32 v185, v178, v179\n\t"
      "v_add_f32 v186, v180, v181\n\t"
      "v_add_f32 v187, v182, v183\n\t"
      "s_nop 1\n\t"
      "v_permlane16_swap_b32 v186, v187\n\t"
      "v_permlane16_swap_b32 v184, v185\n\t"
      "v_add_f32 v186, v186, v187\n\t"
      "v_add_f32 v184, v184, v185\n\t"
      "s_nop 1\n\t"
      "v_permlane32_swap_b32 v184, v186\n\t"
      "v_add_f32 v184, v184, v186\n\t"
      "v_rcp_f32 v184, v184\n\t"
      "s_nop 0\n\t"
      "v_mov_b32 v186, v184\n\t"
      "s_nop 1\n\t"
      "v_permlane32_swap_b32 v184, v186\n\t"
      "v_mov_b32 v185, v184\n\t"
      "v_mov_b32 v187, v186\n\t"
      "s_nop 0\n\t"
      "v_permlane16_swap_b32 v184, v185\n\t"
      "v_permlane16_swap_b32 v186, v187\n\t"
      "v_mul_f32 v176, v176, v184\n\t"
      "v_mul_f32 v177, v177, v184\n\t"
      "v_mul_f32 v178, v178, v185\n\t"
      "v_mul_f32 v179, v179, v185\n\t"
      "v_mul_f32 v180, v180, v186\n\t"
      "v_mul_f32 v181, v181, v186\n\t"
      "v_mul_f32 v182, v182, v187\n\t"
      "v_mul_f32 v183, v183, v187\n\t"
      "v_cvt_pk_bf16_f32 v176, v176, v176\n\t"
      "v_cvt_pk_bf16_f32 v177, v177, v177\n\t"
      "v_cvt_pk_bf16_f32 v178, v178, v178\n\t"
      "v_cvt_pk_bf16_f32 v179, v179, v179\n\t"
      "v_cvt_pk_bf16_f32 v180, v180, v180\n\t"
      "v_cvt_pk_bf16_f32 v181, v181, v181\n\t"
      "v_cvt_pk_bf16_f32 v182, v182, v182\n\t"
      "v_cvt_pk_bf16_f32 v183, v183, v183\n\t"
      "v_and_b32 v64, v176, v121\n\t"
      "v_and_b32 v66, v176, v123\n\t"
      "v_and_b32 v65, v177, v122\n\t"
      "v_and_b32 v67, v177, v124\n\t"
      "v_and_b32 v68, v178, v121\n\t"
      "v_and_b32 v70, v178, v123\n\t"
      "v_and_b32 v69, v179, v122\n\t"
      "v_and_b32 v71, v179, v124\n\t"
      "v_and_b32 v72, v180, v121\n\t"
      "v_and_b32 v74, v180, v123\n\t"
      "v_and_b32 v73, v181, v122\n\t"
      "v_and_b32 v75, v181, v124\n\t"
      "v_and_b32 v76, v182, v121\n\t"
      "v_and_b32 v78, v182, v123\n\t"
      "v_and_b32 v77, v183, v122\n\t"
      "v_and_b32 v79, v183, v124\n\t"
      "v_mfma_f32_16x16x32_bf16 v[80:83], v[96:99], v[64:67], 0\n\t"
      "v_mfma_f32_16x16x32_bf16 v[160:163], v[106:109], v[64:67], 0\n\t"
      "v_mfma_f32_16x16x32_bf16 v[84:87], v[98:101], v[68:71], 0\n\t"
      "v_mfma_f32_16x16x32_bf16 v[164:167], v[108:111], v[68:71], 0\n\t"
      "v_mfma_f32_16x16x32_bf16 v[88:91], v[100:103], v[72:75], 0\n\t"
      "v_mfma_f32_16x16x32_bf16 v[168:171], v[110:113], v[72:75], 0\n\t"
      "v_mfma_f32_16x16x32_bf16 v[92:95], v[102:105], v[76:79], 0\n\t"
      "ds_read_b64_tr_b16 a[160:161], v18 offset:2048\n\t"
      "ds_read_b64_tr_b16 a[162:163], v18 offset:6144\n\t"
      "ds_read_b64_tr_b16 a[164:165], v19 offset:2048\n\t"
      "ds_read_b64_tr_b16 a[166:167], v19 offset:6144\n\t"
      "ds_read_b64_tr_b16 a[168:169], v20 offset:2048\n\t"
      "ds_read_b64_tr_b16 a[170:171], v20 offset:6144\n\t"
      "ds_read_b64_tr_b16 a[172:173], v21 offset:2048\n\t"
      "ds_read_b64_tr_b16 a[174:175], v21 offset:6144\n\t"
      "v_mfma_f32_16x16x32_bf16 v[172:175], v[112:115], v[76:79], 0\n\t"
      "v_cvt_pk_bf16_f32 v64, v80, v81\n\t"
      "v_cvt_pk_bf16_f32 v65, v82, v83\n\t"
      "v_cvt_pk_bf16_f32 v66, v160, v161\n\t"
      "v_cvt_pk_bf16_f32 v67, v162, v163\n\t"
      "buffer_load_dwordx2 v[2:3], v27, s[96:99], s16 offen offset:2048 sc1\n\t"
      "buffer_store_dwordx4 v[64:67], v22, s[76:79], s31 offen offset:0 sc1\n\t"
      "v_cvt_pk_bf16_f32 v68, v84, v85\n\t"
      "v_cvt_pk_bf16_f32 v69, v86, v87\n\t"
      "v_cvt_pk_bf16_f32 v70, v164, v165\n\t"
      "v_cvt_pk_bf16_f32 v71, v166, v167\n\t"
      "buffer_store_dwordx4 v[68:71], v22, s[76:79], s31 offen offset:1024 sc1\n\t"
      "v_cvt_pk_bf16_f32 v72, v88, v89\n\t"
      "v_cvt_pk_bf16_f32 v73, v90, v91\n\t"
      "v_cvt_pk_bf16_f32 v74, v168, v169\n\t"
      "v_cvt_pk_bf16_f32 v75, v170, v171\n\t"
      "buffer_store_dwordx4 v[72:75], v22, s[76:79], s31 offen offset:2048 sc1\n\t"
      "v_cvt_pk_bf16_f32 v76, v92, v93\n\t"
      "v_cvt_pk_bf16_f32 v77, v94, v95\n\t"
      "v_cvt_pk_bf16_f32 v78, v172, v173\n\t"
      "v_cvt_pk_bf16_f32 v79, v174, v175\n\t"
      "buffer_store_dwordx4 v[76:79], v22, s[76:79], s31 offen offset:3072 sc1\n\t"
      "s_cmp_eq_u32 s34, 0\n\t"
      "s_cbranch_scc1 Lsw_%=\n\t"
      "v_swap_b32 v32, v40\n\t"
      "v_swap_b32 v33, v41\n\t"
      "v_swap_b32 v34, v42\n\t"
      "v_swap_b32 v35, v43\n\t"
      "v_swap_b32 v36, v44\n\t"
      "v_swap_b32 v37, v45\n\t"
      "v_swap_b32 v38, v46\n\t"
      "v_swap_b32 v39, v47\n\t"
      "v_swap_b32 v64, v72\n\t"
      "v_swap_b32 v65, v73\n\t"
      "v_swap_b32 v66, v74\n\t"
      "v_swap_b32 v67, v75\n\t"
      "v_swap_b32 v68, v76\n\t"
      "v_swap_b32 v69, v77\n\t"
      "v_swap_b32 v70, v78\n\t"
      "v_swap_b32 v71, v79\n\t"
      "Lsw_%=:\n\t"
      STAMP(24, 25)
      "s_waitcnt lgkmcnt(6)\n\t"
      "v_mfma_f32_16x16x32_bf16 v[48:51], a[160:163], v[32:35], 0\n\t"
      "ds_read_b64_tr_b16 a[176:177], v18 offset:0\n\t"
      "v_readlane_b32 s53, v23, 33\n\t"
      "v_mfma_f32_16x16x32_bf16 v[52:55], a[160:163], v[36:39], 0\n\t"
      "ds_read_b64_tr_b16 a[178:179], v18 offset:4096\n\t"
      "v_subrev_u32 v4, s64, v18\n\t"
      "v_mfma_f32_16x16x32_bf16 v[56:59], a[160:163], v[40:43], 0\n\t"
      "v_add_u32 v0, s53, v16\n\t"
      "v_subrev_u32 v5, s64, v19\n\t"
      "v_mfma_f32_16x16x32_bf16 v[60:63], a[160:163], v[44:47], 0\n\t"
      "s_waitcnt lgkmcnt(6)\n\t"
      "s_mov_b32 m0, s24\n\t"
      "v_subrev_u32 v6, s64, v20\n\t"
      "v_mfma_f32_16x16x32_bf16 v[92:95], a[164:167], v[44:47], 0\n\t"
      "ds_read_b64_tr_b16 a[180:181], v19 offset:0\n\t"
      "buffer_load_dwordx4 v1, s[36:39], s20 offen lds\n\t"
      "v_mfma_f32_16x16x32_bf16 v[88:91], a[164:167], v[40:43], 0\n\t"
      "ds_read_b64_tr_b16 a[182:183], v19 offset:4096\n\t"
      "v_subrev_u32 v7, s64, v21\n\t"
      "v_mfma_f32_16x16x32_bf16 v[84:87], a[164:167], v[36:39], 0\n\t"
      "v_mfma_f32_16x16x32_bf16 v[80:83], a[164:167], v[32:35], 0\n\t"
      "s_waitcnt lgkmcnt(6)\n\t"
      LSTW(24, "")
      "v_mfma_f32_16x16x32_bf16 v[240:243], a[168:171], v[32:35], 0\n\t"
      "s_waitcnt vmcnt(5)\n\t"
      STAMP(8, 9)
      "v_cmp_eq_u64 vcc, s[8:9], v[2:3]\n\t"
      "s_nop 0\n\t"
      "s_cmp_eq_u64 vcc, -1\n\t"
      LSTW(8, "s_waitcnt lgkmcnt(0)\n\t")
      "s_cbranch_scc1 Lpd12_%=\n\t"
      "s_mov_b32 s10, 200000\n\t"
      "Lp12_%=:\n\t"
      "buffer_load_dwordx2 v[2:3], v27, s[96:99], s16 offen offset:2048 sc1\n\t"
      "s_waitcnt vmcnt(0)\n\t"
      "v_cmp_eq_u64 vcc, s[8:9], v[2:3]\n\t"
      "s_nop 0\n\t"
      "s_cmp_eq_u64 vcc, -1\n\t"
      "s_cbranch_scc1 Lpd12_%=\n\t"
      "s_sleep 2\n\t"
      "s_sub_u32 s10, s10, 1\n\t"
      "s_cmp_lg_u32 s10, 0\n\t"
      "s_cbranch_scc1 Lp12_%=\n\t"
      "Lpd12_%=:\n\t"
      STAMP(44, 45)
      "ds_read_b64_tr_b16 a[184:185], v20 offset:0\n\t"
      "v_mfma_f32_16x16x32_bf16 v[244:247], a[168:171], v[36:39], 0\n\t"
      "ds_read_b64_tr_b16 a[186:187], v20 offset:4096\n\t"
      "buffer_load_dwordx4 a[224:227], v0, s[76:79], s34 offen sc1\n\t"
      "v_mfma_f32_16x16x32_bf16 v[248:251], a[168:171], v[40:43], 0\n\t"
      "buffer_load_dwordx4 a[228:231], v0, s[76:79], s34 offen offset:1024 sc1\n\t"
      "v_mfma_f32_16x16x32_bf16 v[252:255], a[168:171], v[44:47], 0\n\t"
      "s_waitcnt lgkmcnt(6)\n\t"
      "buffer_load_dwordx4 a[232:235], v0, s[76:79], s35 offen sc1\n\t"
      "v_mfma_f32_16x16x32_bf16 v[236:239], a[172:175], v[44:47], 0\n\t"
      "ds_read_b64_tr_b16 a[188:189], v21 offset:0\n\t"
      "buffer_load_dwordx4 a[236:239], v0, s[76:79], s35 offen offset:1024 sc1\n\t"
      "v_mfma_f32_16x16x32_bf16 v[232:235], a[172:175], v[40:43], 0\n\t"
      "ds_read_b64_tr_b16 a[190:191], v21 offset:4096\n\t"
      "buffer_load_dwordx4 v1, s[36:39], s21 offen offset:1024 lds\n\t"
      "v_mfma_f32_16x16x32_bf16 v[228:231], a[172:175], v[36:39], 0\n\t"
      "v_add_u32 v0, s51, v0\n\t"
      "v_mfma_f32_16x16x32_bf16 v[224:227], a[172:175], v[32:35], 0\n\t"
      "s_waitcnt lgkmcnt(6)\n\t"
      "v_mfma_f32_16x16x32_bf16 v[144:147], a[176:179], v[32:35], 0\n\t"
      "ds_read_b64_tr_b16 a[160:161], v18 offset:10240\n\t"
      "v_mfma_f32_16x16x32_bf16 v[148:151], a[176:179], v[36:39], 0\n\t"
      "ds_read_b64_tr_b16 a[162:163], v18 offset:14336\n\t"
      LSTW(44, "")
      "buffer_load_dwordx4 a[240:243], v0, s[76:79], s34 offen sc1\n\t"
      "v_mfma_f32_16x16x32_bf16 v[152:155], a[176:179], v[40:43], 0\n\t"
      "v_mfma_f32_16x16x32_bf16 v[156:159], a[176:179], v[44:47], 0\n\t"
      "s_waitcnt lgkmcnt(6)\n\t"
      "buffer_load_dwordx4 a[244:247], v0, s[76:79], s34 offen offset:1024 sc1\n\t"
      "v_mfma_f32_16x16x32_bf16 v[188:191], a[180:183], v[44:47], 0\n\t"
      "ds_read_b64_tr_b16 a[164:165], v19 offset:10240\n\t"
      "v_mfma_f32_16x16x32_bf16 v[184:187], a[180:183], v[40:43], 0\n\t"
      "ds_read_b64_tr_b16 a[166:167], v19 offset:14336\n\t"
      "buffer_load_dwordx4 a[248:251], v0, s[76:79], s35 offen sc1\n\t"
      "v_mfma_f32_16x16x32_bf16 v[180:183], a[180:183], v[36:39], 0\n\t"
      "v_mfma_f32_16x16x32_bf16 v[176:179], a[180:183], v[32:35], 0\n\t"
      "s_waitcnt lgkmcnt(6)\n\t"
      "buffer_load_dwordx4 a[252:255], v0, s[76:79], s35 offen offset:1024 sc1\n\t"
      "v_mfma_f32_16x16x32_bf16 v[112:115], a[184:187], v[32:35], 0\n\t"
      "ds_read_b64_tr_b16 a[168:169], v20 offset:10240\n\t"
      "buffer_load_dwordx4 v1, s[36:39], s22 offen offset:2048 lds\n\t"
      "v_mfma_f32_16x16x32_bf16 v[116:119], a[184:187], v[36:39], 0\n\t"
      "ds_read_b64_tr_b16 a[170:171], v20 offset:14336\n\t"
      "v_readlane_b32 s53, v23, 34\n\t"
      "v_mfma_f32_16x16x32_bf16 v[120:123], a[184:187], v[40:43], 0\n\t"
      "v_mfma_f32_16x16x32_bf16 v[124:127], a[184:187], v[44:47], 0\n\t"
      "s_waitcnt lgkmcnt(6)\n\t"
      "v_add_u32 v0, s53, v16\n\t"
      "v_mfma_f32_16x16x32_bf16 v[220:223], a[188:191], v[44:47], 0\n\t"
      "ds_read_b64_tr_b16 a[172:173], v21 offset:10240\n\t"
      "v_mfma_f32_16x16x32_bf16 v[216:219], a[188:191], v[40:43], 0\n\t"
      "ds_read_b64_tr_b16 a[174:175], v21 offset:14336\n\t"
      "v_mfma_f32_16x16x32_bf16 v[212:215], a[188:191], v[36:39], 0\n\t"
      "v_mfma_f32_16x16x32_bf16 v[208:211], a[188:191], v[32:35], 0\n\t"
      "s_waitcnt lgkmcnt(6)\n\t"
      "buffer_load_dwordx4 a[192:195], v0, s[76:79], s34 offen sc1\n\t"
      "v_mfma_f32_16x16x32_bf16 v[48:51], a[160:163], v[64:67], v[48:51]\n\t"
      "ds_read_b64_tr_b16 a[176:177], v18 offset:8192\n\t"
      "buffer_load_dwordx4 v1, s[36:39], s23 offen offset:3072 lds\n\t"
      "v_mfma_f32_16x16x32_bf16 v[52:55], a[160:163], v[68:71], v[52:55]\n\t"
      "s_add_u32 s24, s24, 16384\n\t"
      "ds_read_b64_tr_b16 a[178:179], v18 offset:12288\n\t"
      "v_readlane_b32 s54, v23, 4\n\t"
      "v_mfma_f32_16x16x32_bf16 v[56:59], a[160:163], v[72:75], v[56:59]\n\t"
      "s_and_b32 s24, s24, 0x1ffff\n\t"
      "v_mfma_f32_16x16x32_bf16 v[60:63], a[160:163], v[76:79], v[60:63]\n\t"
      "s_waitcnt lgkmcnt(6)\n\t"
      "s_bitcmp1_b32 s54, 0\n\t"
      "buffer_load_dwordx4 a[196:199], v0, s[76:79], s34 offen offset:1024 sc1\n\t"
      "v_mfma_f32_16x16x32_bf16 v[92:95], a[164:167], v[76:79], v[92:95]\n\t"
      "s_cselect_b64 s[36:37], s[80:81], s[84:85]\n\t"
      "s_cselect_b32 s38, s82, s86\n\t"
      "ds_read_b64_tr_b16 a[180:181], v19 offset:8192\n\t"
      "v_mfma_f32_16x16x32_bf16 v[88:91], a[164:167], v[72:75], v[88:91]\n\t"
      "s_and_b32 s54, s54, -2\n\t"
      "ds_read_b64_tr_b16 a[182:183], v19 offset:12288\n\t"
      "v_add_u32 v1, s54, v17\n\t"
      "v_mfma_f32_16x16x32_bf16 v[84:87], a[164:167], v[68:71], v[84:87]\n\t"
      "v_mfma_f32_16x16x32_bf16 v[80:83], a[164:167], v[64:67], v[80:83]\n\t"
      "s_waitcnt lgkmcnt(6)\n\t"
      "buffer_load_dwordx4 a[200:203], v0, s[76:79], s35 offen sc1\n\t"
      "v_mfma_f32_16x16x32_bf16 v[240:243], a[168:171], v[64:67], v[240:243]\n\t"
      "ds_read_b64_tr_b16 a[184:185], v20 offset:8192\n\t"
      "v_mfma_f32_16x16x32_bf16 v[244:247], a[168:171], v[68:71], v[244:247]\n\t"
      "ds_read_b64_tr_b16 a[186:187], v20 offset:12288\n\t"
      "v_mfma_f32_16x16x32_bf16 v[248:251], a[168:171], v[72:75], v[248:251]\n\t"
      "v_mfma_f32_16x16x32_bf16 v[252:255], a[168:171], v[76:79], v[252:255]\n\t"
      "s_waitcnt lgkmcnt(6)\n\t"
      "buffer_load_dwordx4 a[204:207], v0, s[76:79], s35 offen offset:1024 sc1\n\t"
      "v_mfma_f32_16x16x32_bf16 v[236:239], a[172:175], v[76:79], v[236:239]\n\t"
      "ds_read_b64_tr_b16 a[188:189], v21 offset:8192\n\t"
      "v_add_u32 v0, s51, v0\n\t"
      "v_mfma_f32_16x16x32_bf16 v[232:235], a[172:175], v[72:75], v[232:235]\n\t"
      "ds_read_b64_tr_b16 a[190:191], v21 offset:12288\n\t"
      "v_mfma_f32_16x16x32_bf16 v[228:231], a[172:175], v[68:71], v[228:231]\n\t"
      "v_mfma_f32_16x16x32_bf16 v[224:227], a[172:175], v[64:67], v[224:227]\n\t"
      "s_waitcnt lgkmcnt(6)\n\t"
      "v_mfma_f32_16x16x32_bf16 v[144:147], a[176:179], v[64:67], v[144:147]\n\t"
      "s_waitcnt vmcnt(16)\n\t"
      "s_waitcnt vmcnt(35)\n\t"
      "s_barrier\n\t"
      "s_lshl_b32 s5, s32, 24\n\t"
      "s_add_u32 s5, s5, s27\n\t"
      "v_mov_b32 v12, s8\n\t"
      "v_mov_b32 v13, s9\n\t"
      "s_mov_b64 exec, 1\n\t"
      "buffer_store_dwordx2 v[12:13], off, s[96:99], s5 sc1\n\t"
      "s_mov_b64 exec, -1\n\t"
      STAMP(22, 23)
      "ds_read_b64_tr_b16 a[160:161], v18 offset:18432\n\t"
      "v_mfma_f32_16x16x32_bf16 v[148:151], a[176:179], v[68:71], v[148:151]\n\t"
      "ds_read_b64_tr_b16 a[162:163], v18 offset:22528\n\t"
      "v_mfma_f32_16x16x32_bf16 v[152:155], a[176:179], v[72:75], v[152:155]\n\t"
      "v_mfma_f32_16x16x32_bf16 v[156:159], a[176:179], v[76:79], v[156:159]\n\t"
      "s_waitcnt lgkmcnt(6)\n\t"
      "v_mfma_f32_16x16x32_bf16 v[188:191], a[180:183], v[76:79], v[188:191]\n\t"
      "ds_read_b64_tr_b16 a[164:165], v19 offset:18432\n\t"
      "v_mfma_f32_16x16x32_bf16 v[184:187], a[180:183], v[72:75], v[184:187]\n\t"
      "ds_read_b64_tr_b16 a[166:167], v19 offset:22528\n\t"
      "v_mfma_f32_16x16x32_bf16 v[180:183], a[180:183], v[68:71], v[180:183]\n\t"
      "v_mfma_f32_16x16x32_bf16 v[176:179], a[180:183], v[64:67], v[176:179]\n\t"
      "s_waitcnt lgkmcnt(6)\n\t"
      "v_mfma_f32_16x16x32_bf16 v[112:115], a[184:187], v[64:67], v[112:115]\n\t"
      "ds_read_b64_tr_b16 a[168:169], v20 offset:18432\n\t"
      "v_mfma_f32_16x16x32_bf16 v[116:119], a[184:187], v[68:71], v[116:119]\n\t"
      "ds_read_b64_tr_b16 a[170:171], v20 offset:22528\n\t"
      "v_mfma_f32_16x16x32_bf16 v[120:123], a[184:187], v[72:75], v[120:123]\n\t"
      "v_mfma_f32_16x16x32_bf16 v[124:127], a[184:187], v[76:79], v[124:127]\n\t"
      "s_waitcnt lgkmcnt(6)\n\t"
      "v_mfma_f32_16x16x32_bf16 v[220:223], a[188:191], v[76:79], v[220:223]\n\t"
      "ds_read_b64_tr_b16 a[172:173], v21 offset:18432\n\t"
      "v_mfma_f32_16x16x32_bf16 v[216:219], a[188:191], v[72:75], v[216:219]\n\t"
      LSTW(22, "")
      "ds_read_b64_tr_b16 a[174:175], v21 offset:22528\n\t"
      "v_mfma_f32_16x16x32_bf16 v[212:215], a[188:191], v[68:71], v[212:215]\n\t"
      "s_waitcnt vmcnt(12)\n\t"
      "v_mfma_f32_16x16x32_bf16 v[208:211], a[188:191], v[64:67], v[208:211]\n\t"
      "s_waitcnt lgkmcnt(6)\n\t"
      "v_mfma_f32_16x16x32_bf16 v[48:51], a[160:163], a[224:227], v[48:51]\n\t"
      "ds_read_b64_tr_b16 a[176:177], v18 offset:16384\n\t"
      "v_mfma_f32_16x16x32_bf16 v[52:55], a[160:163], a[228:231], v[52:55]\n\t"
      "ds_read_b64_tr_b16 a[178:179], v18 offset:20480\n\t"
      "v_mfma_f32_16x16x32_bf16 v[56:59], a[160:163], a[232:235], v[56:59]\n\t"
      "buffer_load_dwordx4 a[208:211], v0, s[76:79], s34 offen sc1\n\t"
      "v_mfma_f32_16x16x32_bf16 v[60:63], a[160:163], a[236:239], v[60:63]\n\t"
      "s_waitcnt lgkmcnt(6)\n\t"
      "s_mov_b32 m0, s24\n\t"
      "v_mfma_f32_16x16x32_bf16 v[92:95], a[164:167], a[236:239], v[92:95]\n\t"
      "ds_read_b64_tr_b16 a[180:181], v19 offset:16384\n\t"
      "buffer_load_dwordx4 v1, s[36:39], s20 offen lds\n\t"
      "v_mfma_f32_16x16x32_bf16 v[88:91], a[164:167], a[232:235], v[88:91]\n\t"
      "ds_read_b64_tr_b16 a[182:183], v19 offset:20480\n\t"
      "v_mfma_f32_16x16x32_bf16 v[84:87], a[164:167], a[228:231], v[84:87]\n\t"
      "v_mfma_f32_16x16x32_bf16 v[80:83], a[164:167], a[224:227], v[80:83]\n\t"
      "s_waitcnt lgkmcnt(6)\n\t"
      "v_and_b32 v40, 1023, v16\n\t"
      "v_mfma_f32_16x16x32_bf16 v[240:243], a[168:171], a[224:227], v[240:243]\n\t"
      "ds_read_b64_tr_b16 a[184:185], v20 offset:16384\n\t"
      "v_lshrrev_b32 v45, 4, v40\n\t"
      "v_mfma_f32_16x16x32_bf16 v[244:247], a[168:171], a[228:231], v[244:247]\n\t"
      "ds_read_b64_tr_b16 a[186:187], v20 offset:20480\n\t"
      "v_and_b32 v44, 3, v45\n\t"
      "v_mfma_f32_16x16x32_bf16 v[248:251], a[168:171], a[232:235], v[248:251]\n\t"
      "buffer_load_dwordx4 a[212:215], v0, s[76:79], s34 offen offset:1024 sc1\n\t"
      "v_or_b32 v38, 2, v44\n\t"
      "v_mfma_f32_16x16x32_bf16 v[252:255], a[168:171], a[236:239], v[252:255]\n\t"
      "s_waitcnt lgkmcnt(6)\n\t"
      "v_lshlrev_b32 v38, 8, v38\n\t"
      "v_mfma_f32_16x16x32_bf16 v[236:239], a[172:175], a[236:239], v[236:239]\n\t"
      "ds_read_b64_tr_b16 a[188:189], v21 offset:16384\n\t"
      "v_xor_b32 v38, s19, v38\n\t"
      "v_mfma_f32_16x16x32_bf16 v[232:235], a[172:175], a[232:235], v[232:235]\n\t"
      "ds_read_b64_tr_b16 a[190:191], v21 offset:20480\n\t"
      "buffer_load_dwordx4 v1, s[36:39], s21 offen offset:1024 lds\n\t"
      "v_mfma_f32_16x16x32_bf16 v[228:231], a[172:175], a[228:231], v[228:231]\n\t"
      "v_add_u32 v38, 64, v38\n\t"
      "v_mfma_f32_16x16x32_bf16 v[224:227], a[172:175], a[224:227], v[224:227]\n\t"
      "s_waitcnt lgkmcnt(6)\n\t"
      "v_mov_b32 v39, 256\n\t"
      "v_mfma_f32_16x16x32_bf16 v[144:147], a[176:179], a[224:227], v[144:147]\n\t"
      "ds_read_b64_tr_b16 a[160:161], v18 offset:26624\n\t"
      "v_xor_b32 v39, s19, v39\n\t"
      "v_mfma_f32_16x16x32_bf16 v[148:151], a[176:179], a[228:231], v[148:151]\n\t"
      "ds_read_b64_tr_b16 a[162:163], v18 offset:30720\n\t"
      "v_and_b32 v47, 15, v45\n\t"
      "v_mfma_f32_16x16x32_bf16 v[152:155], a[176:179], a[232:235], v[152:155]\n\t"
      "buffer_load_dwordx4 a[216:219], v0, s[76:79], s35 offen sc1\n\t"
      "v_lshl_add_u32 v26, v47, 7, s55\n\t"
      "v_mfma_f32_16x16x32_bf16 v[156:159], a[176:179], a[236:239], v[156:159]\n\t"
      "s_waitcnt lgkmcnt(6)\n\t"
      "v_lshrrev_b32 v46, 4, v45\n\t"
      "v_mfma_f32_16x16x32_bf16 v[188:191], a[180:183], a[236:239], v[188:191]\n\t"
      "ds_read_b64_tr_b16 a[164:165], v19 offset:26624\n\t"
      "v_bfe_u32 v47, v45, 2, 2\n\t"
      "v_mfma_f32_16x16x32_bf16 v[184:187], a[180:183], a[232:235], v[184:187]\n\t"
      "ds_read_b64_tr_b16 a[166:167], v19 offset:30720\n\t"
      "v_cmp_eq_u32 vcc, v46, v47\n\t"
      "v_mfma_f32_16x16x32_bf16 v[180:183], a[180:183], a[228:231], v[180:183]\n\t"
      "v_and_b32 v46, 1, v44\n\t"
      "v_mfma_f32_16x16x32_bf16 v[176:179], a[180:183], a[224:227], v[176:179]\n\t"
      "s_waitcnt lgkmcnt(6)\n\t"
      "buffer_load_dwordx4 v1, s[36:39], s22 offen offset:2048 lds\n\t"
      "v_lshlrev_b32 v46, 4, v46\n\t"
      "v_mfma_f32_16x16x32_bf16 v[112:115], a[184:187], a[224:227], v[112:115]\n\t"
      "ds_read_b64_tr_b16 a[168:169], v20 offset:26624\n\t"
      "v_mov_b32 v47, 0x3f80\n\t"
      "v_mfma_f32_16x16x32_bf16 v[116:119], a[184:187], a[228:231], v[116:119]\n\t"
      "ds_read_b64_tr_b16 a[170:171], v20 offset:30720\n\t"
      "v_lshlrev_b32 v46, v46, v47\n\t"
      "v_mfma_f32_16x16x32_bf16 v[120:123], a[184:187], a[232:235], v[120:123]\n\t"
      "s_add_u32 s64, s64, 32768\n\t"
      "s_and_b32 s64, s64, 0x1ffff\n\t"
      "buffer_load_dwordx4 a[220:223], v0, s[76:79], s35 offen offset:1024 sc1\n\t"
      "v_mfma_f32_16x16x32_bf16 v[124:127], a[184:187], a[236:239], v[124:127]\n\t"
      "s_waitcnt lgkmcnt(6)\n\t"
      "v_readlane_b32 s53, v23, 35\n\t"
      "v_cndmask_b32 v46, 0, v46, vcc\n\t"
      "v_mfma_f32_16x16x32_bf16 v[220:223], a[188:191], a[236:239], v[220:223]\n\t"
      "ds_read_b64_tr_b16 a[172:173], v21 offset:26624\n\t"
      "v_cmp_gt_u32 vcc, 2, v44\n\t"
      "v_mfma_f32_16x16x32_bf16 v[216:219], a[188:191], a[232:235], v[216:219]\n\t"
      "ds_read_b64_tr_b16 a[174:175], v21 offset:30720\n\t"
      "v_add_u32 v0, s53, v16\n\t"
      "v_mfma_f32_16x16x32_bf16 v[212:215], a[188:191], a[228:231], v[212:215]\n\t"
      "s_waitcnt vmcnt(14)\n\t"
      "v_mov_b64 v[32:33], 0\n\t"
      "v_mfma_f32_16x16x32_bf16 v[208:211], a[188:191], a[224:227], v[208:211]\n\t"
      "s_waitcnt lgkmcnt(6)\n\t"
      "v_mov_b64 v[36:37], 0\n\t"
      "v_mfma_f32_16x16x32_bf16 v[48:51], a[160:163], a[240:243], v[48:51]\n\t"
      "ds_read_b64_tr_b16 a[176:177], v18 offset:24576\n\t"
      "buffer_load_dwordx4 v1, s[36:39], s23 offen offset:3072 lds\n\t"
      "v_mfma_f32_16x16x32_bf16 v[52:55], a[160:163], a[244:247], v[52:55]\n\t"
      "s_add_u32 s24, s24, 16384\n\t"
      "ds_read_b64_tr_b16 a[178:179], v18 offset:28672\n\t"
      "v_readlane_b32 s54, v23, 5\n\t"
      "v_mfma_f32_16x16x32_bf16 v[56:59], a[160:163], a[248:251], v[56:59]\n\t"
      "s_and_b32 s24, s24, 0x1ffff\n\t"
      "buffer_load_dwordx4 a[224:227], v0, s[76:79], s34 offen sc1\n\t"
      "v_add_u32 v18, s64, v4\n\t"
      "v_mfma_f32_16x16x32_bf16 v[60:63], a[160:163], a[252:255], v[60:63]\n\t"
      "s_waitcnt lgkmcnt(6)\n\t"
      "s_bitcmp1_b32 s54, 0\n\t"
      "s_cselect_b64 s[36:37], s[80:81], s[84:85]\n\t"
      "v_mfma_f32_16x16x32_bf16 v[92:95], a[164:167], a[252:255], v[92:95]\n\t"
      "s_cselect_b32 s38, s82, s86\n\t"
      "s_and_b32 s54, s54, -2\n\t"
      "ds_read_b64_tr_b16 a[180:181], v19 offset:24576\n\t"
      "v_mfma_f32_16x16x32_bf16 v[88:91], a[164:167], a[248:251], v[88:91]\n\t"
      "ds_read_b64_tr_b16 a[182:183], v19 offset:28672\n\t"
      "v_add_u32 v1, s54, v17\n\t"
      "v_mfma_f32_16x16x32_bf16 v[84:87], a[164:167], a[244:247], v[84:87]\n\t"
      "v_add_u32 v19, s64, v5\n\t"
      "v_cndmask_b32 v34, 0, v46, vcc\n\t"
      "v_mfma_f32_16x16x32_bf16 v[80:83], a[164:167], a[240:243], v[80:83]\n\t"
      "s_waitcnt lgkmcnt(6)\n\t"
      "v_sub_u32 v35, v46, v34\n\t"
      "v_mfma_f32_16x16x32_bf16 v[240:243], a[168:171], a[240:243], v[240:243]\n\t"
      "ds_read_b64_tr_b16 a[184:185], v20 offset:24576\n\t"
      "v_and_b32 v46, 1, v45\n\t"
      "v_mfma_f32_16x16x32_bf16 v[244:247], a[168:171], a[244:247], v[244:247]\n\t"
      "ds_read_b64_tr_b16 a[186:187], v20 offset:28672\n\t"
      "v_lshlrev_b32 v46, 4, v46\n\t"
      "v_mfma_f32_16x16x32_bf16 v[248:251], a[168:171], a[248:251], v[248:251]\n\t"
      "buffer_load_dwordx4 a[228:231], v0, s[76:79], s34 offen offset:1024 sc1\n\t"
      "v_add_u32 v20, s64, v6\n\t"
      "v_mfma_f32_16x16x32_bf16 v[252:255], a[168:171], a[252:255], v[252:255]\n\t"
      "s_waitcnt lgkmcnt(6)\n\t"
      "v_mov_b32 v47, 0x3f80\n\t"
      "v_mfma_f32_16x16x32_bf16 v[236:239], a[172:175], a[252:255], v[236:239]\n\t"
      "ds_read_b64_tr_b16 a[188:189], v21 offset:24576\n\t"
      "v_lshlrev_b32 v46, v46, v47\n\t"
      "v_mfma_f32_16x16x32_bf16 v[232:235], a[172:175], a[248:251], v[232:235]\n\t"
      "ds_read_b64_tr_b16 a[190:191], v21 offset:28672\n\t"
      "v_lshrrev_b32 v47, 5, v45\n\t"
      "v_mfma_f32_16x16x32_bf16 v[228:231], a[172:175], a[244:247], v[228:231]\n\t"
      "v_add_u32 v21, s64, v7\n\t"
      "v_bfe_u32 v44, v45, 3, 1\n\t"
      "v_mfma_f32_16x16x32_bf16 v[224:227], a[172:175], a[240:243], v[224:227]\n\t"
      "s_waitcnt lgkmcnt(6)\n\t"
      "v_cmp_eq_u32 vcc, v47, v44\n\t"
      "v_mfma_f32_16x16x32_bf16 v[144:147], a[176:179], a[240:243], v[144:147]\n\t"
      "s_waitcnt vmcnt(32)\n\t"
      "s_barrier\n\t"
      "ds_read_b64_tr_b16 a[160:161], v18 offset:2048\n\t"
      "v_mfma_f32_16x16x32_bf16 v[148:151], a[176:179], a[244:247], v[148:151]\n\t"
      "ds_read_b64_tr_b16 a[162:163], v18 offset:6144\n\t"
      "v_bfe_u32 v47, v45, 4, 1\n\t"
      "v_mfma_f32_16x16x32_bf16 v[152:155], a[176:179], a[248:251], v[152:155]\n\t"
      "buffer_load_dwordx4 a[232:235], v0, s[76:79], s35 offen sc1\n\t"
      "v_bfe_u32 v44, v45, 1, 2\n\t"
      "v_mfma_f32_16x16x32_bf16 v[156:159], a[176:179], a[252:255], v[156:159]\n\t"
      "s_waitcnt lgkmcnt(6)\n\t"
      "v_cndmask_b32 v46, 0, v46, vcc\n\t"
      "v_mfma_f32_16x16x32_bf16 v[188:191], a[180:183], a[252:255], v[188:191]\n\t"
      "ds_read_b64_tr_b16 a[164:165], v19 offset:2048\n\t"
      "v_lshl_or_b32 v44, v47, 2, v44\n\t"
      "v_mfma_f32_16x16x32_bf16 v[184:187], a[180:183], a[248:251], v[184:187]\n\t"
      "ds_read_b64_tr_b16 a[166:167], v19 offset:6144\n\t"
      "v_cmp_eq_u32 vcc, 0, v44\n\t"
      "v_mfma_f32_16x16x32_bf16 v[180:183], a[180:183], a[244:247], v[180:183]\n\t"
      "s_nop 0\n\t"
      "v_cndmask_b32 v64, 0, v46, vcc\n\t"
      "v_mfma_f32_16x16x32_bf16 v[176:179], a[180:183], a[240:243], v[176:179]\n\t"
      "s_waitcnt lgkmcnt(6)\n\t"
      "v_cmp_eq_u32 vcc, 1, v44\n\t"
      "v_mfma_f32_16x16x32_bf16 v[112:115], a[184:187], a[240:243], v[112:115]\n\t"
      "ds_read_b64_tr_b16 a[168:169], v20 offset:2048\n\t"
      "v_cndmask_b32 v65, 0, v46, vcc\n\t"
      "v_mfma_f32_16x16x32_bf16 v[116:119], a[184:187], a[244:247], v[116:119]\n\t"
      "ds_read_b64_tr_b16 a[170:171], v20 offset:6144\n\t"
      "v_cmp_eq_u32 vcc, 2, v44\n\t"
      "v_mfma_f32_16x16x32_bf16 v[120:123], a[184:187], a[248:251], v[120:123]\n\t"
      "buffer_load_dwordx4 a[236:239], v0, s[76:79], s35 offen offset:1024 sc1\n\t"
      "v_cndmask_b32 v66, 0, v46, vcc\n\t"
      "v_mfma_f32_16x16x32_bf16 v[124:127], a[184:187], a[252:255], v[124:127]\n\t"
      "s_waitcnt lgkmcnt(6)\n\t"
      "v_add_u32 v0, s51, v0\n\t"
      "v_cmp_eq_u32 vcc, 3, v44\n\t"
      "v_mfma_f32_16x16x32_bf16 v[220:223], a[188:191], a[252:255], v[220:223]\n\t"
      "ds_read_b64_tr_b16 a[172:173], v21 offset:2048\n\t"
      "v_cndmask_b32 v67, 0, v46, vcc\n\t"
      "v_mfma_f32_16x16x32_bf16 v[216:219], a[188:191], a[248:251], v[216:219]\n\t"
      "ds_read_b64_tr_b16 a[174:175], v21 offset:6144\n\t"
      "v_cmp_eq_u32 vcc, 4, v44\n\t"
      "v_mfma_f32_16x16x32_bf16 v[212:215], a[188:191], a[244:247], v[212:215]\n\t"
      "s_waitcnt vmcnt(13)\n\t"
      "v_cndmask_b32 v68, 0, v46, vcc\n\t"
      "v_mfma_f32_16x16x32_bf16 v[208:211], a[188:191], a[240:243], v[208:211]\n\t"
      "s_waitcnt lgkmcnt(6)\n\t"
      "v_cmp_eq_u32 vcc, 5, v44\n\t"
      "v_mfma_f32_16x16x32_bf16 v[48:51], a[160:163], a[192:195], v[48:51]\n\t"
      "ds_read_b64_tr_b16 a[176:177], v18 offset:0\n\t"
      "v_cndmask_b32 v69, 0, v46, vcc\n\t"
      "v_mfma_f32_16x16x32_bf16 v[52:55], a[160:163], a[196:199], v[52:55]\n\t"
      "ds_read_b64_tr_b16 a[178:179], v18 offset:4096\n\t"
      "v_cmp_eq_u32 vcc, 6, v44\n\t"
      "v_mfma_f32_16x16x32_bf16 v[56:59], a[160:163], a[200:203], v[56:59]\n\t"
      "buffer_load_dwordx4 a[240:243], v0, s[76:79], s34 offen sc1\n\t"
      "v_cndmask_b32 v70, 0, v46, vcc\n\t"
      "v_mfma_f32_16x16x32_bf16 v[60:63], a[160:163], a[204:207], v[60:63]\n\t"
      "s_waitcnt lgkmcnt(6)\n\t"
      "s_mov_b32 m0, s24\n\t"
      "v_cmp_eq_u32 vcc, 7, v44\n\t"
      "v_mfma_f32_16x16x32_bf16 v[92:95], a[164:167], a[204:207], v[92:95]\n\t"
      "ds_read_b64_tr_b16 a[180:181], v19 offset:0\n\t"
      "buffer_load_dwordx4 v1, s[36:39], s20 offen lds\n\t"
      "v_mfma_f32_16x16x32_bf16 v[88:91], a[164:167], a[200:203], v[88:91]\n\t"
      "ds_read_b64_tr_b16 a[182:183], v19 offset:4096\n\t"
      "v_cndmask_b32 v71, 0, v46, vcc\n\t"
      "v_mfma_f32_16x16x32_bf16 v[84:87], a[164:167], a[196:199], v[84:87]\n\t"
      "s_lshl_b32 s3, s32, 12\n\t"
      "v_mfma_f32_16x16x32_bf16 v[80:83], a[164:167], a[192:195], v[80:83]\n\t"
      "s_waitcnt lgkmcnt(6)\n\t"
      "s_add_u32 s3, s3, 147456\n\t"
      "v_mfma_f32_16x16x32_bf16 v[240:243], a[168:171], a[192:195], v[240:243]\n\t"
      "ds_read_b64_tr_b16 a[184:185], v20 offset:0\n\t"
      "v_add_u32 v78, s3, v40\n\t"
      "v_mfma_f32_16x16x32_bf16 v[244:247], a[168:171], a[196:199], v[244:247]\n\t"
      "ds_read_b64_tr_b16 a[186:187], v20 offset:4096\n\t"
      "v_and_b32 v46, 15, v45\n\t"
      "v_mfma_f32_16x16x32_bf16 v[248:251], a[168:171], a[200:203], v[248:251]\n\t"
      "buffer_load_dwordx4 a[244:247], v0, s[76:79], s34 offen offset:1024 sc1\n\t"
      "v_lshl_add_u32 v46, v46, 7, s3\n\t"
      "v_mfma_f32_16x16x32_bf16 v[252:255], a[168:171], a[204:207], v[252:255]\n\t"
      "s_waitcnt lgkmcnt(6)\n\t"
      "v_and_b32 v47, 6, v45\n\t"
      "v_mfma_f32_16x16x32_bf16 v[236:239], a[172:175], a[204:207], v[236:239]\n\t"
      "ds_read_b64_tr_b16 a[188:189], v21 offset:0\n\t"
      "v_lshl_or_b32 v46, v47, 4, v46\n\t"
      "v_mfma_f32_16x16x32_bf16 v[232:235], a[172:175], a[200:203], v[232:235]\n\t"
      "ds_read_b64_tr_b16 a[190:191], v21 offset:4096\n\t"
      "buffer_load_dwordx4 v1, s[36:39], s21 offen offset:1024 lds\n\t"
      "v_mfma_f32_16x16x32_bf16 v[228:231], a[172:175], a[196:199], v[228:231]\n\t"
      "v_bfe_u32 v47, v45, 5, 1\n\t"
      "v_mfma_f32_16x16x32_bf16 v[224:227], a[172:175], a[192:195], v[224:227]\n\t"
      "s_waitcnt lgkmcnt(6)\n\t"
      "v_lshlrev_b32 v47, 4, v47\n\t"
      "v_mfma_f32_16x16x32_bf16 v[144:147], a[176:179], a[192:195], v[144:147]\n\t"
      "ds_read_b64_tr_b16 a[160:161], v18 offset:10240\n\t"
      "v_bfe_u32 v44, v45, 4, 1\n\t"
      "v_mfma_f32_16x16x32_bf16 v[148:151], a[176:179], a[196:199], v[148:151]\n\t"
      "ds_read_b64_tr_b16 a[162:163], v18 offset:14336\n\t"
      "v_lshl_or_b32 v72, v44, 5, v47\n\t"
      "v_mfma_f32_16x16x32_bf16 v[152:155], a[176:179], a[200:203], v[152:155]\n\t"
      "buffer_load_dwordx4 a[248:251], v0, s[76:79], s35 offen sc1\n\t"
      "v_xor_b32 v72, v72, v46\n\t"
      "v_mfma_f32_16x16x32_bf16 v[156:159], a[176:179], a[204:207], v[156:159]\n\t"
      "s_waitcnt lgkmcnt(6)\n\t"
      "v_xor_b32 v73, 64, v72\n\t"
      "v_mfma_f32_16x16x32_bf16 v[188:191], a[180:183], a[204:207], v[188:191]\n\t"
      "ds_read_b64_tr_b16 a[164:165], v19 offset:10240\n\t"
      "v_lshl_or_b32 v74, v44, 3, v47\n\t"
      "v_mfma_f32_16x16x32_bf16 v[184:187], a[180:183], a[200:203], v[184:187]\n\t"
      "ds_read_b64_tr_b16 a[166:167], v19 offset:14336\n\t"
      "v_xor_b32 v74, v74, v46\n\t"
      "v_mfma_f32_16x16x32_bf16 v[180:183], a[180:183], a[196:199], v[180:183]\n\t"
      "v_xor_b32 v75, 32, v74\n\t"
      "v_mfma_f32_16x16x32_bf16 v[176:179], a[180:183], a[192:195], v[176:179]\n\t"
      "s_waitcnt lgkmcnt(6)\n\t"
      "buffer_load_dwordx4 v1, s[36:39], s22 offen offset:2048 lds\n\t"
      "v_xor_b32 v76, 64, v74\n\t"
      "v_mfma_f32_16x16x32_bf16 v[112:115], a[184:187], a[192:195], v[112:115]\n\t"
      "ds_read_b64_tr_b16 a[168:169], v20 offset:10240\n\t"
      "v_xor_b32 v77, 0x60, v74\n\t"
      "v_mfma_f32_16x16x32_bf16 v[116:119], a[184:187], a[196:199], v[116:119]\n\t"
      "ds_read_b64_tr_b16 a[170:171], v20 offset:14336\n\t"
      "v_mfma_f32_16x16x32_bf16 v[120:123], a[184:187], a[200:203], v[120:123]\n\t"
      "buffer_load_dwordx4 a[252:255], v0, s[76:79], s35 offen offset:1024 sc1\n\t"
      "v_mfma_f32_16x16x32_bf16 v[124:127], a[184:187], a[204:207], v[124:127]\n\t"
      "s_waitcnt lgkmcnt(6)\n\t"
      "v_readlane_b32 s53, v23, 36\n\t"
      "v_mfma_f32_16x16x32_bf16 v[220:223], a[188:191], a[204:207], v[220:223]\n\t"
      "ds_read_b64_tr_b16 a[172:173], v21 offset:10240\n\t"
      "v_mfma_f32_16x16x32_bf16 v[216:219], a[188:191], a[200:203], v[216:219]\n\t"
      "ds_read_b64_tr_b16 a[174:175], v21 offset:14336\n\t"
      "v_add_u32 v0, s53, v16\n\t"
      "v_mfma_f32_16x16x32_bf16 v[212:215], a[188:191], a[196:199], v[212:215]\n\t"
      "s_waitcnt vmcnt(12)\n\t"
      "v_mfma_f32_16x16x32_bf16 v[208:211], a[188:191], a[192:195], v[208:211]\n\t"
      "s_waitcnt lgkmcnt(6)\n\t"
      "v_mfma_f32_16x16x32_bf16 v[48:51], a[160:163], a[208:211], v[48:51]\n\t"
      "ds_read_b64_tr_b16 a[176:177], v18 offset:8192\n\t"
      "buffer_load_dwordx4 v1, s[36:39], s23 offen offset:3072 lds\n\t"
      "v_mfma_f32_16x16x32_bf16 v[52:55], a[160:163], a[212:215], v[52:55]\n\t"
      "s_add_u32 s24, s24, 16384\n\t"
      "ds_read_b64_tr_b16 a[178:179], v18 offset:12288\n\t"
      "v_readlane_b32 s54, v23, 6\n\t"
      "v_mfma_f32_16x16x32_bf16 v[56:59], a[160:163], a[216:219], v[56:59]\n\t"
      "s_and_b32 s24, s24, 0x1ffff\n\t"
      "buffer_load_dwordx4 a[192:195], v0, s[76:79], s34 offen sc1\n\t"
      "buffer_load_dwordx2 v[2:3], v27, s[96:99], 0 offen sc1\n\t"
      "v_mfma_f32_16x16x32_bf16 v[60:63], a[160:163], a[220:223], v[60:63]\n\t"
      "s_waitcnt lgkmcnt(6)\n\t"
      "s_bitcmp1_b32 s54, 0\n\t"
      "s_cselect_b64 s[36:37], s[80:81], s[84:85]\n\t"
      "v_mfma_f32_16x16x32_bf16 v[92:95], a[164:167], a[220:223], v[92:95]\n\t"
      "s_cselect_b32 s38, s82, s86\n\t"
      "s_and_b32 s54, s54, -2\n\t"
      "ds_read_b64_tr_b16 a[180:181], v19 offset:8192\n\t"
      "v_mfma_f32_16x16x32_bf16 v[88:91], a[164:167], a[216:219], v[88:91]\n\t"
      "ds_read_b64_tr_b16 a[182:183], v19 offset:12288\n\t"
      "v_add_u32 v1, s54, v17\n\t"
      "v_mfma_f32_16x16x32_bf16 v[84:87], a[164:167], a[212:215], v[84:87]\n\t"
      "s_getpc_b64 s[40:41]\n\t"
      "Lpc20_%=:\n\t"
      "s_add_u32 s40, s40, Llast_%=-Lpc20_%=\n\t"
      "s_addc_u32 s41, s41, 0\n\t"
      "v_mfma_f32_16x16x32_bf16 v[80:83], a[164:167], a[208:211], v[80:83]\n\t"
      "s_waitcnt lgkmcnt(6)\n\t"
      "s_and_b32 s41, s41, 0xffff\n\t"
      "v_mfma_f32_16x16x32_bf16 v[240:243], a[168:171], a[208:211], v[240:243]\n\t"
      "ds_read_b64_tr_b16 a[184:185], v20 offset:8192\n\t"
      "s_mov_b32 s42, Lend_%=-Llast_%=\n\t"
      "v_mfma_f32_16x16x32_bf16 v[244:247], a[168:171], a[212:215], v[244:247]\n\t"
      "ds_read_b64_tr_b16 a[186:187], v20 offset:12288\n\t"
      "s_lshr_b32 s3, s2, 3\n\t"
      "v_mfma_f32_16x16x32_bf16 v[248:251], a[168:171], a[216:219], v[248:251]\n\t"
      "buffer_load_dwordx4 a[196:199], v0, s[76:79], s34 offen offset:1024 sc1\n\t"
      "s_lshl2_add_u32 s3, s3, s32\n\t"
      "v_mfma_f32_16x16x32_bf16 v[252:255], a[168:171], a[220:223], v[252:255]\n\t"
      "s_waitcnt lgkmcnt(6)\n\t"
      "s_lshl_b32 s3, s3, 7\n\t"
      "v_mfma_f32_16x16x32_bf16 v[236:239], a[172:175], a[220:223], v[236:239]\n\t"
      "ds_read_b64_tr_b16 a[188:189], v21 offset:8192\n\t"
      "v_lshlrev_b32 v41, 10, v40\n\t"
      "v_mfma_f32_16x16x32_bf16 v[232:235], a[172:175], a[216:219], v[232:235]\n\t"
      "ds_read_b64_tr_b16 a[190:191], v21 offset:12288\n\t"
      "v_add_u32 v41, s3, v41\n\t"
      "v_mfma_f32_16x16x32_bf16 v[228:231], a[172:175], a[212:215], v[228:231]\n\t"
      "v_mfma_f32_16x16x32_bf16 v[224:227], a[172:175], a[208:211], v[224:227]\n\t"
      "s_waitcnt lgkmcnt(6)\n\t"
      "v_mfma_f32_16x16x32_bf16 v[144:147], a[176:179], a[208:211], v[144:147]\n\t"
      "s_waitcnt vmcnt(27)\n\t"
      "s_barrier\n\t"
      "ds_read_b64_tr_b16 a[160:161], v18 offset:18432\n\t"
      "v_mfma_f32_16x16x32_bf16 v[148:151], a[176:179], a[212:215], v[148:151]\n\t"
      "ds_read_b64_tr_b16 a[162:163], v18 offset:22528\n\t"
      "v_mfma_f32_16x16x32_bf16 v[152:155], a[176:179], a[216:219], v[152:155]\n\t"
      "buffer_load_dwordx4 a[200:203], v0, s[76:79], s35 offen sc1\n\t"
      "v_mfma_f32_16x16x32_bf16 v[156:159], a[176:179], a[220:223], v[156:159]\n\t"
      "s_waitcnt lgkmcnt(6)\n\t"
      "v_mfma_f32_16x16x32_bf16 v[188:191], a[180:183], a[220:223], v[188:191]\n\t"
      "ds_read_b64_tr_b16 a[164:165], v19 offset:18432\n\t"
      "v_mfma_f32_16x16x32_bf16 v[184:187], a[180:183], a[216:219], v[184:187]\n\t"
      "ds_read_b64_tr_b16 a[166:167], v19 offset:22528\n\t"
      "v_mfma_f32_16x16x32_bf16 v[180:183], a[180:183], a[212:215], v[180:183]\n\t"
      "v_mfma_f32_16x16x32_bf16 v[176:179], a[180:183], a[208:211], v[176:179]\n\t"
      "s_waitcnt lgkmcnt(6)\n\t"
      "v_mfma_f32_16x16x32_bf16 v[112:115], a[184:187], a[208:211], v[112:115]\n\t"
      "ds_read_b64_tr_b16 a[168:169], v20 offset:18432\n\t"
      "v_mfma_f32_16x16x32_bf16 v[116:119], a[184:187], a[212:215], v[116:119]\n\t"
      "ds_read_b64_tr_b16 a[170:171], v20 offset:22528\n\t"
      "v_mfma_f32_16x16x32_bf16 v[120:123], a[184:187], a[216:219], v[120:123]\n\t"
      "buffer_load_dwordx4 a[204:207], v0, s[76:79], s35 offen offset:1024 sc1\n\t"
      "v_mfma_f32_16x16x32_bf16 v[124:127], a[184:187], a[220:223], v[124:127]\n\t"
      "s_waitcnt lgkmcnt(6)\n\t"
      "v_add_u32 v0, s51, v0\n\t"
      "v_mfma_f32_16x16x32_bf16 v[220:223], a[188:191], a[220:223], v[220:223]\n\t"
      "ds_read_b64_tr_b16 a[172:173], v21 offset:18432\n\t"
      "v_mfma_f32_16x16x32_bf16 v[216:219], a[188:191], a[216:219], v[216:219]\n\t"
      "ds_read_b64_tr_b16 a[174:175], v21 offset:22528\n\t"
      "v_mfma_f32_16x16x32_bf16 v[212:215], a[188:191], a[212:215], v[212:215]\n\t"
      "s_waitcnt vmcnt(13)\n\t"
      "v_mfma_f32_16x16x32_bf16 v[208:211], a[188:191], a[208:211], v[208:211]\n\t"
      "s_waitcnt lgkmcnt(6)\n\t"
      "v_mfma_f32_16x16x32_bf16 v[48:51], a[160:163], a[224:227], v[48:51]\n\t"
      "ds_read_b64_tr_b16 a[176:177], v18 offset:16384\n\t"
      "v_mfma_f32_16x16x32_bf16 v[52:55], a[160:163], a[228:231], v[52:55]\n\t"
      "ds_read_b64_tr_b16 a[178:179], v18 offset:20480\n\t"
      "v_mfma_f32_16x16x32_bf16 v[56:59], a[160:163], a[232:235], v[56:59]\n\t"
      "buffer_load_dwordx4 a[208:211], v0, s[76:79], s34 offen sc1\n\t"
      "v_mfma_f32_16x16x32_bf16 v[60:63], a[160:163], a[236:239], v[60:63]\n\t"
      "s_waitcnt lgkmcnt(6)\n\t"
      "s_mov_b32 m0, s24\n\t"
      "v_mfma_f32_16x16x32_bf16 v[92:95], a[164:167], a[236:239], v[92:95]\n\t"
      "ds_read_b64_tr_b16 a[180:181], v19 offset:16384\n\t"
      "buffer_load_dwordx4 v1, s[36:39], s20 offen lds\n\t"
      "v_mfma_f32_16x16x32_bf16 v[88:91], a[164:167], a[232:235], v[88:91]\n\t"
      "ds_read_b64_tr_b16 a[182:183], v19 offset:20480\n\t"
      "v_mfma_f32_16x16x32_bf16 v[84:87], a[164:167], a[228:231], v[84:87]\n\t"
      "v_mfma_f32_16x16x32_bf16 v[80:83], a[164:167], a[224:227], v[80:83]\n\t"
      "s_waitcnt lgkmcnt(6)\n\t"
      "v_mfma_f32_16x16x32_bf16 v[240:243], a[168:171], a[224:227], v[240:243]\n\t"
      "ds_read_b64_tr_b16 a[184:185], v20 offset:16384\n\t"
      "v_mfma_f32_16x16x32_bf16 v[244:247], a[168:171], a[228:231], v[244:247]\n\t"
      "ds_read_b64_tr_b16 a[186:187], v20 offset:20480\n\t"
      "v_mfma_f32_16x16x32_bf16 v[248:251], a[168:171], a[232:235], v[248:251]\n\t"
      "buffer_load_dwordx4 a[212:215], v0, s[76:79], s34 offen offset:1024 sc1\n\t"
      "v_mfma_f32_16x16x32_bf16 v[252:255], a[168:171], a[236:239], v[252:255]\n\t"
      "s_waitcnt lgkmcnt(6)\n\t"
      "v_mfma_f32_16x16x32_bf16 v[236:239], a[172:175], a[236:239], v[236:239]\n\t"
      "ds_read_b64_tr_b16 a[188:189], v21 offset:16384\n\t"
      "v_mfma_f32_16x16x32_bf16 v[232:235], a[172:175], a[232:235], v[232:235]\n\t"
      "ds_read_b64_tr_b16 a[190:191], v21 offset:20480\n\t"
      "buffer_load_dwordx4 v1, s[36:39], s21 offen offset:1024 lds\n\t"
      "v_mfma_f32_16x16x32_bf16 v[228:231], a[172:175], a[228:231], v[228:231]\n\t"
      "v_mfma_f32_16x16x32_bf16 v[224:227], a[172:175], a[224:227], v[224:227]\n\t"
      "s_waitcnt lgkmcnt(6)\n\t"
      "v_mfma_f32_16x16x32_bf16 v[144:147], a[176:179], a[224:227], v[144:147]\n\t"
      "ds_read_b64_tr_b16 a[160:161], v18 offset:26624\n\t"
      "v_mfma_f32_16x16x32_bf16 v[148:151], a[176:179], a[228:231], v[148:151]\n\t"
      "ds_read_b64_tr_b16 a[162:163], v18 offset:30720\n\t"
      "v_mfma_f32_16x16x32_bf16 v[152:155], a[176:179], a[232:235], v[152:155]\n\t"
      "buffer_load_dwordx4 a[216:219], v0, s[76:79], s35 offen sc1\n\t"
      "v_mfma_f32_16x16x32_bf16 v[156:159], a[176:179], a[236:239], v[156:159]\n\t"
      "s_waitcnt lgkmcnt(6)\n\t"
      "v_mfma_f32_16x16x32_bf16 v[188:191], a[180:183], a[236:239], v[188:191]\n\t"
      "ds_read_b64_tr_b16 a[164:165], v19 offset:26624\n\t"
      "v_mfma_f32_16x16x32_bf16 v[184:187], a[180:183], a[232:235], v[184:187]\n\t"
      "ds_read_b64_tr_b16 a[166:167], v19 offset:30720\n\t"
      "v_mfma_f32_16x16x32_bf16 v[180:183], a[180:183], a[228:231], v[180:183]\n\t"
      "v_mfma_f32_16x16x32_bf16 v[176:179], a[180:183], a[224:227], v[176:179]\n\t"
      "s_waitcnt lgkmcnt(6)\n\t"
      "buffer_load_dwordx4 v1, s[36:39], s22 offen offset:2048 lds\n\t"
      "v_mfma_f32_16x16x32_bf16 v[112:115], a[184:187], a[224:227], v[112:115]\n\t"
      "ds_read_b64_tr_b16 a[168:169], v20 offset:26624\n\t"
      "v_mfma_f32_16x16x32_bf16 v[116:119], a[184:187], a[228:231], v[116:119]\n\t"
      "ds_read_b64_tr_b16 a[170:171], v20 offset:30720\n\t"
      "v_mfma_f32_16x16x32_bf16 v[120:123], a[184:187], a[232:235], v[120:123]\n\t"
      "s_add_u32 s64, s64, 32768\n\t"
      "s_and_b32 s64, s64, 0x1ffff\n\t"
      "buffer_load_dwordx4 a[220:223], v0, s[76:79], s35 offen offset:1024 sc1\n\t"
      "v_mfma_f32_16x16x32_bf16 v[124:127], a[184:187], a[236:239], v[124:127]\n\t"
      "s_waitcnt lgkmcnt(6)\n\t"
      "v_readlane_b32 s53, v23, 37\n\t"
      "v_mfma_f32_16x16x32_bf16 v[220:223], a[188:191], a[236:239], v[220:223]\n\t"
      "ds_read_b64_tr_b16 a[172:173], v21 offset:26624\n\t"
      "v_mfma_f32_16x16x32_bf16 v[216:219], a[188:191], a[232:235], v[216:219]\n\t"
      "ds_read_b64_tr_b16 a[174:175], v21 offset:30720\n\t"
      "v_add_u32 v0, s53, v16\n\t"
      "v_mfma_f32_16x16x32_bf16 v[212:215], a[188:191], a[228:231], v[212:215]\n\t"
      "s_waitcnt vmcnt(13)\n\t"
      "v_mfma_f32_16x16x32_bf16 v[208:211], a[188:191], a[224:227], v[208:211]\n\t"
      "s_waitcnt lgkmcnt(6)\n\t"
      "v_mfma_f32_16x16x32_bf16 v[48:51], a[160:163], a[240:243], v[48:51]\n\t"
      "ds_read_b64_tr_b16 a[176:177], v18 offset:24576\n\t"
      "buffer_load_dwordx4 v1, s[36:39], s23 offen offset:3072 lds\n\t"
      "s_waitcnt vmcnt(11)\n\t"
      "v_cmp_eq_u64 vcc, s[8:9], v[2:3]\n\t"
      "s_nop 0\n\t"
      "s_cmp_eq_u64 vcc, -1\n\t"
      "s_cbranch_scc1 Lpd11_%=\n\t"
      "s_mov_b32 s10, 200000\n\t"
      "Lp11_%=:\n\t"
      "buffer_load_dwordx2 v[2:3], v27, s[96:99], 0 offen offset:0 sc1\n\t"
      "s_waitcnt vmcnt(0)\n\t"
      "v_cmp_eq_u64 vcc, s[8:9], v[2:3]\n\t"
      "s_nop 0\n\t"
      "s_cmp_eq_u64 vcc, -1\n\t"
      "s_cbranch_scc1 Lpd11_%=\n\t"
      "s_sleep 2\n\t"
      "s_sub_u32 s10, s10, 1\n\t"
      "s_cmp_lg_u32 s10, 0\n\t"
      "s_cbranch_scc1 Lp11_%=\n\t"
      "Lpd11_%=:\n\t"
      STAMP(10, 11)
      "v_mfma_f32_16x16x32_bf16 v[52:55], a[160:163], a[244:247], v[52:55]\n\t"
      "s_add_u32 s24, s24, 16384\n\t"
      "ds_read_b64_tr_b16 a[178:179], v18 offset:28672\n\t"
      "v_readlane_b32 s54, v23, 7\n\t"
      "v_mfma_f32_16x16x32_bf16 v[56:59], a[160:163], a[248:251], v[56:59]\n\t"
      "s_and_b32 s24, s24, 0x1ffff\n\t"
      "buffer_load_dwordx4 a[224:227], v0, s[76:79], s34 offen sc1\n\t"
      "v_add_u32 v18, s64, v4\n\t"
      "v_mfma_f32_16x16x32_bf16 v[60:63], a[160:163], a[252:255], v[60:63]\n\t"
      "s_waitcnt lgkmcnt(6)\n\t"
      "s_bitcmp1_b32 s54, 0\n\t"
      "s_cselect_b64 s[36:37], s[80:81], s[84:85]\n\t"
      "v_mfma_f32_16x16x32_bf16 v[92:95], a[164:167], a[252:255], v[92:95]\n\t"
      "s_cselect_b32 s38, s82, s86\n\t"
      "s_and_b32 s54, s54, -2\n\t"
      "ds_read_b64_tr_b16 a[180:181], v19 offset:24576\n\t"
      "v_mfma_f32_16x16x32_bf16 v[88:91], a[164:167], a[248:251], v[88:91]\n\t"
      "ds_read_b64_tr_b16 a[182:183], v19 offset:28672\n\t"
      "v_add_u32 v1, s54, v17\n\t"
      "v_mfma_f32_16x16x32_bf16 v[84:87], a[164:167], a[244:247], v[84:87]\n\t"
      "v_add_u32 v19, s64, v5\n\t"
      "v_mfma_f32_16x16x32_bf16 v[80:83], a[164:167], a[240:243], v[80:83]\n\t"
      "s_waitcnt lgkmcnt(6)\n\t"
      LSTW(10, "")
      "v_mfma_f32_16x16x32_bf16 v[240:243], a[168:171], a[240:243], v[240:243]\n\t"
      "ds_read_b64_tr_b16 a[184:185], v20 offset:24576\n\t"
      "v_mfma_f32_16x16x32_bf16 v[244:247], a[168:171], a[244:247], v[244:247]\n\t"
      "ds_read_b64_tr_b16 a[186:187], v20 offset:28672\n\t"
      "v_mfma_f32_16x16x32_bf16 v[248:251], a[168:171], a[248:251], v[248:251]\n\t"
      "buffer_load_dwordx4 a[228:231], v0, s[76:79], s34 offen offset:1024 sc1\n\t"
      "v_add_u32 v20, s64, v6\n\t"
      "v_mfma_f32_16x16x32_bf16 v[252:255], a[168:171], a[252:255], v[252:255]\n\t"
      "s_waitcnt lgkmcnt(6)\n\t"
      "v_mfma_f32_16x16x32_bf16 v[236:239], a[172:175], a[252:255], v[236:239]\n\t"
      "ds_read_b64_tr_b16 a[188:189], v21 offset:24576\n\t"
      "v_mfma_f32_16x16x32_bf16 v[232:235], a[172:175], a[248:251], v[232:235]\n\t"
      "ds_read_b64_tr_b16 a[190:191], v21 offset:28672\n\t"
      "v_mfma_f32_16x16x32_bf16 v[228:231], a[172:175], a[244:247], v[228:231]\n\t"
      "v_add_u32 v21, s64, v7\n\t"
      "v_mfma_f32_16x16x32_bf16 v[224:227], a[172:175], a[240:243], v[224:227]\n\t"
      "s_waitcnt lgkmcnt(6)\n\t"
      "v_mfma_f32_16x16x32_bf16 v[144:147], a[176:179], a[240:243], v[144:147]\n\t"
      "s_waitcnt vmcnt(27)\n\t"
      "s_barrier\n\t"
      "ds_read_b64_tr_b16 a[160:161], v18 offset:2048\n\t"
      "v_mfma_f32_16x16x32_bf16 v[148:151], a[176:179], a[244:247], v[148:151]\n\t"
      "ds_read_b64_tr_b16 a[162:163], v18 offset:6144\n\t"
      "v_mfma_f32_16x16x32_bf16 v[152:155], a[176:179], a[248:251], v[152:155]\n\t"
      "buffer_load_dwordx4 a[232:235], v0, s[76:79], s35 offen sc1\n\t"
      "v_mfma_f32_16x16x32_bf16 v[156:159], a[176:179], a[252:255], v[156:159]\n\t"
      "s_waitcnt lgkmcnt(6)\n\t"
      "v_mfma_f32_16x16x32_bf16 v[188:191], a[180:183], a[252:255], v[188:191]\n\t"
      "ds_read_b64_tr_b16 a[164:165], v19 offset:2048\n\t"
      "v_mfma_f32_16x16x32_bf16 v[184:187], a[180:183], a[248:251], v[184:187]\n\t"
      "ds_read_b64_tr_b16 a[166:167], v19 offset:6144\n\t"
      "v_mfma_f32_16x16x32_bf16 v[180:183], a[180:183], a[244:247], v[180:183]\n\t"
      "v_mfma_f32_16x16x32_bf16 v[176:179], a[180:183], a[240:243], v[176:179]\n\t"
      "s_waitcnt lgkmcnt(6)\n\t"
      "v_mfma_f32_16x16x32_bf16 v[112:115], a[184:187], a[240:243], v[112:115]\n\t"
      "ds_read_b64_tr_b16 a[168:169], v20 offset:2048\n\t"
      "v_mfma_f32_16x16x32_bf16 v[116:119], a[184:187], a[244:247], v[116:119]\n\t"
      "ds_read_b64_tr_b16 a[170:171], v20 offset:6144\n\t"
      "v_mfma_f32_16x16x32_bf16 v[120:123], a[184:187], a[248:251], v[120:123]\n\t"
      "buffer_load_dwordx4 a[236:239], v0, s[76:79], s35 offen offset:1024 sc1\n\t"
      "v_mfma_f32_16x16x32_bf16 v[124:127], a[184:187], a[252:255], v[124:127]\n\t"
      "s_waitcnt lgkmcnt(6)\n\t"
      "v_add_u32 v0, s51, v0\n\t"
      "v_mfma_f32_16x16x32_bf16 v[220:223], a[188:191], a[252:255], v[220:223]\n\t"
      "ds_read_b64_tr_b16 a[172:173], v21 offset:2048\n\t"
      "v_mfma_f32_16x16x32_bf16 v[216:219], a[188:191], a[248:251], v[216:219]\n\t"
      "ds_read_b64_tr_b16 a[174:175], v21 offset:6144\n\t"
      "v_mfma_f32_16x16x32_bf16 v[212:215], a[188:191], a[244:247], v[212:215]\n\t"
      "s_waitcnt vmcnt(12)\n\t"
      "v_mfma_f32_16x16x32_bf16 v[208:211], a[188:191], a[240:243], v[208:211]\n\t"
      "s_waitcnt lgkmcnt(6)\n\t"
      "Lg2_%=:\n\t"
      "v_mfma_f32_16x16x32_bf16 v[48:51], a[160:163], a[192:195], v[48:51]\n\t"
      "ds_read_b64_tr_b16 a[176:177], v18 offset:0\n\t"
      "v_mfma_f32_16x16x32_bf16 v[52:55], a[160:163], a[196:199], v[52:55]\n\t"
      "ds_read_b64_tr_b16 a[178:179], v18 offset:4096\n\t"
      "v_mfma_f32_16x16x32_bf16 v[56:59], a[160:163], a[200:203], v[56:59]\n\t"
      "buffer_load_dwordx4 a[240:243], v0, s[76:79], s34 offen sc1\n\t"
      "v_mfma_f32_16x16x32_bf16 v[60:63], a[160:163], a[204:207], v[60:63]\n\t"
      "s_waitcnt lgkmcnt(6)\n\t"
      "s_mov_b32 m0, s24\n\t"
      "v_mfma_f32_16x16x32_bf16 v[92:95], a[164:167], a[204:207], v[92:95]\n\t"
      "ds_read_b64_tr_b16 a[180:181], v19 offset:0\n\t"
      "buffer_load_dwordx4 v1, s[36:39], s20 offen lds\n\t"
      "v_mfma_f32_16x16x32_bf16 v[88:91], a[164:167], a[200:203], v[88:91]\n\t"
      "ds_read_b64_tr_b16 a[182:183], v19 offset:4096\n\t"
      "v_mfma_f32_16x16x32_bf16 v[84:87], a[164:167], a[196:199], v[84:87]\n\t"
      "v_mfma_f32_16x16x32_bf16 v[80:83], a[164:167], a[192:195], v[80:83]\n\t"
      "s_waitcnt lgkmcnt(6)\n\t"
      "v_mfma_f32_16x16x32_bf16 v[240:243], a[168:171], a[192:195], v[240:243]\n\t"
      "ds_read_b64_tr_b16 a[184:185], v20 offset:0\n\t"
      "v_mfma_f32_16x16x32_bf16 v[244:247], a[168:171], a[196:199], v[244:247]\n\t"
      "ds_read_b64_tr_b16 a[186:187], v20 offset:4096\n\t"
      "v_mfma_f32_16x16x32_bf16 v[248:251], a[168:171], a[200:203], v[248:251]\n\t"
      "buffer_load_dwordx4 a[244:247], v0, s[76:79], s34 offen offset:1024 sc1\n\t"
      "v_mfma_f32_16x16x32_bf16 v[252:255], a[168:171], a[204:207], v[252:255]\n\t"
      "s_waitcnt lgkmcnt(6)\n\t"
      "v_mfma_f32_16x16x32_bf16 v[236:239], a[172:175], a[204:207], v[236:239]\n\t"
      "ds_read_b64_tr_b16 a[188:189], v21 offset:0\n\t"
      "v_mfma_f32_16x16x32_bf16 v[232:235], a[172:175], a[200:203], v[232:235]\n\t"
      "ds_read_b64_tr_b16 a[190:191], v21 offset:4096\n\t"
      "buffer_load_dwordx4 v1, s[36:39], s21 offen offset:1024 lds\n\t"
      "v_mfma_f32_16x16x32_bf16 v[228:231], a[172:175], a[196:199], v[228:231]\n\t"
      "v_mfma_f32_16x16x32_bf16 v[224:227], a[172:175], a[192:195], v[224:227]\n\t"
      "s_waitcnt lgkmcnt(6)\n\t"
      "v_mfma_f32_16x16x32_bf16 v[144:147], a[176:179], a[192:195], v[144:147]\n\t"
      "ds_read_b64_tr_b16 a[160:161], v18 offset:10240\n\t"
      "v_mfma_f32_16x16x32_bf16 v[148:151], a[176:179], a[196:199], v[148:151]\n\t"
      "ds_read_b64_tr_b16 a[162:163], v18 offset:14336\n\t"
      "v_mfma_f32_16x16x32_bf16 v[152:155], a[176:179], a[200:203], v[152:155]\n\t"
      "buffer_load_dwordx4 a[248:251], v0, s[76:79], s35 offen sc1\n\t"
      "v_mfma_f32_16x16x32_bf16 v[156:159], a[176:179], a[204:207], v[156:159]\n\t"
      "s_waitcnt lgkmcnt(6)\n\t"
      "v_mfma_f32_16x16x32_bf16 v[188:191], a[180:183], a[204:207], v[188:191]\n\t"
      "ds_read_b64_tr_b16 a[164:165], v19 offset:10240\n\t"
      "v_mfma_f32_16x16x32_bf16 v[184:187], a[180:183], a[200:203], v[184:187]\n\t"
      "ds_read_b64_tr_b16 a[166:167], v19 offset:14336\n\t"
      "v_mfma_f32_16x16x32_bf16 v[180:183], a[180:183], a[196:199], v[180:183]\n\t"
      "v_mfma_f32_16x16x32_bf16 v[176:179], a[180:183], a[192:195], v[176:179]\n\t"
      "s_waitcnt lgkmcnt(6)\n\t"
      "buffer_load_dwordx4 v1, s[36:39], s22 offen offset:2048 lds\n\t"
      "v_mfma_f32_16x16x32_bf16 v[112:115], a[184:187], a[192:195], v[112:115]\n\t"
      "ds_read_b64_tr_b16 a[168:169], v20 offset:10240\n\t"
      "v_mfma_f32_16x16x32_bf16 v[116:119], a[184:187], a[196:199], v[116:119]\n\t"
      "ds_read_b64_tr_b16 a[170:171], v20 offset:14336\n\t"
      "v_mfma_f32_16x16x32_bf16 v[120:123], a[184:187], a[200:203], v[120:123]\n\t"
      "buffer_load_dwordx4 a[252:255], v0, s[76:79], s35 offen offset:1024 sc1\n\t"
      "v_mfma_f32_16x16x32_bf16 v[124:127], a[184:187], a[204:207], v[124:127]\n\t"
      "s_waitcnt lgkmcnt(6)\n\t"
      "v_readlane_b32 s53, v23, s48\n\t"
      "v_mfma_f32_16x16x32_bf16 v[220:223], a[188:191], a[204:207], v[220:223]\n\t"
      "ds_read_b64_tr_b16 a[172:173], v21 offset:10240\n\t"
      "v_mfma_f32_16x16x32_bf16 v[216:219], a[188:191], a[200:203], v[216:219]\n\t"
      "ds_read_b64_tr_b16 a[174:175], v21 offset:14336\n\t"
      "v_add_u32 v0, s53, v16\n\t"
      "v_mfma_f32_16x16x32_bf16 v[212:215], a[188:191], a[196:199], v[212:215]\n\t"
      "s_waitcnt vmcnt(12)\n\t"
      "s_add_u32 s48, s48, 1\n\t"
      "v_mfma_f32_16x16x32_bf16 v[208:211], a[188:191], a[192:195], v[208:211]\n\t"
      "s_waitcnt lgkmcnt(6)\n\t"
      "s_cmp_eq_u32 s68, 2\n\t"
      "s_cbranch_scc1 Lh_%=\n\t"
      "Lhr_%=:\n\t"
      "v_mfma_f32_16x16x32_bf16 v[48:51], a[160:163], a[208:211], v[48:51]\n\t"
      "ds_read_b64_tr_b16 a[176:177], v18 offset:8192\n\t"
      "buffer_load_dwordx4 v1, s[36:39], s23 offen offset:3072 lds\n\t"
      "v_mfma_f32_16x16x32_bf16 v[52:55], a[160:163], a[212:215], v[52:55]\n\t"
      "s_add_u32 s24, s24, 16384\n\t"
      "ds_read_b64_tr_b16 a[178:179], v18 offset:12288\n\t"
      "v_readlane_b32 s54, v23, s50\n\t"
      "v_mfma_f32_16x16x32_bf16 v[56:59], a[160:163], a[216:219], v[56:59]\n\t"
      "s_and_b32 s24, s24, 0x1ffff\n\t"
      "buffer_load_dwordx4 a[192:195], v0, s[76:79], s34 offen sc1\n\t"
      "v_mfma_f32_16x16x32_bf16 v[60:63], a[160:163], a[220:223], v[60:63]\n\t"
      "s_waitcnt lgkmcnt(6)\n\t"
      "s_bitcmp1_b32 s54, 0\n\t"
      "s_cselect_b64 s[36:37], s[80:81], s[84:85]\n\t"
      "v_mfma_f32_16x16x32_bf16 v[92:95], a[164:167], a[220:223], v[92:95]\n\t"
      "s_cselect_b32 s38, s82, s86\n\t"
      "s_and_b32 s54, s54, -2\n\t"
      "ds_read_b64_tr_b16 a[180:181], v19 offset:8192\n\t"
      "v_mfma_f32_16x16x32_bf16 v[88:91], a[164:167], a[216:219], v[88:91]\n\t"
      "ds_read_b64_tr_b16 a[182:183], v19 offset:12288\n\t"
      "v_add_u32 v1, s54, v17\n\t"
      "v_mfma_f32_16x16x32_bf16 v[84:87], a[164:167], a[212:215], v[84:87]\n\t"
      "s_add_u32 s50, s50, 1\n\t"
      "v_mfma_f32_16x16x32_bf16 v[80:83], a[164:167], a[208:211], v[80:83]\n\t"
      "s_waitcnt lgkmcnt(6)\n\t"
      "v_mfma_f32_16x16x32_bf16 v[240:243], a[168:171], a[208:211], v[240:243]\n\t"
      "ds_read_b64_tr_b16 a[184:185], v20 offset:8192\n\t"
      "v_mfma_f32_16x16x32_bf16 v[244:247], a[168:171], a[212:215], v[244:247]\n\t"
      "ds_read_b64_tr_b16 a[186:187], v20 offset:12288\n\t"
      "v_mfma_f32_16x16x32_bf16 v[248:251], a[168:171], a[216:219], v[248:251]\n\t"
      "buffer_load_dwordx4 a[196:199], v0, s[76:79], s34 offen offset:1024 sc1\n\t"
      "v_mfma_f32_16x16x32_bf16 v[252:255], a[168:171], a[220:223], v[252:255]\n\t"
      "s_waitcnt lgkmcnt(6)\n\t"
      "v_mfma_f32_16x16x32_bf16 v[236:239], a[172:175], a[220:223], v[236:239]\n\t"
      "ds_read_b64_tr_b16 a[188:189], v21 offset:8192\n\t"
      "buffer_load_dword v42, v41, s[40:43], 0 offen\n\t"
      "v_mfma_f32_16x16x32_bf16 v[232:235], a[172:175], a[216:219], v[232:235]\n\t"
      "ds_read_b64_tr_b16 a[190:191], v21 offset:12288\n\t"
      "v_mfma_f32_16x16x32_bf16 v[228:231], a[172:175], a[212:215], v[228:231]\n\t"
      "v_mfma_f32_16x16x32_bf16 v[224:227], a[172:175], a[208:211], v[224:227]\n\t"
      "s_waitcnt lgkmcnt(6)\n\t"
      "v_mfma_f32_16x16x32_bf16 v[144:147], a[176:179], a[208:211], v[144:147]\n\t"
      "s_waitcnt vmcnt(28)\n\t"
      "s_barrier\n\t"
      "ds_read_b64_tr_b16 a[160:161], v18 offset:18432\n\t"
      "v_mfma_f32_16x16x32_bf16 v[148:151], a[176:179], a[212:215], v[148:151]\n\t"
      "ds_read_b64_tr_b16 a[162:163], v18 offset:22528\n\t"
      "v_mfma_f32_16x16x32_bf16 v[152:155], a[176:179], a[216:219], v[152:155]\n\t"
      "buffer_load_dwordx4 a[200:203], v0, s[76:79], s35 offen sc1\n\t"
      "v_mfma_f32_16x16x32_bf16 v[156:159], a[176:179], a[220:223], v[156:159]\n\t"
      "s_waitcnt lgkmcnt(6)\n\t"
      "v_mfma_f32_16x16x32_bf16 v[188:191], a[180:183], a[220:223], v[188:191]\n\t"
      "ds_read_b64_tr_b16 a[164:165], v19 offset:18432\n\t"
      "v_mfma_f32_16x16x32_bf16 v[184:187], a[180:183], a[216:219], v[184:187]\n\t"
      "ds_read_b64_tr_b16 a[166:167], v19 offset:22528\n\t"
      "v_mfma_f32_16x16x32_bf16 v[180:183], a[180:183], a[212:215], v[180:183]\n\t"
      "v_mfma_f32_16x16x32_bf16 v[176:179], a[180:183], a[208:211], v[176:179]\n\t"
      "s_waitcnt lgkmcnt(6)\n\t"
      "v_mfma_f32_16x16x32_bf16 v[112:115], a[184:187], a[208:211], v[112:115]\n\t"
      "ds_read_b64_tr_b16 a[168:169], v20 offset:18432\n\t"
      "v_mfma_f32_16x16x32_bf16 v[116:119], a[184:187], a[212:215], v[116:119]\n\t"
      "ds_read_b64_tr_b16 a[170:171], v20 offset:22528\n\t"
      "v_mfma_f32_16x16x32_bf16 v[120:123], a[184:187], a[216:219], v[120:123]\n\t"
      "buffer_load_dwordx4 a[204:207], v0, s[76:79], s35 offen offset:1024 sc1\n\t"
      "v_mfma_f32_16x16x32_bf16 v[124:127], a[184:187], a[220:223], v[124:127]\n\t"
      "s_waitcnt lgkmcnt(6)\n\t"
      "v_add_u32 v0, s51, v0\n\t"
      "v_mfma_f32_16x16x32_bf16 v[220:223], a[188:191], a[220:223], v[220:223]\n\t"
      "ds_read_b64_tr_b16 a[172:173], v21 offset:18432\n\t"
      "v_mfma_f32_16x16x32_bf16 v[216:219], a[188:191], a[216:219], v[216:219]\n\t"
      "ds_read_b64_tr_b16 a[174:175], v21 offset:22528\n\t"
      "v_mfma_f32_16x16x32_bf16 v[212:215], a[188:191], a[212:215], v[212:215]\n\t"
      "s_waitcnt vmcnt(13)\n\t"
      "v_mfma_f32_16x16x32_bf16 v[208:211], a[188:191], a[208:211], v[208:211]\n\t"
      "s_waitcnt lgkmcnt(6)\n\t"
      "v_mfma_f32_16x16x32_bf16 v[48:51], a[160:163], a[224:227], v[48:51]\n\t"
      "ds_read_b64_tr_b16 a[176:177], v18 offset:16384\n\t"
      "v_mfma_f32_16x16x32_bf16 v[52:55], a[160:163], a[228:231], v[52:55]\n\t"
      "ds_read_b64_tr_b16 a[178:179], v18 offset:20480\n\t"
      "v_mfma_f32_16x16x32_bf16 v[56:59], a[160:163], a[232:235], v[56:59]\n\t"
      "buffer_load_dwordx4 a[208:211], v0, s[76:79], s34 offen sc1\n\t"
      "v_mfma_f32_16x16x32_bf16 v[60:63], a[160:163], a[236:239], v[60:63]\n\t"
      "s_waitcnt lgkmcnt(6)\n\t"
      "s_mov_b32 m0, s24\n\t"
      "v_mfma_f32_16x16x32_bf16 v[92:95], a[164:167], a[236:239], v[92:95]\n\t"
      "ds_read_b64_tr_b16 a[180:181], v19 offset:16384\n\t"
      "buffer_load_dwordx4 v1, s[36:39], s20 offen lds\n\t"
      "v_mfma_f32_16x16x32_bf16 v[88:91], a[164:167], a[232:235], v[88:91]\n\t"
      "ds_read_b64_tr_b16 a[182:183], v19 offset:20480\n\t"
      "v_mfma_f32_16x16x32_bf16 v[84:87], a[164:167], a[228:231], v[84:87]\n\t"
      "v_mfma_f32_16x16x32_bf16 v[80:83], a[164:167], a[224:227], v[80:83]\n\t"
      "s_waitcnt lgkmcnt(6)\n\t"
      "v_mfma_f32_16x16x32_bf16 v[240:243], a[168:171], a[224:227], v[240:243]\n\t"
      "ds_read_b64_tr_b16 a[184:185], v20 offset:16384\n\t"
      "v_mfma_f32_16x16x32_bf16 v[244:247], a[168:171], a[228:231], v[244:247]\n\t"
      "ds_read_b64_tr_b16 a[186:187], v20 offset:20480\n\t"
      "v_mfma_f32_16x16x32_bf16 v[248:251], a[168:171], a[232:235], v[248:251]\n\t"
      "buffer_load_dwordx4 a[212:215], v0, s[76:79], s34 offen offset:1024 sc1\n\t"
      "v_mfma_f32_16x16x32_bf16 v[252:255], a[168:171], a[236:239], v[252:255]\n\t"
      "s_waitcnt lgkmcnt(6)\n\t"
      "v_mfma_f32_16x16x32_bf16 v[236:239], a[172:175], a[236:239], v[236:239]\n\t"
      "ds_read_b64_tr_b16 a[188:189], v21 offset:16384\n\t"
      "buffer_load_dwordx2 v[2:3], v27, s[96:99], 0 offen offset:2048 sc1\n\t"
      "v_mfma_f32_16x16x32_bf16 v[232:235], a[172:175], a[232:235], v[232:235]\n\t"
      "ds_read_b64_tr_b16 a[190:191], v21 offset:20480\n\t"
      "buffer_load_dwordx4 v1, s[36:39], s21 offen offset:1024 lds\n\t"
      "v_mfma_f32_16x16x32_bf16 v[228:231], a[172:175], a[228:231], v[228:231]\n\t"
      "v_mfma_f32_16x16x32_bf16 v[224:227], a[172:175], a[224:227], v[224:227]\n\t"
      "s_waitcnt lgkmcnt(6)\n\t"
      "v_mfma_f32_16x16x32_bf16 v[144:147], a[176:179], a[224:227], v[144:147]\n\t"
      "ds_read_b64_tr_b16 a[160:161], v18 offset:26624\n\t"
      "v_mfma_f32_16x16x32_bf16 v[148:151], a[176:179], a[228:231], v[148:151]\n\t"
      "ds_read_b64_tr_b16 a[162:163], v18 offset:30720\n\t"
      "v_mfma_f32_16x16x32_bf16 v[152:155], a[176:179], a[232:235], v[152:155]\n\t"
      "buffer_load_dwordx4 a[216:219], v0, s[76:79], s35 offen sc1\n\t"
      "v_mfma_f32_16x16x32_bf16 v[156:159], a[176:179], a[236:239], v[156:159]\n\t"
      "s_waitcnt lgkmcnt(6)\n\t"
      "v_mfma_f32_16x16x32_bf16 v[188:191], a[180:183], a[236:239], v[188:191]\n\t"
      "ds_read_b64_tr_b16 a[164:165], v19 offset:26624\n\t"
      "v_mfma_f32_16x16x32_bf16 v[184:187], a[180:183], a[232:235], v[184:187]\n\t"
      "ds_read_b64_tr_b16 a[166:167], v19 offset:30720\n\t"
      "v_mfma_f32_16x16x32_bf16 v[180:183], a[180:183], a[228:231], v[180:183]\n\t"
      "v_mfma_f32_16x16x32_bf16 v[176:179], a[180:183], a[224:227], v[176:179]\n\t"
      "s_waitcnt lgkmcnt(6)\n\t"
      "buffer_load_dwordx4 v1, s[36:39], s22 offen offset:2048 lds\n\t"
      "v_mfma_f32_16x16x32_bf16 v[112:115], a[184:187], a[224:227], v[112:115]\n\t"
      "ds_read_b64_tr_b16 a[168:169], v20 offset:26624\n\t"
      "v_mfma_f32_16x16x32_bf16 v[116:119], a[184:187], a[228:231], v[116:119]\n\t"
      "ds_read_b64_tr_b16 a[170:171], v20 offset:30720\n\t"
      "v_mfma_f32_16x16x32_bf16 v[120:123], a[184:187], a[232:235], v[120:123]\n\t"
      "s_add_u32 s64, s64, 32768\n\t"
      "s_and_b32 s64, s64, 0x1ffff\n\t"
      "buffer_load_dwordx4 a[220:223], v0, s[76:79], s35 offen offset:1024 sc1\n\t"
      "v_mfma_f32_16x16x32_bf16 v[124:127], a[184:187], a[236:239], v[124:127]\n\t"
      "s_waitcnt lgkmcnt(6)\n\t"
      "v_readlane_b32 s53, v23, s48\n\t"
      "v_mfma_f32_16x16x32_bf16 v[220:223], a[188:191], a[236:239], v[220:223]\n\t"
      "ds_read_b64_tr_b16 a[172:173], v21 offset:26624\n\t"
      "v_mfma_f32_16x16x32_bf16 v[216:219], a[188:191], a[232:235], v[216:219]\n\t"
      "ds_read_b64_tr_b16 a[174:175], v21 offset:30720\n\t"
      "v_add_u32 v0, s53, v16\n\t"
      "v_mfma_f32_16x16x32_bf16 v[212:215], a[188:191], a[228:231], v[212:215]\n\t"
      "s_waitcnt vmcnt(14)\n\t"
      "s_add_u32 s48, s48, 1\n\t"
      "v_mfma_f32_16x16x32_bf16 v[208:211], a[188:191], a[224:227], v[208:211]\n\t"
      "s_waitcnt lgkmcnt(6)\n\t"
      "v_mfma_f32_16x16x32_bf16 v[48:51], a[160:163], a[240:243], v[48:51]\n\t"
      "ds_read_b64_tr_b16 a[176:177], v18 offset:24576\n\t"
      "buffer_load_dwordx4 v1, s[36:39], s23 offen offset:3072 lds\n\t"
      "v_mfma_f32_16x16x32_bf16 v[52:55], a[160:163], a[244:247], v[52:55]\n\t"
      "s_add_u32 s24, s24, 16384\n\t"
      "ds_read_b64_tr_b16 a[178:179], v18 offset:28672\n\t"
      "v_readlane_b32 s54, v23, s50\n\t"
      "v_mfma_f32_16x16x32_bf16 v[56:59], a[160:163], a[248:251], v[56:59]\n\t"
      "s_and_b32 s24, s24, 0x1ffff\n\t"
      "buffer_load_dwordx4 a[224:227], v0, s[76:79], s34 offen sc1\n\t"
      "v_add_u32 v18, s64, v4\n\t"
      "v_mfma_f32_16x16x32_bf16 v[60:63], a[160:163], a[252:255], v[60:63]\n\t"
      "s_waitcnt lgkmcnt(6)\n\t"
      "s_bitcmp1_b32 s54, 0\n\t"
      "s_cselect_b64 s[36:37], s[80:81], s[84:85]\n\t"
      "v_mfma_f32_16x16x32_bf16 v[92:95], a[164:167], a[252:255], v[92:95]\n\t"
      "s_cselect_b32 s38, s82, s86\n\t"
      "s_and_b32 s54, s54, -2\n\t"
      "ds_read_b64_tr_b16 a[180:181], v19 offset:24576\n\t"
      "v_mfma_f32_16x16x32_bf16 v[88:91], a[164:167], a[248:251], v[88:91]\n\t"
      "ds_read_b64_tr_b16 a[182:183], v19 offset:28672\n\t"
      "v_add_u32 v1, s54, v17\n\t"
      "v_mfma_f32_16x16x32_bf16 v[84:87], a[164:167], a[244:247], v[84:87]\n\t"
      "s_add_u32 s50, s50, 1\n\t"
      "v_add_u32 v19, s64, v5\n\t"
      "v_mfma_f32_16x16x32_bf16 v[80:83], a[164:167], a[240:243], v[80:83]\n\t"
      "s_waitcnt lgkmcnt(6)\n\t"
      "v_mfma_f32_16x16x32_bf16 v[240:243], a[168:171], a[240:243], v[240:243]\n\t"
      "ds_read_b64_tr_b16 a[184:185], v20 offset:24576\n\t"
      "v_mfma_f32_16x16x32_bf16 v[244:247], a[168:171], a[244:247], v[244:247]\n\t"
      "ds_read_b64_tr_b16 a[186:187], v20 offset:28672\n\t"
      "v_mfma_f32_16x16x32_bf16 v[248:251], a[168:171], a[248:251], v[248:251]\n\t"
      "buffer_load_dwordx4 a[228:231], v0, s[76:79], s34 offen offset:1024 sc1\n\t"
      "v_add_u32 v20, s64, v6\n\t"
      "v_mfma_f32_16x16x32_bf16 v[252:255], a[168:171], a[252:255], v[252:255]\n\t"
      "s_waitcnt lgkmcnt(6)\n\t"
      "v_mfma_f32_16x16x32_bf16 v[236:239], a[172:175], a[252:255], v[236:239]\n\t"
      "ds_read_b64_tr_b16 a[188:189], v21 offset:24576\n\t"
      "v_mfma_f32_16x16x32_bf16 v[232:235], a[172:175], a[248:251], v[232:235]\n\t"
      "ds_read_b64_tr_b16 a[190:191], v21 offset:28672\n\t"
      "v_mfma_f32_16x16x32_bf16 v[228:231], a[172:175], a[244:247], v[228:231]\n\t"
      "v_add_u32 v21, s64, v7\n\t"
      "v_mfma_f32_16x16x32_bf16 v[224:227], a[172:175], a[240:243], v[224:227]\n\t"
      "s_waitcnt lgkmcnt(6)\n\t"
      "v_mfma_f32_16x16x32_bf16 v[144:147], a[176:179], a[240:243], v[144:147]\n\t"
      "s_waitcnt vmcnt(28)\n\t"
      "s_barrier\n\t"
      "ds_read_b64_tr_b16 a[160:161], v18 offset:2048\n\t"
      "v_mfma_f32_16x16x32_bf16 v[148:151], a[176:179], a[244:247], v[148:151]\n\t"
      "ds_read_b64_tr_b16 a[162:163], v18 offset:6144\n\t"
      "v_mfma_f32_16x16x32_bf16 v[152:155], a[176:179], a[248:251], v[152:155]\n\t"
      "buffer_load_dwordx4 a[232:235], v0, s[76:79], s35 offen sc1\n\t"
      "v_mfma_f32_16x16x32_bf16 v[156:159], a[176:179], a[252:255], v[156:159]\n\t"
      "s_waitcnt lgkmcnt(6)\n\t"
      "v_mfma_f32_16x16x32_bf16 v[188:191], a[180:183], a[252:255], v[188:191]\n\t"
      "ds_read_b64_tr_b16 a[164:165], v19 offset:2048\n\t"
      "v_mfma_f32_16x16x32_bf16 v[184:187], a[180:183], a[248:251], v[184:187]\n\t"
      "ds_read_b64_tr_b16 a[166:167], v19 offset:6144\n\t"
      "v_mfma_f32_16x16x32_bf16 v[180:183], a[180:183], a[244:247], v[180:183]\n\t"
      "v_mfma_f32_16x16x32_bf16 v[176:179], a[180:183], a[240:243], v[176:179]\n\t"
      "s_waitcnt lgkmcnt(6)\n\t"
      "v_mfma_f32_16x16x32_bf16 v[112:115], a[184:187], a[240:243], v[112:115]\n\t"
      "ds_read_b64_tr_b16 a[168:169], v20 offset:2048\n\t"
      "v_mfma_f32_16x16x32_bf16 v[116:119], a[184:187], a[244:247], v[116:119]\n\t"
      "ds_read_b64_tr_b16 a[170:171], v20 offset:6144\n\t"
      "v_mfma_f32_16x16x32_bf16 v[120:123], a[184:187], a[248:251], v[120:123]\n\t"
      "buffer_load_dwordx4 a[236:239], v0, s[76:79], s35 offen offset:1024 sc1\n\t"
      "v_mfma_f32_16x16x32_bf16 v[124:127], a[184:187], a[252:255], v[124:127]\n\t"
      "s_waitcnt lgkmcnt(6)\n\t"
      "v_add_u32 v0, s51, v0\n\t"
      "v_mfma_f32_16x16x32_bf16 v[220:223], a[188:191], a[252:255], v[220:223]\n\t"
      "ds_read_b64_tr_b16 a[172:173], v21 offset:2048\n\t"
      "v_mfma_f32_16x16x32_bf16 v[216:219], a[188:191], a[248:251], v[216:219]\n\t"
      "s_sub_u32 s68, s68, 1\n\t"
      "ds_read_b64_tr_b16 a[174:175], v21 offset:6144\n\t"
      "v_mfma_f32_16x16x32_bf16 v[212:215], a[188:191], a[244:247], v[212:215]\n\t"
      "s_cmp_lg_u32 s68, 0\n\t"
      "s_waitcnt vmcnt(13)\n\t"
      "v_mfma_f32_16x16x32_bf16 v[208:211], a[188:191], a[240:243], v[208:211]\n\t"
      "s_waitcnt lgkmcnt(6)\n\t"
      "s_cbranch_scc1 Lg2_%=\n\t"
      "Llast_%=:\n\t"
      "v_mfma_f32_16x16x32_bf16 v[48:51], a[160:163], a[192:195], v[48:51]\n\t"
      STAMP(34, 35)
      "v_mfma_f32_16x16x32_bf16 v[52:55], a[160:163], a[196:199], v[52:55]\n\t"
      "v_add_u32 v10, 65536, v18\n\t"
      "v_add_u32 v11, 65536, v19\n\t"
      "v_mfma_f32_16x16x32_bf16 v[56:59], a[160:163], a[200:203], v[56:59]\n\t"
      "buffer_load_dwordx4 a[240:243], v0, s[76:79], s34 offen sc1\n\t"
      "v_add_u32 v14, 65536, v20\n\t"
      "v_mfma_f32_16x16x32_bf16 v[60:63], a[160:163], a[204:207], v[60:63]\n\t"
      "s_waitcnt lgkmcnt(4)\n\t"
      "v_add_u32 v15, 65536, v21\n\t"
      "s_add_u32 s71, s35, 131072\n\t"
      "v_mfma_f32_16x16x32_bf16 v[92:95], a[164:167], a[204:207], v[92:95]\n\t"
      "s_add_u32 s52, s34, 131072\n\t"
      "ds_read_b128 a[32:35], v72 offset:0\n\t"
      "v_mfma_f32_16x16x32_bf16 v[88:91], a[164:167], a[200:203], v[88:91]\n\t"
      "ds_read_b128 a[36:39], v73 offset:0\n\t"
      "v_mfma_f32_16x16x32_bf16 v[84:87], a[164:167], a[196:199], v[84:87]\n\t"
      "buffer_load_dwordx4 a[244:247], v0, s[76:79], s34 offen offset:1024 sc1\n\t"
      "ds_read_b128 a[40:43], v72 offset:2048\n\t"
      "v_mfma_f32_16x16x32_bf16 v[80:83], a[164:167], a[192:195], v[80:83]\n\t"
      "s_waitcnt lgkmcnt(5)\n\t"
      "ds_read_b128 a[44:47], v73 offset:2048\n\t"
      "v_mfma_f32_16x16x32_bf16 v[240:243], a[168:171], a[192:195], v[240:243]\n\t"
      "ds_read_b64_tr_b16 a[176:177], v18 offset:0\n\t"
      LSTW(34, "")
      "v_mfma_f32_16x16x32_bf16 v[244:247], a[168:171], a[196:199], v[244:247]\n\t"
      "ds_read_b64_tr_b16 a[178:179], v18 offset:4096\n\t"
      "v_mfma_f32_16x16x32_bf16 v[248:251], a[168:171], a[200:203], v[248:251]\n\t"
      "ds_read_b64_tr_b16 a[180:181], v19 offset:0\n\t"
      "buffer_load_dwordx4 a[248:251], v0, s[76:79], s35 offen sc1\n\t"
      "s_add_u32 s17, s18, 64\n\t"
      "s_add_u32 s14, s56, 8192\n\t"
      "s_add_u32 s15, s56, 4096\n\t"
      "v_mfma_f32_16x16x32_bf16 v[252:255], a[168:171], a[204:207], v[252:255]\n\t"
      "s_waitcnt lgkmcnt(7)\n\t"
      "ds_read_b64_tr_b16 a[182:183], v19 offset:4096\n\t"
      "v_mfma_f32_16x16x32_bf16 v[236:239], a[172:175], a[204:207], v[236:239]\n\t"
      "ds_read_b64_tr_b16 a[184:185], v20 offset:0\n\t"
      "v_mfma_f32_16x16x32_bf16 v[232:235], a[172:175], a[200:203], v[232:235]\n\t"
      "ds_read_b64_tr_b16 a[186:187], v20 offset:4096\n\t"
      "v_mfma_f32_16x16x32_bf16 v[228:231], a[172:175], a[196:199], v[228:231]\n\t"
      "ds_read_b64_tr_b16 a[188:189], v21 offset:0\n\t"
      "buffer_load_dwordx4 a[252:255], v0, s[76:79], s35 offen offset:1024 sc1\n\t"
      "v_mfma_f32_16x16x32_bf16 v[224:227], a[172:175], a[192:195], v[224:227]\n\t"
      "s_waitcnt lgkmcnt(3)\n\t"
      "ds_read_b64_tr_b16 a[190:191], v21 offset:4096\n\t"
      "v_mfma_f32_16x16x32_bf16 v[152:155], a[176:179], a[200:203], v[152:155]\n\t"
      "ds_read_b64_tr_b16 a[160:161], v18 offset:10240\n\t"
      "v_mfma_f32_16x16x32_bf16 v[156:159], a[176:179], a[204:207], v[156:159]\n\t"
      "ds_read_b64_tr_b16 a[162:163], v18 offset:14336\n\t"
      "v_readlane_b32 s53, v23, 44\n\t"
      "v_mfma_f32_16x16x32_bf16 v[188:191], a[180:183], a[204:207], v[188:191]\n\t"
      "ds_read_b64_tr_b16 a[164:165], v19 offset:10240\n\t"
      "v_mfma_f32_16x16x32_bf16 v[184:187], a[180:183], a[200:203], v[184:187]\n\t"
      "s_waitcnt lgkmcnt(3)\n\t"
      "ds_read_b64_tr_b16 a[166:167], v19 offset:14336\n\t"
      "v_add_u32 v0, s53, v16\n\t"
      "v_mfma_f32_16x16x32_bf16 v[120:123], a[184:187], a[200:203], v[120:123]\n\t"
      "ds_read_b64_tr_b16 a[168:169], v20 offset:10240\n\t"
      "buffer_load_dwordx4 a[0:3], v0, s[76:79], s35 offen sc1\n\t"
      "v_mfma_f32_16x16x32_bf16 v[124:127], a[184:187], a[204:207], v[124:127]\n\t"
      "ds_read_b64_tr_b16 a[170:171], v20 offset:14336\n\t"
      "v_mfma_f32_16x16x32_bf16 v[220:223], a[188:191], a[204:207], v[220:223]\n\t"
      "s_waitcnt vmcnt(10)\n\t"
      "ds_read_b64_tr_b16 a[172:173], v21 offset:10240\n\t"
      "v_mfma_f32_16x16x32_bf16 v[216:219], a[188:191], a[200:203], v[216:219]\n\t"
      "s_waitcnt lgkmcnt(3)\n\t"
      "ds_read_b64_tr_b16 a[174:175], v21 offset:14336\n\t"
      "v_mfma_f32_16x16x32_bf16 v[56:59], a[160:163], a[216:219], v[56:59]\n\t"
      "ds_read_b64_tr_b16 a[176:177], v18 offset:8192\n\t"
      "buffer_load_dwordx4 a[4:7], v0, s[76:79], s35 offen offset:1024 sc1\n\t"
      "v_mfma_f32_16x16x32_bf16 v[60:63], a[160:163], a[220:223], v[60:63]\n\t"
      "ds_read_b64_tr_b16 a[178:179], v18 offset:12288\n\t"
      "v_mfma_f32_16x16x32_bf16 v[92:95], a[164:167], a[220:223], v[92:95]\n\t"
      "ds_read_b64_tr_b16 a[180:181], v19 offset:8192\n\t"
      "v_mfma_f32_16x16x32_bf16 v[88:91], a[164:167], a[216:219], v[88:91]\n\t"
      "s_waitcnt lgkmcnt(3)\n\t"
      "ds_read_b64_tr_b16 a[182:183], v19 offset:12288\n\t"
      "v_mfma_f32_16x16x32_bf16 v[248:251], a[168:171], a[216:219], v[248:251]\n\t"
      "ds_read_b64_tr_b16 a[184:185], v20 offset:8192\n\t"
      "buffer_load_dwordx4 a[8:11], v0, s[76:79], s71 offen sc1\n\t"
      "v_mfma_f32_16x16x32_bf16 v[252:255], a[168:171], a[220:223], v[252:255]\n\t"
      "ds_read_b64_tr_b16 a[186:187], v20 offset:12288\n\t"
      "v_mfma_f32_16x16x32_bf16 v[236:239], a[172:175], a[220:223], v[236:239]\n\t"
      "ds_read_b64_tr_b16 a[188:189], v21 offset:8192\n\t"
      "v_mfma_f32_16x16x32_bf16 v[232:235], a[172:175], a[216:219], v[232:235]\n\t"
      "s_waitcnt lgkmcnt(3)\n\t"
      "ds_read_b64_tr_b16 a[190:191], v21 offset:12288\n\t"
      "v_mfma_f32_16x16x32_bf16 v[152:155], a[176:179], a[216:219], v[152:155]\n\t"
      "s_waitcnt vmcnt(25)\n\t"
      "s_barrier\n\t"
      "ds_read_b64_tr_b16 a[160:161], v18 offset:18432\n\t"
      "buffer_load_dwordx4 a[12:15], v0, s[76:79], s71 offen offset:1024 sc1\n\t"
      "v_mfma_f32_16x16x32_bf16 v[156:159], a[176:179], a[220:223], v[156:159]\n\t"
      "s_mov_b32 m0, s24\n\t"
      "ds_read_b64_tr_b16 a[162:163], v18 offset:22528\n\t"
      "v_mfma_f32_16x16x32_bf16 v[188:191], a[180:183], a[220:223], v[188:191]\n\t"
      "ds_read_b64_tr_b16 a[164:165], v19 offset:18432\n\t"
      "buffer_load_dwordx4 v1, s[36:39], s20 offen lds\n\t"
      "v_mfma_f32_16x16x32_bf16 v[184:187], a[180:183], a[216:219], v[184:187]\n\t"
      "s_waitcnt lgkmcnt(3)\n\t"
      "ds_read_b64_tr_b16 a[166:167], v19 offset:22528\n\t"
      "v_mfma_f32_16x16x32_bf16 v[120:123], a[184:187], a[216:219], v[120:123]\n\t"
      "ds_read_b64_tr_b16 a[168:169], v20 offset:18432\n\t"
      "v_mfma_f32_16x16x32_bf16 v[124:127], a[184:187], a[220:223], v[124:127]\n\t"
      "ds_read_b64_tr_b16 a[170:171], v20 offset:22528\n\t"
      "v_mfma_f32_16x16x32_bf16 v[220:223], a[188:191], a[220:223], v[220:223]\n\t"
      "s_waitcnt vmcnt(9)\n\t"
      "ds_read_b64_tr_b16 a[172:173], v21 offset:18432\n\t"
      "buffer_load_dwordx4 v1, s[36:39], s21 offen offset:1024 lds\n\t"
      "v_mfma_f32_16x16x32_bf16 v[216:219], a[188:191], a[216:219], v[216:219]\n\t"
      "s_waitcnt lgkmcnt(3)\n\t"
      "ds_read_b64_tr_b16 a[174:175], v21 offset:22528\n\t"
      "v_mfma_f32_16x16x32_bf16 v[56:59], a[160:163], a[232:235], v[56:59]\n\t"
      "ds_read_b64_tr_b16 a[176:177], v18 offset:16384\n\t"
      "v_mfma_f32_16x16x32_bf16 v[60:63], a[160:163], a[236:239], v[60:63]\n\t"
      "ds_read_b64_tr_b16 a[178:179], v18 offset:20480\n\t"
      "v_mfma_f32_16x16x32_bf16 v[92:95], a[164:167], a[236:239], v[92:95]\n\t"
      "ds_read_b64_tr_b16 a[180:181], v19 offset:16384\n\t"
      "buffer_load_dwordx4 v1, s[36:39], s22 offen offset:2048 lds\n\t"
      "v_mfma_f32_16x16x32_bf16 v[88:91], a[164:167], a[232:235], v[88:91]\n\t"
      "s_waitcnt lgkmcnt(3)\n\t"
      "ds_read_b64_tr_b16 a[182:183], v19 offset:20480\n\t"
      "v_mfma_f32_16x16x32_bf16 v[248:251], a[168:171], a[232:235], v[248:251]\n\t"
      "ds_read_b64_tr_b16 a[184:185], v20 offset:16384\n\t"
      "v_mfma_f32_16x16x32_bf16 v[252:255], a[168:171], a[236:239], v[252:255]\n\t"
      "ds_read_b64_tr_b16 a[186:187], v20 offset:20480\n\t"
      "v_mfma_f32_16x16x32_bf16 v[236:239], a[172:175], a[236:239], v[236:239]\n\t"
      "ds_read_b64_tr_b16 a[188:189], v21 offset:16384\n\t"
      "buffer_load_dwordx4 v1, s[36:39], s23 offen offset:3072 lds\n\t"
      "v_mfma_f32_16x16x32_bf16 v[232:235], a[172:175], a[232:235], v[232:235]\n\t"
      "s_waitcnt lgkmcnt(3)\n\t"
      "ds_read_b64_tr_b16 a[190:191], v21 offset:20480\n\t"
      "v_readlane_b32 s54, v23, 14\n\t"
      "v_mfma_f32_16x16x32_bf16 v[152:155], a[176:179], a[232:235], v[152:155]\n\t"
      "s_add_u32 s24, s24, 16384\n\t"
      "s_and_b32 s24, s24, 0x1ffff\n\t"
      "ds_read_b64_tr_b16 a[160:161], v18 offset:26624\n\t"
      "v_mfma_f32_16x16x32_bf16 v[156:159], a[176:179], a[236:239], v[156:159]\n\t"
      "s_bitcmp1_b32 s54, 0\n\t"
      "s_cselect_b64 s[36:37], s[80:81], s[84:85]\n\t"
      "ds_read_b64_tr_b16 a[162:163], v18 offset:30720\n\t"
      "v_readlane_b32 s53, v23, 45\n\t"
      "v_mfma_f32_16x16x32_bf16 v[188:191], a[180:183], a[236:239], v[188:191]\n\t"
      "s_cselect_b32 s38, s82, s86\n\t"
      "s_and_b32 s54, s54, -2\n\t"
      "ds_read_b64_tr_b16 a[164:165], v19 offset:26624\n\t"
      "v_mfma_f32_16x16x32_bf16 v[184:187], a[180:183], a[232:235], v[184:187]\n\t"
      "s_waitcnt lgkmcnt(3)\n\t"
      "ds_read_b64_tr_b16 a[166:167], v19 offset:30720\n\t"
      "v_add_u32 v1, s54, v17\n\t"
      "v_add_u32 v0, s53, v16\n\t"
      "v_mfma_f32_16x16x32_bf16 v[120:123], a[184:187], a[232:235], v[120:123]\n\t"
      "ds_read_b64_tr_b16 a[168:169], v20 offset:26624\n\t"
      "buffer_load_dwordx4 a[16:19], v0, s[76:79], s35 offen sc1\n\t"
      "v_mfma_f32_16x16x32_bf16 v[124:127], a[184:187], a[236:239], v[124:127]\n\t"
      "ds_read_b64_tr_b16 a[170:171], v20 offset:30720\n\t"
      "v_mfma_f32_16x16x32_bf16 v[220:223], a[188:191], a[236:239], v[220:223]\n\t"
      "s_waitcnt vmcnt(9)\n\t"
      "ds_read_b64_tr_b16 a[172:173], v21 offset:26624\n\t"
      "v_mfma_f32_16x16x32_bf16 v[216:219], a[188:191], a[232:235], v[216:219]\n\t"
      "s_waitcnt lgkmcnt(3)\n\t"
      "ds_read_b64_tr_b16 a[174:175], v21 offset:30720\n\t"
      "v_mfma_f32_16x16x32_bf16 v[56:59], a[160:163], a[248:251], v[56:59]\n\t"
      "ds_read_b64_tr_b16 a[176:177], v18 offset:24576\n\t"
      "buffer_load_dwordx4 a[20:23], v0, s[76:79], s35 offen offset:1024 sc1\n\t"
      "v_mfma_f32_16x16x32_bf16 v[60:63], a[160:163], a[252:255], v[60:63]\n\t"
      "ds_read_b64_tr_b16 a[178:179], v18 offset:28672\n\t"
      "v_mfma_f32_16x16x32_bf16 v[92:95], a[164:167], a[252:255], v[92:95]\n\t"
      "ds_read_b64_tr_b16 a[180:181], v19 offset:24576\n\t"
      "v_mfma_f32_16x16x32_bf16 v[88:91], a[164:167], a[248:251], v[88:91]\n\t"
      "s_waitcnt lgkmcnt(3)\n\t"
      "ds_read_b64_tr_b16 a[182:183], v19 offset:28672\n\t"
      "v_mfma_f32_16x16x32_bf16 v[248:251], a[168:171], a[248:251], v[248:251]\n\t"
      "ds_read_b64_tr_b16 a[184:185], v20 offset:24576\n\t"
      "buffer_load_dwordx4 a[24:27], v0, s[76:79], s71 offen sc1\n\t"
      "v_mfma_f32_16x16x32_bf16 v[252:255], a[168:171], a[252:255], v[252:255]\n\t"
      "ds_read_b64_tr_b16 a[186:187], v20 offset:28672\n\t"
      "v_mfma_f32_16x16x32_bf16 v[236:239], a[172:175], a[252:255], v[236:239]\n\t"
      "ds_read_b64_tr_b16 a[188:189], v21 offset:24576\n\t"
      "v_mfma_f32_16x16x32_bf16 v[232:235], a[172:175], a[248:251], v[232:235]\n\t"
      "s_waitcnt lgkmcnt(3)\n\t"
      "ds_read_b64_tr_b16 a[190:191], v21 offset:28672\n\t"
      "v_mfma_f32_16x16x32_bf16 v[152:155], a[176:179], a[248:251], v[152:155]\n\t"
      "s_waitcnt vmcnt(19)\n\t"
      "s_barrier\n\t"
      "ds_read_b64_tr_b16 a[160:161], v18 offset:34816\n\t"
      "buffer_load_dwordx4 a[28:31], v0, s[76:79], s71 offen offset:1024 sc1\n\t"
      "v_mfma_f32_16x16x32_bf16 v[156:159], a[176:179], a[252:255], v[156:159]\n\t"
      "s_mov_b32 m0, s24\n\t"
      "ds_read_b64_tr_b16 a[162:163], v18 offset:38912\n\t"
      "v_mfma_f32_16x16x32_bf16 v[188:191], a[180:183], a[252:255], v[188:191]\n\t"
      "ds_read_b64_tr_b16 a[164:165], v19 offset:34816\n\t"
      "buffer_load_dwordx4 v1, s[36:39], s20 offen lds\n\t"
      "v_mfma_f32_16x16x32_bf16 v[184:187], a[180:183], a[248:251], v[184:187]\n\t"
      "s_waitcnt lgkmcnt(3)\n\t"
      "ds_read_b64_tr_b16 a[166:167], v19 offset:38912\n\t"
      "v_mfma_f32_16x16x32_bf16 v[120:123], a[184:187], a[248:251], v[120:123]\n\t"
      "ds_read_b64_tr_b16 a[168:169], v20 offset:34816\n\t"
      "v_mfma_f32_16x16x32_bf16 v[124:127], a[184:187], a[252:255], v[124:127]\n\t"
      "ds_read_b64_tr_b16 a[170:171], v20 offset:38912\n\t"
      "v_mfma_f32_16x16x32_bf16 v[220:223], a[188:191], a[252:255], v[220:223]\n\t"
      "s_waitcnt vmcnt(11)\n\t"
      "ds_read_b64_tr_b16 a[172:173], v21 offset:34816\n\t"
      "buffer_load_dwordx4 v1, s[36:39], s21 offen offset:1024 lds\n\t"
      "v_mfma_f32_16x16x32_bf16 v[216:219], a[188:191], a[248:251], v[216:219]\n\t"
      "s_waitcnt lgkmcnt(3)\n\t"
      "ds_read_b64_tr_b16 a[174:175], v21 offset:38912\n\t"
      "v_mfma_f32_16x16x32_bf16 v[56:59], a[160:163], a[0:3], v[56:59]\n\t"
      "ds_read_b64_tr_b16 a[176:177], v18 offset:32768\n\t"
      "v_mfma_f32_16x16x32_bf16 v[60:63], a[160:163], a[4:7], v[60:63]\n\t"
      "ds_read_b64_tr_b16 a[178:179], v18 offset:36864\n\t"
      "v_mfma_f32_16x16x32_bf16 v[92:95], a[164:167], a[4:7], v[92:95]\n\t"
      "ds_read_b64_tr_b16 a[180:181], v19 offset:32768\n\t"
      "buffer_load_dwordx4 v1, s[36:39], s22 offen offset:2048 lds\n\t"
      "v_mfma_f32_16x16x32_bf16 v[88:91], a[164:167], a[0:3], v[88:91]\n\t"
      "s_waitcnt lgkmcnt(3)\n\t"
      "ds_read_b64_tr_b16 a[182:183], v19 offset:36864\n\t"
      "v_mfma_f32_16x16x32_bf16 v[248:251], a[168:171], a[0:3], v[248:251]\n\t"
      "ds_read_b64_tr_b16 a[184:185], v20 offset:32768\n\t"
      "v_mfma_f32_16x16x32_bf16 v[252:255], a[168:171], a[4:7], v[252:255]\n\t"
      "ds_read_b64_tr_b16 a[186:187], v20 offset:36864\n\t"
      "v_mfma_f32_16x16x32_bf16 v[236:239], a[172:175], a[4:7], v[236:239]\n\t"
      "ds_read_b64_tr_b16 a[188:189], v21 offset:32768\n\t"
      "buffer_load_dwordx4 v1, s[36:39], s23 offen offset:3072 lds\n\t"
      "v_mfma_f32_16x16x32_bf16 v[232:235], a[172:175], a[0:3], v[232:235]\n\t"
      "s_waitcnt lgkmcnt(3)\n\t"
      "ds_read_b64_tr_b16 a[190:191], v21 offset:36864\n\t"
      "v_readlane_b32 s54, v23, 15\n\t"
      "v_mfma_f32_16x16x32_bf16 v[152:155], a[176:179], a[0:3], v[152:155]\n\t"
      "s_add_u32 s24, s24, 16384\n\t"
      "s_and_b32 s24, s24, 0x1ffff\n\t"
      "ds_read_b64_tr_b16 a[160:161], v18 offset:43008\n\t"
      "v_mfma_f32_16x16x32_bf16 v[156:159], a[176:179], a[4:7], v[156:159]\n\t"
      "s_bitcmp1_b32 s54, 0\n\t"
      "s_cselect_b64 s[36:37], s[80:81], s[84:85]\n\t"
      "ds_read_b64_tr_b16 a[162:163], v18 offset:47104\n\t"
      "v_readlane_b32 s53, v23, 46\n\t"
      "v_mfma_f32_16x16x32_bf16 v[188:191], a[180:183], a[4:7], v[188:191]\n\t"
      "s_cselect_b32 s38, s82, s86\n\t"
      "s_and_b32 s54, s54, -2\n\t"
      "ds_read_b64_tr_b16 a[164:165], v19 offset:43008\n\t"
      "v_mfma_f32_16x16x32_bf16 v[184:187], a[180:183], a[0:3], v[184:187]\n\t"
      "s_waitcnt lgkmcnt(3)\n\t"
      "ds_read_b64_tr_b16 a[166:167], v19 offset:47104\n\t"
      "v_add_u32 v1, s54, v17\n\t"
      "v_add_u32 v0, s53, v16\n\t"
      "v_mfma_f32_16x16x32_bf16 v[120:123], a[184:187], a[0:3], v[120:123]\n\t"
      "ds_read_b64_tr_b16 a[168:169], v20 offset:43008\n\t"
      "buffer_load_dwordx4 a[200:203], v0, s[76:79], s35 offen sc1\n\t"
      "v_mfma_f32_16x16x32_bf16 v[124:127], a[184:187], a[4:7], v[124:127]\n\t"
      "ds_read_b64_tr_b16 a[170:171], v20 offset:47104\n\t"
      "v_mfma_f32_16x16x32_bf16 v[220:223], a[188:191], a[4:7], v[220:223]\n\t"
      "s_waitcnt vmcnt(13)\n\t"
      "ds_read_b64_tr_b16 a[172:173], v21 offset:43008\n\t"
      "v_mfma_f32_16x16x32_bf16 v[216:219], a[188:191], a[0:3], v[216:219]\n\t"
      "s_waitcnt lgkmcnt(3)\n\t"
      "ds_read_b64_tr_b16 a[174:175], v21 offset:47104\n\t"
      "v_mfma_f32_16x16x32_bf16 v[56:59], a[160:163], a[8:11], v[56:59]\n\t"
      "ds_read_b64_tr_b16 a[176:177], v18 offset:40960\n\t"
      "buffer_load_dwordx4 a[204:207], v0, s[76:79], s35 offen offset:1024 sc1\n\t"
      "v_mfma_f32_16x16x32_bf16 v[60:63], a[160:163], a[12:15], v[60:63]\n\t"
      "ds_read_b64_tr_b16 a[178:179], v18 offset:45056\n\t"
      "v_mfma_f32_16x16x32_bf16 v[92:95], a[164:167], a[12:15], v[92:95]\n\t"
      "ds_read_b64_tr_b16 a[180:181], v19 offset:40960\n\t"
      "v_mfma_f32_16x16x32_bf16 v[88:91], a[164:167], a[8:11], v[88:91]\n\t"
      "s_waitcnt lgkmcnt(3)\n\t"
      "ds_read_b64_tr_b16 a[182:183], v19 offset:45056\n\t"
      "v_mfma_f32_16x16x32_bf16 v[248:251], a[168:171], a[8:11], v[248:251]\n\t"
      "ds_read_b64_tr_b16 a[184:185], v20 offset:40960\n\t"
      "buffer_load_dwordx4 a[216:219], v0, s[76:79], s71 offen sc1\n\t"
      "v_mfma_f32_16x16x32_bf16 v[252:255], a[168:171], a[12:15], v[252:255]\n\t"
      "ds_read_b64_tr_b16 a[186:187], v20 offset:45056\n\t"
      "v_mfma_f32_16x16x32_bf16 v[236:239], a[172:175], a[12:15], v[236:239]\n\t"
      "ds_read_b64_tr_b16 a[188:189], v21 offset:40960\n\t"
      "v_mfma_f32_16x16x32_bf16 v[232:235], a[172:175], a[8:11], v[232:235]\n\t"
      "s_waitcnt lgkmcnt(3)\n\t"
      "ds_read_b64_tr_b16 a[190:191], v21 offset:45056\n\t"
      "v_mfma_f32_16x16x32_bf16 v[152:155], a[176:179], a[8:11], v[152:155]\n\t"
      "s_waitcnt vmcnt(11)\n\t"
      "s_barrier\n\t"
      "ds_read_b64_tr_b16 a[160:161], v18 offset:51200\n\t"
      "buffer_load_dwordx4 a[220:223], v0, s[76:79], s71 offen offset:1024 sc1\n\t"
      "v_mfma_f32_16x16x32_bf16 v[156:159], a[176:179], a[12:15], v[156:159]\n\t"
      "s_mov_b32 m0, s24\n\t"
      "ds_read_b64_tr_b16 a[162:163], v18 offset:55296\n\t"
      "v_mfma_f32_16x16x32_bf16 v[188:191], a[180:183], a[12:15], v[188:191]\n\t"
      "ds_read_b64_tr_b16 a[164:165], v19 offset:51200\n\t"
      "buffer_load_dwordx4 v1, s[36:39], s20 offen lds\n\t"
      "v_mfma_f32_16x16x32_bf16 v[184:187], a[180:183], a[8:11], v[184:187]\n\t"
      "s_waitcnt lgkmcnt(3)\n\t"
      "ds_read_b64_tr_b16 a[166:167], v19 offset:55296\n\t"
      "v_mfma_f32_16x16x32_bf16 v[120:123], a[184:187], a[8:11], v[120:123]\n\t"
      "ds_read_b64_tr_b16 a[168:169], v20 offset:51200\n\t"
      "v_mfma_f32_16x16x32_bf16 v[124:127], a[184:187], a[12:15], v[124:127]\n\t"
      "ds_read_b64_tr_b16 a[170:171], v20 offset:55296\n\t"
      "v_mfma_f32_16x16x32_bf16 v[220:223], a[188:191], a[12:15], v[220:223]\n\t"
      "s_waitcnt vmcnt(11)\n\t"
      "ds_read_b64_tr_b16 a[172:173], v21 offset:51200\n\t"
      "buffer_load_dwordx4 v1, s[36:39], s21 offen offset:1024 lds\n\t"
      "v_mfma_f32_16x16x32_bf16 v[216:219], a[188:191], a[8:11], v[216:219]\n\t"
      "s_waitcnt lgkmcnt(3)\n\t"
      "ds_read_b64_tr_b16 a[174:175], v21 offset:55296\n\t"
      "v_mfma_f32_16x16x32_bf16 v[56:59], a[160:163], a[16:19], v[56:59]\n\t"
      "ds_read_b64_tr_b16 a[176:177], v18 offset:49152\n\t"
      "v_mfma_f32_16x16x32_bf16 v[60:63], a[160:163], a[20:23], v[60:63]\n\t"
      "ds_read_b64_tr_b16 a[178:179], v18 offset:53248\n\t"
      "v_mfma_f32_16x16x32_bf16 v[92:95], a[164:167], a[20:23], v[92:95]\n\t"
      "ds_read_b64_tr_b16 a[180:181], v19 offset:49152\n\t"
      "buffer_load_dwordx4 v1, s[36:39], s22 offen offset:2048 lds\n\t"
      "v_mfma_f32_16x16x32_bf16 v[88:91], a[164:167], a[16:19], v[88:91]\n\t"
      "s_waitcnt lgkmcnt(3)\n\t"
      "ds_read_b64_tr_b16 a[182:183], v19 offset:53248\n\t"
      "v_mfma_f32_16x16x32_bf16 v[248:251], a[168:171], a[16:19], v[248:251]\n\t"
      "ds_read_b64_tr_b16 a[184:185], v20 offset:49152\n\t"
      "v_mfma_f32_16x16x32_bf16 v[252:255], a[168:171], a[20:23], v[252:255]\n\t"
      "ds_read_b64_tr_b16 a[186:187], v20 offset:53248\n\t"
      "v_mfma_f32_16x16x32_bf16 v[236:239], a[172:175], a[20:23], v[236:239]\n\t"
      "ds_read_b64_tr_b16 a[188:189], v21 offset:49152\n\t"
      "buffer_load_dwordx4 v1, s[36:39], s23 offen offset:3072 lds\n\t"
      "v_mfma_f32_16x16x32_bf16 v[232:235], a[172:175], a[16:19], v[232:235]\n\t"
      "s_waitcnt lgkmcnt(3)\n\t"
      "ds_read_b64_tr_b16 a[190:191], v21 offset:53248\n\t"
      "v_readlane_b32 s54, v23, 16\n\t"
      "v_mfma_f32_16x16x32_bf16 v[152:155], a[176:179], a[16:19], v[152:155]\n\t"
      "s_add_u32 s24, s24, 16384\n\t"
      "s_and_b32 s24, s24, 0x1ffff\n\t"
      "ds_read_b64_tr_b16 a[160:161], v18 offset:59392\n\t"
      "v_mfma_f32_16x16x32_bf16 v[156:159], a[176:179], a[20:23], v[156:159]\n\t"
      "s_bitcmp1_b32 s54, 0\n\t"
      "s_cselect_b64 s[36:37], s[80:81], s[84:85]\n\t"
      "ds_read_b64_tr_b16 a[162:163], v18 offset:63488\n\t"
      "v_readlane_b32 s53, v23, 47\n\t"
      "v_mfma_f32_16x16x32_bf16 v[188:191], a[180:183], a[20:23], v[188:191]\n\t"
      "s_cselect_b32 s38, s82, s86\n\t"
      "s_and_b32 s54, s54, -2\n\t"
      "ds_read_b64_tr_b16 a[164:165], v19 offset:59392\n\t"
      "v_mfma_f32_16x16x32_bf16 v[184:187], a[180:183], a[16:19], v[184:187]\n\t"
      "s_waitcnt lgkmcnt(3)\n\t"
      "ds_read_b64_tr_b16 a[166:167], v19 offset:63488\n\t"
      "v_add_u32 v1, s54, v17\n\t"
      "v_add_u32 v0, s53, v16\n\t"
      "v_mfma_f32_16x16x32_bf16 v[120:123], a[184:187], a[16:19], v[120:123]\n\t"
      "ds_read_b64_tr_b16 a[168:169], v20 offset:59392\n\t"
      "buffer_load_dwordx4 a[232:235], v0, s[76:79], s35 offen sc1\n\t"
      "v_mfma_f32_16x16x32_bf16 v[124:127], a[184:187], a[20:23], v[124:127]\n\t"
      "ds_read_b64_tr_b16 a[170:171], v20 offset:63488\n\t"
      "v_mfma_f32_16x16x32_bf16 v[220:223], a[188:191], a[20:23], v[220:223]\n\t"
      "s_waitcnt vmcnt(13)\n\t"
      "ds_read_b64_tr_b16 a[172:173], v21 offset:59392\n\t"
      "v_mfma_f32_16x16x32_bf16 v[216:219], a[188:191], a[16:19], v[216:219]\n\t"
      "s_waitcnt lgkmcnt(3)\n\t"
      "ds_read_b64_tr_b16 a[174:175], v21 offset:63488\n\t"
      "v_mfma_f32_16x16x32_bf16 v[56:59], a[160:163], a[24:27], v[56:59]\n\t"
      "ds_read_b64_tr_b16 a[176:177], v18 offset:57344\n\t"
      "buffer_load_dwordx4 a[236:239], v0, s[76:79], s35 offen offset:1024 sc1\n\t"
      "v_mfma_f32_16x16x32_bf16 v[60:63], a[160:163], a[28:31], v[60:63]\n\t"
      "ds_read_b64_tr_b16 a[178:179], v18 offset:61440\n\t"
      "v_mfma_f32_16x16x32_bf16 v[92:95], a[164:167], a[28:31], v[92:95]\n\t"
      "ds_read_b64_tr_b16 a[180:181], v19 offset:57344\n\t"
      "v_mfma_f32_16x16x32_bf16 v[88:91], a[164:167], a[24:27], v[88:91]\n\t"
      "s_waitcnt lgkmcnt(3)\n\t"
      "ds_read_b64_tr_b16 a[182:183], v19 offset:61440\n\t"
      "v_mfma_f32_16x16x32_bf16 v[248:251], a[168:171], a[24:27], v[248:251]\n\t"
      "ds_read_b64_tr_b16 a[184:185], v20 offset:57344\n\t"
      "buffer_load_dwordx4 a[248:251], v0, s[76:79], s71 offen sc1\n\t"
      "v_mfma_f32_16x16x32_bf16 v[252:255], a[168:171], a[28:31], v[252:255]\n\t"
      "ds_read_b64_tr_b16 a[186:187], v20 offset:61440\n\t"
      "v_mfma_f32_16x16x32_bf16 v[236:239], a[172:175], a[28:31], v[236:239]\n\t"
      "ds_read_b64_tr_b16 a[188:189], v21 offset:57344\n\t"
      "v_mfma_f32_16x16x32_bf16 v[232:235], a[172:175], a[24:27], v[232:235]\n\t"
      "s_waitcnt lgkmcnt(3)\n\t"
      "ds_read_b64_tr_b16 a[190:191], v21 offset:61440\n\t"
      "v_mfma_f32_16x16x32_bf16 v[152:155], a[176:179], a[24:27], v[152:155]\n\t"
      "s_waitcnt vmcnt(11)\n\t"
      "s_barrier\n\t"
      "ds_read_b64_tr_b16 a[160:161], v10 offset:2048\n\t"
      "buffer_load_dwordx4 a[252:255], v0, s[76:79], s71 offen offset:1024 sc1\n\t"
      "v_mfma_f32_16x16x32_bf16 v[156:159], a[176:179], a[28:31], v[156:159]\n\t"
      "s_mov_b32 m0, s24\n\t"
      "ds_read_b64_tr_b16 a[162:163], v10 offset:6144\n\t"
      "v_mfma_f32_16x16x32_bf16 v[188:191], a[180:183], a[28:31], v[188:191]\n\t"
      "ds_read_b64_tr_b16 a[164:165], v11 offset:2048\n\t"
      "buffer_load_dwordx4 v1, s[36:39], s20 offen lds\n\t"
      "v_mfma_f32_16x16x32_bf16 v[184:187], a[180:183], a[24:27], v[184:187]\n\t"
      "s_waitcnt lgkmcnt(3)\n\t"
      "ds_read_b64_tr_b16 a[166:167], v11 offset:6144\n\t"
      "v_mfma_f32_16x16x32_bf16 v[120:123], a[184:187], a[24:27], v[120:123]\n\t"
      "ds_read_b64_tr_b16 a[168:169], v14 offset:2048\n\t"
      "v_mfma_f32_16x16x32_bf16 v[124:127], a[184:187], a[28:31], v[124:127]\n\t"
      "ds_read_b64_tr_b16 a[170:171], v14 offset:6144\n\t"
      "v_mfma_f32_16x16x32_bf16 v[220:223], a[188:191], a[28:31], v[220:223]\n\t"
      "s_waitcnt vmcnt(11)\n\t"
      "ds_read_b64_tr_b16 a[172:173], v15 offset:2048\n\t"
      "buffer_load_dwordx4 v1, s[36:39], s21 offen offset:1024 lds\n\t"
      "v_mfma_f32_16x16x32_bf16 v[216:219], a[188:191], a[24:27], v[216:219]\n\t"
      "s_waitcnt lgkmcnt(3)\n\t"
      "ds_read_b64_tr_b16 a[174:175], v15 offset:6144\n\t"
      "v_mfma_f32_16x16x32_bf16 v[56:59], a[160:163], a[200:203], v[56:59]\n\t"
      "ds_read_b64_tr_b16 a[176:177], v10 offset:0\n\t"
      "v_mfma_f32_16x16x32_bf16 v[60:63], a[160:163], a[204:207], v[60:63]\n\t"
      "ds_read_b64_tr_b16 a[178:179], v10 offset:4096\n\t"
      "v_mfma_f32_16x16x32_bf16 v[92:95], a[164:167], a[204:207], v[92:95]\n\t"
      "ds_read_b64_tr_b16 a[180:181], v11 offset:0\n\t"
      "buffer_load_dwordx4 v1, s[36:39], s22 offen offset:2048 lds\n\t"
      "v_mfma_f32_16x16x32_bf16 v[88:91], a[164:167], a[200:203], v[88:91]\n\t"
      "s_waitcnt lgkmcnt(3)\n\t"
      "ds_read_b64_tr_b16 a[182:183], v11 offset:4096\n\t"
      "v_mfma_f32_16x16x32_bf16 v[248:251], a[168:171], a[200:203], v[248:251]\n\t"
      "ds_read_b64_tr_b16 a[184:185], v14 offset:0\n\t"
      "v_mfma_f32_16x16x32_bf16 v[252:255], a[168:171], a[204:207], v[252:255]\n\t"
      "ds_read_b64_tr_b16 a[186:187], v14 offset:4096\n\t"
      "v_mfma_f32_16x16x32_bf16 v[236:239], a[172:175], a[204:207], v[236:239]\n\t"
      "ds_read_b64_tr_b16 a[188:189], v15 offset:0\n\t"
      "buffer_load_dwordx4 v1, s[36:39], s23 offen offset:3072 lds\n\t"
      "v_mfma_f32_16x16x32_bf16 v[232:235], a[172:175], a[200:203], v[232:235]\n\t"
      "s_waitcnt lgkmcnt(3)\n\t"
      "ds_read_b64_tr_b16 a[190:191], v15 offset:4096\n\t"
      "v_readlane_b32 s54, v23, 17\n\t"
      "v_mfma_f32_16x16x32_bf16 v[152:155], a[176:179], a[200:203], v[152:155]\n\t"
      "s_add_u32 s24, s24, 16384\n\t"
      "s_and_b32 s24, s24, 0x1ffff\n\t"
      "ds_read_b64_tr_b16 a[160:161], v10 offset:10240\n\t"
      "v_mfma_f32_16x16x32_bf16 v[156:159], a[176:179], a[204:207], v[156:159]\n\t"
      "s_bitcmp1_b32 s54, 0\n\t"
      "s_cselect_b64 s[36:37], s[80:81], s[84:85]\n\t"
      "ds_read_b64_tr_b16 a[162:163], v10 offset:14336\n\t"
      "v_readlane_b32 s53, v23, 48\n\t"
      "v_mfma_f32_16x16x32_bf16 v[188:191], a[180:183], a[204:207], v[188:191]\n\t"
      "s_cselect_b32 s38, s82, s86\n\t"
      "s_and_b32 s54, s54, -2\n\t"
      "ds_read_b64_tr_b16 a[164:165], v11 offset:10240\n\t"
      "v_mfma_f32_16x16x32_bf16 v[184:187], a[180:183], a[200:203], v[184:187]\n\t"
      "s_waitcnt lgkmcnt(3)\n\t"
      "ds_read_b64_tr_b16 a[166:167], v11 offset:14336\n\t"
      "v_add_u32 v1, s54, v17\n\t"
      "v_add_u32 v0, s53, v16\n\t"
      "v_mfma_f32_16x16x32_bf16 v[120:123], a[184:187], a[200:203], v[120:123]\n\t"
      "ds_read_b64_tr_b16 a[168:169], v14 offset:10240\n\t"
      "buffer_load_dwordx4 a[0:3], v0, s[76:79], s35 offen sc1\n\t"
      "v_mfma_f32_16x16x32_bf16 v[124:127], a[184:187], a[204:207], v[124:127]\n\t"
      "ds_read_b64_tr_b16 a[170:171], v14 offset:14336\n\t"
      "v_mfma_f32_16x16x32_bf16 v[220:223], a[188:191], a[204:207], v[220:223]\n\t"
      "s_waitcnt vmcnt(13)\n\t"
      "ds_read_b64_tr_b16 a[172:173], v15 offset:10240\n\t"
      "v_mfma_f32_16x16x32_bf16 v[216:219], a[188:191], a[200:203], v[216:219]\n\t"
      "s_waitcnt lgkmcnt(3)\n\t"
      "ds_read_b64_tr_b16 a[174:175], v15 offset:14336\n\t"
      "v_mfma_f32_16x16x32_bf16 v[56:59], a[160:163], a[216:219], v[56:59]\n\t"
      "ds_read_b64_tr_b16 a[176:177], v10 offset:8192\n\t"
      "buffer_load_dwordx4 a[4:7], v0, s[76:79], s35 offen offset:1024 sc1\n\t"
      "v_mfma_f32_16x16x32_bf16 v[60:63], a[160:163], a[220:223], v[60:63]\n\t"
      "ds_read_b64_tr_b16 a[178:179], v10 offset:12288\n\t"
      "v_mfma_f32_16x16x32_bf16 v[92:95], a[164:167], a[220:223], v[92:95]\n\t"
      "ds_read_b64_tr_b16 a[180:181], v11 offset:8192\n\t"
      "v_mfma_f32_16x16x32_bf16 v[88:91], a[164:167], a[216:219], v[88:91]\n\t"
      "s_waitcnt lgkmcnt(3)\n\t"
      "ds_read_b64_tr_b16 a[182:183], v11 offset:12288\n\t"
      "v_mfma_f32_16x16x32_bf16 v[248:251], a[168:171], a[216:219], v[248:251]\n\t"
      "ds_read_b64_tr_b16 a[184:185], v14 offset:8192\n\t"
      "buffer_load_dwordx4 a[8:11], v0, s[76:79], s71 offen sc1\n\t"
      "v_mfma_f32_16x16x32_bf16 v[252:255], a[168:171], a[220:223], v[252:255]\n\t"
      "ds_read_b64_tr_b16 a[186:187], v14 offset:12288\n\t"
      "v_mfma_f32_16x16x32_bf16 v[236:239], a[172:175], a[220:223], v[236:239]\n\t"
      "ds_read_b64_tr_b16 a[188:189], v15 offset:8192\n\t"
      "v_mfma_f32_16x16x32_bf16 v[232:235], a[172:175], a[216:219], v[232:235]\n\t"
      "s_waitcnt lgkmcnt(3)\n\t"
      "ds_read_b64_tr_b16 a[190:191], v15 offset:12288\n\t"
      "v_mfma_f32_16x16x32_bf16 v[152:155], a[176:179], a[216:219], v[152:155]\n\t"
      "s_waitcnt vmcnt(11)\n\t"
      "s_barrier\n\t"
      "ds_read_b64_tr_b16 a[160:161], v10 offset:18432\n\t"
      "buffer_load_dwordx4 a[12:15], v0, s[76:79], s71 offen offset:1024 sc1\n\t"
      "v_mfma_f32_16x16x32_bf16 v[156:159], a[176:179], a[220:223], v[156:159]\n\t"
      "s_mov_b32 m0, s24\n\t"
      "ds_read_b64_tr_b16 a[162:163], v10 offset:22528\n\t"
      "v_mfma_f32_16x16x32_bf16 v[188:191], a[180:183], a[220:223], v[188:191]\n\t"
      "ds_read_b64_tr_b16 a[164:165], v11 offset:18432\n\t"
      "buffer_load_dwordx4 v1, s[36:39], s20 offen lds\n\t"
      "v_mfma_f32_16x16x32_bf16 v[184:187], a[180:183], a[216:219], v[184:187]\n\t"
      "s_waitcnt lgkmcnt(3)\n\t"
      "ds_read_b64_tr_b16 a[166:167], v11 offset:22528\n\t"
      "v_mfma_f32_16x16x32_bf16 v[120:123], a[184:187], a[216:219], v[120:123]\n\t"
      "ds_read_b64_tr_b16 a[168:169], v14 offset:18432\n\t"
      "v_mfma_f32_16x16x32_bf16 v[124:127], a[184:187], a[220:223], v[124:127]\n\t"
      "ds_read_b64_tr_b16 a[170:171], v14 offset:22528\n\t"
      "v_mfma_f32_16x16x32_bf16 v[220:223], a[188:191], a[220:223], v[220:223]\n\t"
      "s_waitcnt vmcnt(11)\n\t"
      "ds_read_b64_tr_b16 a[172:173], v15 offset:18432\n\t"
      "buffer_load_dwordx4 v1, s[36:39], s21 offen offset:1024 lds\n\t"
      "v_mfma_f32_16x16x32_bf16 v[216:219], a[188:191], a[216:219], v[216:219]\n\t"
      "s_waitcnt lgkmcnt(3)\n\t"
      "ds_read_b64_tr_b16 a[174:175], v15 offset:22528\n\t"
      "v_mfma_f32_16x16x32_bf16 v[56:59], a[160:163], a[232:235], v[56:59]\n\t"
      "ds_read_b64_tr_b16 a[176:177], v10 offset:16384\n\t"
      "v_mfma_f32_16x16x32_bf16 v[60:63], a[160:163], a[236:239], v[60:63]\n\t"
      "ds_read_b64_tr_b16 a[178:179], v10 offset:20480\n\t"
      "v_mfma_f32_16x16x32_bf16 v[92:95], a[164:167], a[236:239], v[92:95]\n\t"
      "ds_read_b64_tr_b16 a[180:181], v11 offset:16384\n\t"
      "buffer_load_dwordx4 v1, s[36:39], s22 offen offset:2048 lds\n\t"
      "v_mfma_f32_16x16x32_bf16 v[88:91], a[164:167], a[232:235], v[88:91]\n\t"
      "s_waitcnt lgkmcnt(3)\n\t"
      "ds_read_b64_tr_b16 a[182:183], v11 offset:20480\n\t"
      "v_mfma_f32_16x16x32_bf16 v[248:251], a[168:171], a[232:235], v[248:251]\n\t"
      "ds_read_b64_tr_b16 a[184:185], v14 offset:16384\n\t"
      "v_mfma_f32_16x16x32_bf16 v[252:255], a[168:171], a[236:239], v[252:255]\n\t"
      "ds_read_b64_tr_b16 a[186:187], v14 offset:20480\n\t"
      "v_mfma_f32_16x16x32_bf16 v[236:239], a[172:175], a[236:239], v[236:239]\n\t"
      "ds_read_b64_tr_b16 a[188:189], v15 offset:16384\n\t"
      "buffer_load_dwordx4 v1, s[36:39], s23 offen offset:3072 lds\n\t"
      "v_mfma_f32_16x16x32_bf16 v[232:235], a[172:175], a[232:235], v[232:235]\n\t"
      "s_waitcnt lgkmcnt(3)\n\t"
      "ds_read_b64_tr_b16 a[190:191], v15 offset:20480\n\t"
      "v_mfma_f32_16x16x32_bf16 v[152:155], a[176:179], a[232:235], v[152:155]\n\t"
      "ds_read_b64_tr_b16 a[160:161], v10 offset:26624\n\t"
      "v_mfma_f32_16x16x32_bf16 v[156:159], a[176:179], a[236:239], v[156:159]\n\t"
      "ds_read_b64_tr_b16 a[162:163], v10 offset:30720\n\t"
      "v_readlane_b32 s53, v23, 49\n\t"
      "v_mfma_f32_16x16x32_bf16 v[188:191], a[180:183], a[236:239], v[188:191]\n\t"
      "ds_read_b64_tr_b16 a[164:165], v11 offset:26624\n\t"
      "v_mfma_f32_16x16x32_bf16 v[184:187], a[180:183], a[232:235], v[184:187]\n\t"
      "s_waitcnt lgkmcnt(3)\n\t"
      "ds_read_b64_tr_b16 a[166:167], v11 offset:30720\n\t"
      "v_add_u32 v0, s53, v16\n\t"
      "v_mfma_f32_16x16x32_bf16 v[120:123], a[184:187], a[232:235], v[120:123]\n\t"
      "ds_read_b64_tr_b16 a[168:169], v14 offset:26624\n\t"
      "buffer_load_dwordx4 a[16:19], v0, s[76:79], s35 offen sc1\n\t"
      "v_mfma_f32_16x16x32_bf16 v[124:127], a[184:187], a[236:239], v[124:127]\n\t"
      "ds_read_b64_tr_b16 a[170:171], v14 offset:30720\n\t"
      "v_mfma_f32_16x16x32_bf16 v[220:223], a[188:191], a[236:239], v[220:223]\n\t"
      "s_waitcnt vmcnt(13)\n\t"
      "ds_read_b64_tr_b16 a[172:173], v15 offset:26624\n\t"
      "v_mfma_f32_16x16x32_bf16 v[216:219], a[188:191], a[232:235], v[216:219]\n\t"
      "s_waitcnt lgkmcnt(3)\n\t"
      "ds_read_b64_tr_b16 a[174:175], v15 offset:30720\n\t"
      "v_mfma_f32_16x16x32_bf16 v[56:59], a[160:163], a[248:251], v[56:59]\n\t"
      "ds_read_b64_tr_b16 a[176:177], v10 offset:24576\n\t"
      "buffer_load_dwordx4 a[20:23], v0, s[76:79], s35 offen offset:1024 sc1\n\t"
      "v_mfma_f32_16x16x32_bf16 v[60:63], a[160:163], a[252:255], v[60:63]\n\t"
      "ds_read_b64_tr_b16 a[178:179], v10 offset:28672\n\t"
      "v_mfma_f32_16x16x32_bf16 v[92:95], a[164:167], a[252:255], v[92:95]\n\t"
      "ds_read_b64_tr_b16 a[180:181], v11 offset:24576\n\t"
      "v_mfma_f32_16x16x32_bf16 v[88:91], a[164:167], a[248:251], v[88:91]\n\t"
      "s_waitcnt lgkmcnt(3)\n\t"
      "ds_read_b64_tr_b16 a[182:183], v11 offset:28672\n\t"
      "v_mfma_f32_16x16x32_bf16 v[248:251], a[168:171], a[248:251], v[248:251]\n\t"
      "ds_read_b64_tr_b16 a[184:185], v14 offset:24576\n\t"
      "buffer_load_dwordx4 a[24:27], v0, s[76:79], s71 offen sc1\n\t"
      "v_mfma_f32_16x16x32_bf16 v[252:255], a[168:171], a[252:255], v[252:255]\n\t"
      "ds_read_b64_tr_b16 a[186:187], v14 offset:28672\n\t"
      "v_mfma_f32_16x16x32_bf16 v[236:239], a[172:175], a[252:255], v[236:239]\n\t"
      "ds_read_b64_tr_b16 a[188:189], v15 offset:24576\n\t"
      "v_mfma_f32_16x16x32_bf16 v[232:235], a[172:175], a[248:251], v[232:235]\n\t"
      "s_waitcnt lgkmcnt(3)\n\t"
      "ds_read_b64_tr_b16 a[190:191], v15 offset:28672\n\t"
      "v_mfma_f32_16x16x32_bf16 v[152:155], a[176:179], a[248:251], v[152:155]\n\t"
      "s_waitcnt vmcnt(11)\n\t"
      "s_barrier\n\t"
      "ds_read_b64_tr_b16 a[160:161], v10 offset:34816\n\t"
      "buffer_load_dwordx4 a[28:31], v0, s[76:79], s71 offen offset:1024 sc1\n\t"
      "v_mfma_f32_16x16x32_bf16 v[156:159], a[176:179], a[252:255], v[156:159]\n\t"
      "ds_read_b64_tr_b16 a[162:163], v10 offset:38912\n\t"
      "v_mfma_f32_16x16x32_bf16 v[188:191], a[180:183], a[252:255], v[188:191]\n\t"
      "ds_read_b64_tr_b16 a[164:165], v11 offset:34816\n\t"
      "v_mfma_f32_16x16x32_bf16 v[184:187], a[180:183], a[248:251], v[184:187]\n\t"
      "s_waitcnt lgkmcnt(3)\n\t"
      "ds_read_b64_tr_b16 a[166:167], v11 offset:38912\n\t"
      "v_mfma_f32_16x16x32_bf16 v[120:123], a[184:187], a[248:251], v[120:123]\n\t"
      "ds_read_b64_tr_b16 a[168:169], v14 offset:34816\n\t"
      "v_mfma_f32_16x16x32_bf16 v[124:127], a[184:187], a[252:255], v[124:127]\n\t"
      "ds_read_b64_tr_b16 a[170:171], v14 offset:38912\n\t"
      "v_readlane_b32 s53, v23, 44\n\t"
      "v_mfma_f32_16x16x32_bf16 v[220:223], a[188:191], a[252:255], v[220:223]\n\t"
      "s_waitcnt vmcnt(10)\n\t"
      "ds_read_b64_tr_b16 a[172:173], v15 offset:34816\n\t"
      "v_mfma_f32_16x16x32_bf16 v[216:219], a[188:191], a[248:251], v[216:219]\n\t"
      "s_waitcnt lgkmcnt(3)\n\t"
      "ds_read_b64_tr_b16 a[174:175], v15 offset:38912\n\t"
      "v_add_u32 v0, s53, v16\n\t"
      "v_mfma_f32_16x16x32_bf16 v[56:59], a[160:163], a[0:3], v[56:59]\n\t"
      "ds_read_b64_tr_b16 a[176:177], v10 offset:32768\n\t"
      "buffer_load_dwordx4 a[64:67], v0, s[76:79], s34 offen sc1\n\t"
      "v_mfma_f32_16x16x32_bf16 v[60:63], a[160:163], a[4:7], v[60:63]\n\t"
      "ds_read_b64_tr_b16 a[178:179], v10 offset:36864\n\t"
      "v_mfma_f32_16x16x32_bf16 v[92:95], a[164:167], a[4:7], v[92:95]\n\t"
      "ds_read_b64_tr_b16 a[180:181], v11 offset:32768\n\t"
      "v_mfma_f32_16x16x32_bf16 v[88:91], a[164:167], a[0:3], v[88:91]\n\t"
      "s_waitcnt lgkmcnt(3)\n\t"
      "ds_read_b64_tr_b16 a[182:183], v11 offset:36864\n\t"
      "buffer_load_dwordx4 a[68:71], v0, s[76:79], s34 offen offset:1024 sc1\n\t"
      "v_mfma_f32_16x16x32_bf16 v[248:251], a[168:171], a[0:3], v[248:251]\n\t"
      "ds_read_b64_tr_b16 a[184:185], v14 offset:32768\n\t"
      "v_mfma_f32_16x16x32_bf16 v[252:255], a[168:171], a[4:7], v[252:255]\n\t"
      "ds_read_b64_tr_b16 a[186:187], v14 offset:36864\n\t"
      "v_mfma_f32_16x16x32_bf16 v[236:239], a[172:175], a[4:7], v[236:239]\n\t"
      "ds_read_b64_tr_b16 a[188:189], v15 offset:32768\n\t"
      "buffer_load_dwordx4 a[72:75], v0, s[76:79], s52 offen sc1\n\t"
      "v_mfma_f32_16x16x32_bf16 v[232:235], a[172:175], a[0:3], v[232:235]\n\t"
      "s_waitcnt lgkmcnt(3)\n\t"
      "ds_read_b64_tr_b16 a[190:191], v15 offset:36864\n\t"
      "v_mfma_f32_16x16x32_bf16 v[152:155], a[176:179], a[0:3], v[152:155]\n\t"
      "ds_read_b64_tr_b16 a[160:161], v10 offset:43008\n\t"
      "v_mfma_f32_16x16x32_bf16 v[156:159], a[176:179], a[4:7], v[156:159]\n\t"
      "ds_read_b64_tr_b16 a[162:163], v10 offset:47104\n\t"
      "buffer_load_dwordx4 a[76:79], v0, s[76:79], s52 offen offset:1024 sc1\n\t"
      "v_mfma_f32_16x16x32_bf16 v[188:191], a[180:183], a[4:7], v[188:191]\n\t"
      "ds_read_b64_tr_b16 a[164:165], v11 offset:43008\n\t"
      "v_mfma_f32_16x16x32_bf16 v[184:187], a[180:183], a[0:3], v[184:187]\n\t"
      "s_waitcnt lgkmcnt(3)\n\t"
      "ds_read_b64_tr_b16 a[166:167], v11 offset:47104\n\t"
      "v_readlane_b32 s53, v23, 45\n\t"
      "v_mfma_f32_16x16x32_bf16 v[120:123], a[184:187], a[0:3], v[120:123]\n\t"
      "ds_read_b64_tr_b16 a[168:169], v14 offset:43008\n\t"
      "v_mfma_f32_16x16x32_bf16 v[124:127], a[184:187], a[4:7], v[124:127]\n\t"
      "ds_read_b64_tr_b16 a[170:171], v14 offset:47104\n\t"
      "v_add_u32 v0, s53, v16\n\t"
      "v_mfma_f32_16x16x32_bf16 v[220:223], a[188:191], a[4:7], v[220:223]\n\t"
      "s_waitcnt vmcnt(12)\n\t"
      "ds_read_b64_tr_b16 a[172:173], v15 offset:43008\n\t"
      "buffer_load_dwordx4 a[80:83], v0, s[76:79], s34 offen sc1\n\t"
      "v_mfma_f32_16x16x32_bf16 v[216:219], a[188:191], a[0:3], v[216:219]\n\t"
      "s_waitcnt lgkmcnt(3)\n\t"
      "ds_read_b64_tr_b16 a[174:175], v15 offset:47104\n\t"
      "v_mfma_f32_16x16x32_bf16 v[56:59], a[160:163], a[8:11], v[56:59]\n\t"
      "ds_read_b64_tr_b16 a[176:177], v10 offset:40960\n\t"
      "v_mfma_f32_16x16x32_bf16 v[60:63], a[160:163], a[12:15], v[60:63]\n\t"
      "ds_read_b64_tr_b16 a[178:179], v10 offset:45056\n\t"
      "buffer_load_dwordx4 a[84:87], v0, s[76:79], s34 offen offset:1024 sc1\n\t"
      "v_mfma_f32_16x16x32_bf16 v[92:95], a[164:167], a[12:15], v[92:95]\n\t"
      "ds_read_b64_tr_b16 a[180:181], v11 offset:40960\n\t"
      "v_mfma_f32_16x16x32_bf16 v[88:91], a[164:167], a[8:11], v[88:91]\n\t"
      "s_waitcnt lgkmcnt(3)\n\t"
      "ds_read_b64_tr_b16 a[182:183], v11 offset:45056\n\t"
      "v_mfma_f32_16x16x32_bf16 v[248:251], a[168:171], a[8:11], v[248:251]\n\t"
      "ds_read_b64_tr_b16 a[184:185], v14 offset:40960\n\t"
      "buffer_load_dwordx4 a[88:91], v0, s[76:79], s52 offen sc1\n\t"
      "v_mfma_f32_16x16x32_bf16 v[252:255], a[168:171], a[12:15], v[252:255]\n\t"
      "ds_read_b64_tr_b16 a[186:187], v14 offset:45056\n\t"
      "v_mfma_f32_16x16x32_bf16 v[236:239], a[172:175], a[12:15], v[236:239]\n\t"
      "ds_read_b64_tr_b16 a[188:189], v15 offset:40960\n\t"
      "v_mfma_f32_16x16x32_bf16 v[232:235], a[172:175], a[8:11], v[232:235]\n\t"
      "s_waitcnt lgkmcnt(3)\n\t"
      "ds_read_b64_tr_b16 a[190:191], v15 offset:45056\n\t"
      "buffer_load_dwordx4 a[92:95], v0, s[76:79], s52 offen offset:1024 sc1\n\t"
      "v_mfma_f32_16x16x32_bf16 v[152:155], a[176:179], a[8:11], v[152:155]\n\t"
      "s_waitcnt vmcnt(12)\n\t"
      "s_barrier\n\t"
      "ds_read_b64_tr_b16 a[160:161], v10 offset:51200\n\t"
      "v_mfma_f32_16x16x32_bf16 v[156:159], a[176:179], a[12:15], v[156:159]\n\t"
      "ds_read_b64_tr_b16 a[162:163], v10 offset:55296\n\t"
      "v_readlane_b32 s53, v23, 46\n\t"
      "v_mfma_f32_16x16x32_bf16 v[188:191], a[180:183], a[12:15], v[188:191]\n\t"
      "ds_read_b64_tr_b16 a[164:165], v11 offset:51200\n\t"
      "v_mfma_f32_16x16x32_bf16 v[184:187], a[180:183], a[8:11], v[184:187]\n\t"
      "s_waitcnt lgkmcnt(3)\n\t"
      "ds_read_b64_tr_b16 a[166:167], v11 offset:55296\n\t"
      "v_add_u32 v0, s53, v16\n\t"
      "v_mfma_f32_16x16x32_bf16 v[120:123], a[184:187], a[8:11], v[120:123]\n\t"
      "ds_read_b64_tr_b16 a[168:169], v14 offset:51200\n\t"
      "buffer_load_dwordx4 a[96:99], v0, s[76:79], s34 offen sc1\n\t"
      "v_mfma_f32_16x16x32_bf16 v[124:127], a[184:187], a[12:15], v[124:127]\n\t"
      "ds_read_b64_tr_b16 a[170:171], v14 offset:55296\n\t"
      "v_mfma_f32_16x16x32_bf16 v[220:223], a[188:191], a[12:15], v[220:223]\n\t"
      "s_waitcnt vmcnt(11)\n\t"
      "ds_read_b64_tr_b16 a[172:173], v15 offset:51200\n\t"
      "v_mfma_f32_16x16x32_bf16 v[216:219], a[188:191], a[8:11], v[216:219]\n\t"
      "s_waitcnt lgkmcnt(3)\n\t"
      "ds_read_b64_tr_b16 a[174:175], v15 offset:55296\n\t"
      "buffer_load_dwordx4 a[100:103], v0, s[76:79], s34 offen offset:1024 sc1\n\t"
      "v_mfma_f32_16x16x32_bf16 v[56:59], a[160:163], a[16:19], v[56:59]\n\t"
      "ds_read_b64_tr_b16 a[176:177], v10 offset:49152\n\t"
      "v_mfma_f32_16x16x32_bf16 v[60:63], a[160:163], a[20:23], v[60:63]\n\t"
      "ds_read_b64_tr_b16 a[178:179], v10 offset:53248\n\t"
      "v_mfma_f32_16x16x32_bf16 v[92:95], a[164:167], a[20:23], v[92:95]\n\t"
      "ds_read_b64_tr_b16 a[180:181], v11 offset:49152\n\t"
      "buffer_load_dwordx4 a[104:107], v0, s[76:79], s52 offen sc1\n\t"
      "v_mfma_f32_16x16x32_bf16 v[88:91], a[164:167], a[16:19], v[88:91]\n\t"
      "s_waitcnt lgkmcnt(3)\n\t"
      "ds_read_b64_tr_b16 a[182:183], v11 offset:53248\n\t"
      "v_mfma_f32_16x16x32_bf16 v[248:251], a[168:171], a[16:19], v[248:251]\n\t"
      "ds_read_b64_tr_b16 a[184:185], v14 offset:49152\n\t"
      "v_mfma_f32_16x16x32_bf16 v[252:255], a[168:171], a[20:23], v[252:255]\n\t"
      "ds_read_b64_tr_b16 a[186:187], v14 offset:53248\n\t"
      "buffer_load_dwordx4 a[108:111], v0, s[76:79], s52 offen offset:1024 sc1\n\t"
      "v_mfma_f32_16x16x32_bf16 v[236:239], a[172:175], a[20:23], v[236:239]\n\t"
      "ds_read_b64_tr_b16 a[188:189], v15 offset:49152\n\t"
      "v_mfma_f32_16x16x32_bf16 v[232:235], a[172:175], a[16:19], v[232:235]\n\t"
      "s_waitcnt lgkmcnt(3)\n\t"
      "ds_read_b64_tr_b16 a[190:191], v15 offset:53248\n\t"
      "v_readlane_b32 s53, v23, 47\n\t"
      "v_mfma_f32_16x16x32_bf16 v[152:155], a[176:179], a[16:19], v[152:155]\n\t"
      "ds_read_b64_tr_b16 a[160:161], v10 offset:59392\n\t"
      "v_mfma_f32_16x16x32_bf16 v[156:159], a[176:179], a[20:23], v[156:159]\n\t"
      "ds_read_b64_tr_b16 a[162:163], v10 offset:63488\n\t"
      "v_add_u32 v0, s53, v16\n\t"
      "v_mfma_f32_16x16x32_bf16 v[188:191], a[180:183], a[20:23], v[188:191]\n\t"
      "ds_read_b64_tr_b16 a[164:165], v11 offset:59392\n\t"
      "buffer_load_dwordx4 a[112:115], v0, s[76:79], s34 offen sc1\n\t"
      "v_mfma_f32_16x16x32_bf16 v[184:187], a[180:183], a[16:19], v[184:187]\n\t"
      "s_waitcnt lgkmcnt(3)\n\t"
      "ds_read_b64_tr_b16 a[166:167], v11 offset:63488\n\t"
      "v_mfma_f32_16x16x32_bf16 v[120:123], a[184:187], a[16:19], v[120:123]\n\t"
      "ds_read_b64_tr_b16 a[168:169], v14 offset:59392\n\t"
      "v_mfma_f32_16x16x32_bf16 v[124:127], a[184:187], a[20:23], v[124:127]\n\t"
      "ds_read_b64_tr_b16 a[170:171], v14 offset:63488\n\t"
      "buffer_load_dwordx4 a[116:119], v0, s[76:79], s34 offen offset:1024 sc1\n\t"
      "v_mfma_f32_16x16x32_bf16 v[220:223], a[188:191], a[20:23], v[220:223]\n\t"
      "s_waitcnt vmcnt(14)\n\t"
      "ds_read_b64_tr_b16 a[172:173], v15 offset:59392\n\t"
      "v_mfma_f32_16x16x32_bf16 v[216:219], a[188:191], a[16:19], v[216:219]\n\t"
      "s_waitcnt lgkmcnt(3)\n\t"
      "ds_read_b64_tr_b16 a[174:175], v15 offset:63488\n\t"
      "v_mfma_f32_16x16x32_bf16 v[56:59], a[160:163], a[24:27], v[56:59]\n\t"
      "ds_read_b64_tr_b16 a[176:177], v10 offset:57344\n\t"
      "buffer_load_dwordx4 a[120:123], v0, s[76:79], s52 offen sc1\n\t"
      "v_mfma_f32_16x16x32_bf16 v[60:63], a[160:163], a[28:31], v[60:63]\n\t"
      "ds_read_b64_tr_b16 a[178:179], v10 offset:61440\n\t"
      "v_mfma_f32_16x16x32_bf16 v[92:95], a[164:167], a[28:31], v[92:95]\n\t"
      "ds_read_b64_tr_b16 a[180:181], v11 offset:57344\n\t"
      "v_mfma_f32_16x16x32_bf16 v[88:91], a[164:167], a[24:27], v[88:91]\n\t"
      "s_waitcnt lgkmcnt(3)\n\t"
      "ds_read_b64_tr_b16 a[182:183], v11 offset:61440\n\t"
      "buffer_load_dwordx4 a[124:127], v0, s[76:79], s52 offen offset:1024 sc1\n\t"
      "v_mfma_f32_16x16x32_bf16 v[248:251], a[168:171], a[24:27], v[248:251]\n\t"
      "ds_read_b64_tr_b16 a[184:185], v14 offset:57344\n\t"
      "v_mfma_f32_16x16x32_bf16 v[252:255], a[168:171], a[28:31], v[252:255]\n\t"
      "ds_read_b64_tr_b16 a[186:187], v14 offset:61440\n\t"
      "v_readlane_b32 s53, v23, 48\n\t"
      "v_mfma_f32_16x16x32_bf16 v[236:239], a[172:175], a[28:31], v[236:239]\n\t"
      "ds_read_b64_tr_b16 a[188:189], v15 offset:57344\n\t"
      "v_mfma_f32_16x16x32_bf16 v[232:235], a[172:175], a[24:27], v[232:235]\n\t"
      "s_waitcnt lgkmcnt(3)\n\t"
      "ds_read_b64_tr_b16 a[190:191], v15 offset:61440\n\t"
      "v_add_u32 v0, s53, v16\n\t"
      "v_mfma_f32_16x16x32_bf16 v[152:155], a[176:179], a[24:27], v[152:155]\n\t"
      "ds_read_b64_tr_b16 a[160:161], v18 offset:10240\n\t"
      "buffer_load_dwordx4 a[128:131], v0, s[76:79], s34 offen sc1\n\t"
      "v_mfma_f32_16x16x32_bf16 v[156:159], a[176:179], a[28:31], v[156:159]\n\t"
      "ds_read_b64_tr_b16 a[162:163], v18 offset:14336\n\t"
      "v_mfma_f32_16x16x32_bf16 v[188:191], a[180:183], a[28:31], v[188:191]\n\t"
      "ds_read_b64_tr_b16 a[164:165], v19 offset:10240\n\t"
      "v_mfma_f32_16x16x32_bf16 v[184:187], a[180:183], a[24:27], v[184:187]\n\t"
      "s_waitcnt lgkmcnt(3)\n\t"
      "ds_read_b64_tr_b16 a[166:167], v19 offset:14336\n\t"
      "buffer_load_dwordx4 a[132:135], v0, s[76:79], s34 offen offset:1024 sc1\n\t"
      "v_mfma_f32_16x16x32_bf16 v[120:123], a[184:187], a[24:27], v[120:123]\n\t"
      "ds_read_b64_tr_b16 a[168:169], v20 offset:10240\n\t"
      "v_mfma_f32_16x16x32_bf16 v[124:127], a[184:187], a[28:31], v[124:127]\n\t"
      "ds_read_b64_tr_b16 a[170:171], v20 offset:14336\n\t"
      "v_mfma_f32_16x16x32_bf16 v[220:223], a[188:191], a[28:31], v[220:223]\n\t"
      "ds_read_b64_tr_b16 a[172:173], v21 offset:10240\n\t"
      "buffer_load_dwordx4 a[136:139], v0, s[76:79], s52 offen sc1\n\t"
      "v_mfma_f32_16x16x32_bf16 v[216:219], a[188:191], a[24:27], v[216:219]\n\t"
      "s_waitcnt lgkmcnt(3)\n\t"
      "ds_read_b64_tr_b16 a[174:175], v21 offset:14336\n\t"
      "v_mfma_f32_16x16x32_bf16 v[48:51], a[160:163], a[208:211], v[48:51]\n\t"
      "ds_read_b64_tr_b16 a[176:177], v18 offset:18432\n\t"
      "v_mfma_f32_16x16x32_bf16 v[52:55], a[160:163], a[212:215], v[52:55]\n\t"
      "ds_read_b64_tr_b16 a[178:179], v18 offset:22528\n\t"
      "buffer_load_dwordx4 a[140:143], v0, s[76:79], s52 offen offset:1024 sc1\n\t"
      "v_mfma_f32_16x16x32_bf16 v[84:87], a[164:167], a[212:215], v[84:87]\n\t"
      "ds_read_b64_tr_b16 a[180:181], v19 offset:18432\n\t"
      "v_cvt_pk_bf16_f32 v56, v56, v57\n\t"
      "v_cvt_pk_bf16_f32 v57, v58, v59\n\t"
      STAMP(36, 37)
      "v_mfma_f32_16x16x32_bf16 v[80:83], a[164:167], a[208:211], v[80:83]\n\t"
      "s_waitcnt lgkmcnt(3)\n\t"
      "ds_read_b64_tr_b16 a[182:183], v19 offset:22528\n\t"
      "v_readlane_b32 s53, v23, 49\n\t"
      "v_cvt_pk_bf16_f32 v58, v88, v89\n\t"
      "v_mfma_f32_16x16x32_bf16 v[240:243], a[168:171], a[208:211], v[240:243]\n\t"
      "ds_read_b64_tr_b16 a[184:185], v20 offset:18432\n\t"
      "v_cvt_pk_bf16_f32 v59, v90, v91\n\t"
      "buffer_store_dwordx4 v[56:59], v40, s[60:63], s69 offen offset:0 sc1\n\t"
      "v_mfma_f32_16x16x32_bf16 v[244:247], a[168:171], a[212:215], v[244:247]\n\t"
      "ds_read_b64_tr_b16 a[186:187], v20 offset:22528\n\t"
      "v_add_u32 v0, s53, v16\n\t"
      "v_mfma_f32_16x16x32_bf16 v[228:231], a[172:175], a[212:215], v[228:231]\n\t"
      "ds_read_b64_tr_b16 a[188:189], v21 offset:18432\n\t"
      "buffer_load_dwordx4 a[144:147], v0, s[76:79], s34 offen sc1\n\t"
      "v_mfma_f32_16x16x32_bf16 v[224:227], a[172:175], a[208:211], v[224:227]\n\t"
      "s_waitcnt lgkmcnt(3)\n\t"
      "ds_read_b64_tr_b16 a[190:191], v21 offset:22528\n\t"
      "v_mfma_f32_16x16x32_bf16 v[48:51], a[176:179], a[224:227], v[48:51]\n\t"
      "ds_read_b64_tr_b16 a[160:161], v18 offset:26624\n\t"
      "v_cvt_pk_bf16_f32 v248, v248, v249\n\t"
      "v_cvt_pk_bf16_f32 v249, v250, v251\n\t"
      "v_mfma_f32_16x16x32_bf16 v[52:55], a[176:179], a[228:231], v[52:55]\n\t"
      LSTW(36, "")
      "ds_read_b64_tr_b16 a[162:163], v18 offset:30720\n\t"
      "buffer_load_dwordx4 a[148:151], v0, s[76:79], s34 offen offset:1024 sc1\n\t"
      "v_cvt_pk_bf16_f32 v250, v232, v233\n\t"
      "v_mfma_f32_16x16x32_bf16 v[84:87], a[180:183], a[228:231], v[84:87]\n\t"
      "ds_read_b64_tr_b16 a[164:165], v19 offset:26624\n\t"
      "v_cvt_pk_bf16_f32 v251, v234, v235\n\t"
      "buffer_store_dwordx4 v[248:251], v40, s[60:63], s69 offen offset:1024 sc1\n\t"
      "v_mfma_f32_16x16x32_bf16 v[80:83], a[180:183], a[224:227], v[80:83]\n\t"
      "s_waitcnt lgkmcnt(3)\n\t"
      "ds_read_b64_tr_b16 a[166:167], v19 offset:30720\n\t"
      "v_mfma_f32_16x16x32_bf16 v[240:243], a[184:187], a[224:227], v[240:243]\n\t"
      "ds_read_b64_tr_b16 a[168:169], v20 offset:26624\n\t"
      "buffer_load_dwordx4 a[152:155], v0, s[76:79], s52 offen sc1\n\t"
      "v_mfma_f32_16x16x32_bf16 v[244:247], a[184:187], a[228:231], v[244:247]\n\t"
      "ds_read_b64_tr_b16 a[170:171], v20 offset:30720\n\t"
      "v_mfma_f32_16x16x32_bf16 v[228:231], a[188:191], a[228:231], v[228:231]\n\t"
      "ds_read_b64_tr_b16 a[172:173], v21 offset:26624\n\t"
      "v_cvt_pk_bf16_f32 v60, v60, v61\n\t"
      "v_cvt_pk_bf16_f32 v61, v62, v63\n\t"
      "v_mfma_f32_16x16x32_bf16 v[224:227], a[188:191], a[224:227], v[224:227]\n\t"
      "s_waitcnt lgkmcnt(3)\n\t"
      "ds_read_b64_tr_b16 a[174:175], v21 offset:30720\n\t"
      "buffer_load_dwordx4 a[156:159], v0, s[76:79], s52 offen offset:1024 sc1\n\t"
      "v_cvt_pk_bf16_f32 v62, v92, v93\n\t"
      "v_mfma_f32_16x16x32_bf16 v[48:51], a[160:163], a[240:243], v[48:51]\n\t"
      "ds_read_b64_tr_b16 a[176:177], v18 offset:34816\n\t"
      "v_cvt_pk_bf16_f32 v63, v94, v95\n\t"
      "buffer_store_dwordx4 v[60:63], v40, s[60:63], s69 offen offset:2048 sc1\n\t"
      "v_mfma_f32_16x16x32_bf16 v[52:55], a[160:163], a[244:247], v[52:55]\n\t"
      "ds_read_b64_tr_b16 a[178:179], v18 offset:38912\n\t"
      "v_mfma_f32_16x16x32_bf16 v[84:87], a[164:167], a[244:247], v[84:87]\n\t"
      "ds_read_b64_tr_b16 a[180:181], v19 offset:34816\n\t"
      "v_mfma_f32_16x16x32_bf16 v[80:83], a[164:167], a[240:243], v[80:83]\n\t"
      "s_waitcnt lgkmcnt(3)\n\t"
      "ds_read_b64_tr_b16 a[182:183], v19 offset:38912\n\t"
      "v_mfma_f32_16x16x32_bf16 v[240:243], a[168:171], a[240:243], v[240:243]\n\t"
      "ds_read_b64_tr_b16 a[184:185], v20 offset:34816\n\t"
      "v_cvt_pk_bf16_f32 v252, v252, v253\n\t"
      "v_cvt_pk_bf16_f32 v253, v254, v255\n\t"
      "v_mfma_f32_16x16x32_bf16 v[244:247], a[168:171], a[244:247], v[244:247]\n\t"
      "ds_read_b64_tr_b16 a[186:187], v20 offset:38912\n\t"
      "v_cvt_pk_bf16_f32 v254, v236, v237\n\t"
      "v_cvt_pk_bf16_f32 v255, v238, v239\n\t"
      "v_mfma_f32_16x16x32_bf16 v[228:231], a[172:175], a[244:247], v[228:231]\n\t"
      "s_waitcnt vmcnt(25)\n\t"
      "ds_read_b64_tr_b16 a[188:189], v21 offset:34816\n\t"
      "buffer_store_dwordx4 v[252:255], v40, s[60:63], s69 offen offset:3072 sc1\n\t"
      "v_mfma_f32_16x16x32_bf16 v[224:227], a[172:175], a[240:243], v[224:227]\n\t"
      "s_waitcnt lgkmcnt(3)\n\t"
      "ds_read_b64_tr_b16 a[190:191], v21 offset:38912\n\t"
      "v_mfma_f32_16x16x32_bf16 v[48:51], a[176:179], a[64:67], v[48:51]\n\t"
      "ds_read_b64_tr_b16 a[160:161], v18 offset:43008\n\t"
      "v_mfma_f32_16x16x32_bf16 v[52:55], a[176:179], a[68:71], v[52:55]\n\t"
      "ds_read_b64_tr_b16 a[162:163], v18 offset:47104\n\t"
      "v_mfma_f32_16x16x32_bf16 v[84:87], a[180:183], a[68:71], v[84:87]\n\t"
      "ds_read_b64_tr_b16 a[164:165], v19 offset:43008\n\t"
      "v_cvt_pk_bf16_f32 v152, v152, v153\n\t"
      "v_cvt_pk_bf16_f32 v153, v154, v155\n\t"
      "v_mfma_f32_16x16x32_bf16 v[80:83], a[180:183], a[64:67], v[80:83]\n\t"
      "s_waitcnt lgkmcnt(3)\n\t"
      "ds_read_b64_tr_b16 a[166:167], v19 offset:47104\n\t"
      "v_cvt_pk_bf16_f32 v154, v184, v185\n\t"
      "v_cvt_pk_bf16_f32 v155, v186, v187\n\t"
      "v_mfma_f32_16x16x32_bf16 v[240:243], a[184:187], a[64:67], v[240:243]\n\t"
      "ds_read_b64_tr_b16 a[168:169], v20 offset:43008\n\t"
      "buffer_store_dwordx4 v[152:155], v40, s[60:63], s58 offen offset:0 sc1\n\t"
      "v_mfma_f32_16x16x32_bf16 v[244:247], a[184:187], a[68:71], v[244:247]\n\t"
      "ds_read_b64_tr_b16 a[170:171], v20 offset:47104\n\t"
      "v_mfma_f32_16x16x32_bf16 v[228:231], a[188:191], a[68:71], v[228:231]\n\t"
      "s_waitcnt vmcnt(25)\n\t"
      "ds_read_b64_tr_b16 a[172:173], v21 offset:43008\n\t"
      "v_mfma_f32_16x16x32_bf16 v[224:227], a[188:191], a[64:67], v[224:227]\n\t"
      "s_waitcnt lgkmcnt(3)\n\t"
      "ds_read_b64_tr_b16 a[174:175], v21 offset:47104\n\t"
      "v_mfma_f32_16x16x32_bf16 v[48:51], a[160:163], a[72:75], v[48:51]\n\t"
      "ds_read_b64_tr_b16 a[176:177], v18 offset:51200\n\t"
      "v_cvt_pk_bf16_f32 v120, v120, v121\n\t"
      "v_cvt_pk_bf16_f32 v121, v122, v123\n\t"
      "v_mfma_f32_16x16x32_bf16 v[52:55], a[160:163], a[76:79], v[52:55]\n\t"
      "ds_read_b64_tr_b16 a[178:179], v18 offset:55296\n\t"
      "v_cvt_pk_bf16_f32 v122, v216, v217\n\t"
      "v_cvt_pk_bf16_f32 v123, v218, v219\n\t"
      "v_mfma_f32_16x16x32_bf16 v[84:87], a[164:167], a[76:79], v[84:87]\n\t"
      "ds_read_b64_tr_b16 a[180:181], v19 offset:51200\n\t"
      "buffer_store_dwordx4 v[120:123], v40, s[60:63], s58 offen offset:1024 sc1\n\t"
      "v_mfma_f32_16x16x32_bf16 v[80:83], a[164:167], a[72:75], v[80:83]\n\t"
      "s_waitcnt lgkmcnt(3)\n\t"
      "ds_read_b64_tr_b16 a[182:183], v19 offset:55296\n\t"
      "v_mfma_f32_16x16x32_bf16 v[240:243], a[168:171], a[72:75], v[240:243]\n\t"
      "ds_read_b64_tr_b16 a[184:185], v20 offset:51200\n\t"
      "v_mfma_f32_16x16x32_bf16 v[244:247], a[168:171], a[76:79], v[244:247]\n\t"
      "ds_read_b64_tr_b16 a[186:187], v20 offset:55296\n\t"
      "v_mfma_f32_16x16x32_bf16 v[228:231], a[172:175], a[76:79], v[228:231]\n\t"
      "s_waitcnt vmcnt(24)\n\t"
      "ds_read_b64_tr_b16 a[188:189], v21 offset:51200\n\t"
      "v_cvt_pk_bf16_f32 v156, v156, v157\n\t"
      "v_cvt_pk_bf16_f32 v157, v158, v159\n\t"
      "v_mfma_f32_16x16x32_bf16 v[224:227], a[172:175], a[72:75], v[224:227]\n\t"
      "s_waitcnt lgkmcnt(3)\n\t"
      "ds_read_b64_tr_b16 a[190:191], v21 offset:55296\n\t"
      "v_cvt_pk_bf16_f32 v158, v188, v189\n\t"
      "v_cvt_pk_bf16_f32 v159, v190, v191\n\t"
      "v_mfma_f32_16x16x32_bf16 v[48:51], a[176:179], a[80:83], v[48:51]\n\t"
      "ds_read_b64_tr_b16 a[160:161], v18 offset:59392\n\t"
      "buffer_store_dwordx4 v[156:159], v40, s[60:63], s58 offen offset:2048 sc1\n\t"
      "v_mfma_f32_16x16x32_bf16 v[52:55], a[176:179], a[84:87], v[52:55]\n\t"
      "ds_read_b64_tr_b16 a[162:163], v18 offset:63488\n\t"
      "v_mfma_f32_16x16x32_bf16 v[84:87], a[180:183], a[84:87], v[84:87]\n\t"
      "ds_read_b64_tr_b16 a[164:165], v19 offset:59392\n\t"
      "v_mfma_f32_16x16x32_bf16 v[80:83], a[180:183], a[80:83], v[80:83]\n\t"
      "s_waitcnt lgkmcnt(3)\n\t"
      "ds_read_b64_tr_b16 a[166:167], v19 offset:63488\n\t"
      "v_mfma_f32_16x16x32_bf16 v[240:243], a[184:187], a[80:83], v[240:243]\n\t"
      "ds_read_b64_tr_b16 a[168:169], v20 offset:59392\n\t"
      "v_cvt_pk_bf16_f32 v124, v124, v125\n\t"
      "v_cvt_pk_bf16_f32 v125, v126, v127\n\t"
      "v_mfma_f32_16x16x32_bf16 v[244:247], a[184:187], a[84:87], v[244:247]\n\t"
      "ds_read_b64_tr_b16 a[170:171], v20 offset:63488\n\t"
      "v_cvt_pk_bf16_f32 v126, v220, v221\n\t"
      "v_cvt_pk_bf16_f32 v127, v222, v223\n\t"
      "v_mfma_f32_16x16x32_bf16 v[228:231], a[188:191], a[84:87], v[228:231]\n\t"
      "s_waitcnt vmcnt(23)\n\t"
      "ds_read_b64_tr_b16 a[172:173], v21 offset:59392\n\t"
      "buffer_store_dwordx4 v[124:127], v40, s[60:63], s58 offen offset:3072 sc1\n\t"
      "v_mfma_f32_16x16x32_bf16 v[224:227], a[188:191], a[80:83], v[224:227]\n\t"
      "s_waitcnt lgkmcnt(3)\n\t"
      "ds_read_b64_tr_b16 a[174:175], v21 offset:63488\n\t"
      "v_mfma_f32_16x16x32_bf16 v[48:51], a[160:163], a[88:91], v[48:51]\n\t"
      "ds_read_b64_tr_b16 a[176:177], v10 offset:2048\n\t"
      "v_mfma_f32_16x16x32_bf16 v[52:55], a[160:163], a[92:95], v[52:55]\n\t"
      "ds_read_b64_tr_b16 a[178:179], v10 offset:6144\n\t"
      "v_mfma_f32_16x16x32_bf16 v[84:87], a[164:167], a[92:95], v[84:87]\n\t"
      "ds_read_b64_tr_b16 a[180:181], v11 offset:2048\n\t"
      "v_mfma_f32_16x16x32_bf16 v[80:83], a[164:167], a[88:91], v[80:83]\n\t"
      "s_waitcnt lgkmcnt(3)\n\t"
      "ds_read_b64_tr_b16 a[182:183], v11 offset:6144\n\t"
      "v_mfma_f32_16x16x32_bf16 v[240:243], a[168:171], a[88:91], v[240:243]\n\t"
      "ds_read_b64_tr_b16 a[184:185], v14 offset:2048\n\t"
      "v_mfma_f32_16x16x32_bf16 v[244:247], a[168:171], a[92:95], v[244:247]\n\t"
      "ds_read_b64_tr_b16 a[186:187], v14 offset:6144\n\t"
      "v_mfma_f32_16x16x32_bf16 v[228:231], a[172:175], a[92:95], v[228:231]\n\t"
      "s_waitcnt vmcnt(22)\n\t"
      "ds_read_b64_tr_b16 a[188:189], v15 offset:2048\n\t"
      "v_mfma_f32_16x16x32_bf16 v[224:227], a[172:175], a[88:91], v[224:227]\n\t"
      "s_waitcnt lgkmcnt(3)\n\t"
      "ds_read_b64_tr_b16 a[190:191], v15 offset:6144\n\t"
      "v_mfma_f32_16x16x32_bf16 v[48:51], a[176:179], a[96:99], v[48:51]\n\t"
      "ds_read_b64_tr_b16 a[160:161], v10 offset:10240\n\t"
      "v_mfma_f32_16x16x32_bf16 v[52:55], a[176:179], a[100:103], v[52:55]\n\t"
      "ds_read_b64_tr_b16 a[162:163], v10 offset:14336\n\t"
      "v_mfma_f32_16x16x32_bf16 v[84:87], a[180:183], a[100:103], v[84:87]\n\t"
      "ds_read_b64_tr_b16 a[164:165], v11 offset:10240\n\t"
      "v_mfma_f32_16x16x32_bf16 v[80:83], a[180:183], a[96:99], v[80:83]\n\t"
      "s_waitcnt lgkmcnt(3)\n\t"
      "ds_read_b64_tr_b16 a[166:167], v11 offset:14336\n\t"
      "v_mfma_f32_16x16x32_bf16 v[240:243], a[184:187], a[96:99], v[240:243]\n\t"
      "ds_read_b64_tr_b16 a[168:169], v14 offset:10240\n\t"
      "v_mfma_f32_16x16x32_bf16 v[244:247], a[184:187], a[100:103], v[244:247]\n\t"
      "ds_read_b64_tr_b16 a[170:171], v14 offset:14336\n\t"
      "v_mfma_f32_16x16x32_bf16 v[228:231], a[188:191], a[100:103], v[228:231]\n\t"
      "s_waitcnt vmcnt(20)\n\t"
      "ds_read_b64_tr_b16 a[172:173], v15 offset:10240\n\t"
      "v_mfma_f32_16x16x32_bf16 v[224:227], a[188:191], a[96:99], v[224:227]\n\t"
      "s_waitcnt lgkmcnt(3)\n\t"
      "ds_read_b64_tr_b16 a[174:175], v15 offset:14336\n\t"
      "v_mfma_f32_16x16x32_bf16 v[48:51], a[160:163], a[104:107], v[48:51]\n\t"
      "ds_read_b64_tr_b16 a[176:177], v10 offset:18432\n\t"
      "v_mfma_f32_16x16x32_bf16 v[52:55], a[160:163], a[108:111], v[52:55]\n\t"
      "ds_read_b64_tr_b16 a[178:179], v10 offset:22528\n\t"
      "v_mfma_f32_16x16x32_bf16 v[84:87], a[164:167], a[108:111], v[84:87]\n\t"
      "ds_read_b64_tr_b16 a[180:181], v11 offset:18432\n\t"
      "v_mfma_f32_16x16x32_bf16 v[80:83], a[164:167], a[104:107], v[80:83]\n\t"
      "s_waitcnt lgkmcnt(3)\n\t"
      "ds_read_b64_tr_b16 a[182:183], v11 offset:22528\n\t"
      "v_mfma_f32_16x16x32_bf16 v[240:243], a[168:171], a[104:107], v[240:243]\n\t"
      "ds_read_b64_tr_b16 a[184:185], v14 offset:18432\n\t"
      "v_mfma_f32_16x16x32_bf16 v[244:247], a[168:171], a[108:111], v[244:247]\n\t"
      "ds_read_b64_tr_b16 a[186:187], v14 offset:22528\n\t"
      "v_mfma_f32_16x16x32_bf16 v[228:231], a[172:175], a[108:111], v[228:231]\n\t"
      "s_waitcnt vmcnt(18)\n\t"
      "ds_read_b64_tr_b16 a[188:189], v15 offset:18432\n\t"
      "v_mfma_f32_16x16x32_bf16 v[224:227], a[172:175], a[104:107], v[224:227]\n\t"
      "s_waitcnt lgkmcnt(3)\n\t"
      "ds_read_b64_tr_b16 a[190:191], v15 offset:22528\n\t"
      "v_mfma_f32_16x16x32_bf16 v[48:51], a[176:179], a[112:115], v[48:51]\n\t"
      "ds_read_b64_tr_b16 a[160:161], v10 offset:26624\n\t"
      "v_mfma_f32_16x16x32_bf16 v[52:55], a[176:179], a[116:119], v[52:55]\n\t"
      "ds_read_b64_tr_b16 a[162:163], v10 offset:30720\n\t"
      "v_mfma_f32_16x16x32_bf16 v[84:87], a[180:183], a[116:119], v[84:87]\n\t"
      "ds_read_b64_tr_b16 a[164:165], v11 offset:26624\n\t"
      "v_mfma_f32_16x16x32_bf16 v[80:83], a[180:183], a[112:115], v[80:83]\n\t"
      "s_waitcnt lgkmcnt(3)\n\t"
      "ds_read_b64_tr_b16 a[166:167], v11 offset:30720\n\t"
      "v_mfma_f32_16x16x32_bf16 v[240:243], a[184:187], a[112:115], v[240:243]\n\t"
      "ds_read_b64_tr_b16 a[168:169], v14 offset:26624\n\t"
      "v_mfma_f32_16x16x32_bf16 v[244:247], a[184:187], a[116:119], v[244:247]\n\t"
      "ds_read_b64_tr_b16 a[170:171], v14 offset:30720\n\t"
      "v_mfma_f32_16x16x32_bf16 v[228:231], a[188:191], a[116:119], v[228:231]\n\t"
      "s_waitcnt vmcnt(16)\n\t"
      "ds_read_b64_tr_b16 a[172:173], v15 offset:26624\n\t"
      "v_mfma_f32_16x16x32_bf16 v[224:227], a[188:191], a[112:115], v[224:227]\n\t"
      "s_waitcnt lgkmcnt(3)\n\t"
      "ds_read_b64_tr_b16 a[174:175], v15 offset:30720\n\t"
      "v_mfma_f32_16x16x32_bf16 v[48:51], a[160:163], a[120:123], v[48:51]\n\t"
      "ds_read_b64_tr_b16 a[176:177], v10 offset:34816\n\t"
      "v_mfma_f32_16x16x32_bf16 v[52:55], a[160:163], a[124:127], v[52:55]\n\t"
      "ds_read_b64_tr_b16 a[178:179], v10 offset:38912\n\t"
      "v_mfma_f32_16x16x32_bf16 v[84:87], a[164:167], a[124:127], v[84:87]\n\t"
      "ds_read_b64_tr_b16 a[180:181], v11 offset:34816\n\t"
      "v_mfma_f32_16x16x32_bf16 v[80:83], a[164:167], a[120:123], v[80:83]\n\t"
      "s_waitcnt lgkmcnt(3)\n\t"
      "ds_read_b64_tr_b16 a[182:183], v11 offset:38912\n\t"
      "v_mfma_f32_16x16x32_bf16 v[240:243], a[168:171], a[120:123], v[240:243]\n\t"
      "ds_read_b64_tr_b16 a[184:185], v14 offset:34816\n\t"
      "v_mfma_f32_16x16x32_bf16 v[244:247], a[168:171], a[124:127], v[244:247]\n\t"
      "ds_read_b64_tr_b16 a[186:187], v14 offset:38912\n\t"
      "v_mfma_f32_16x16x32_bf16 v[228:231], a[172:175], a[124:127], v[228:231]\n\t"
      "s_waitcnt vmcnt(14)\n\t"
      "ds_read_b64_tr_b16 a[188:189], v15 offset:34816\n\t"
      "v_mfma_f32_16x16x32_bf16 v[224:227], a[172:175], a[120:123], v[224:227]\n\t"
      "s_waitcnt lgkmcnt(3)\n\t"
      "ds_read_b64_tr_b16 a[190:191], v15 offset:38912\n\t"
      "v_mfma_f32_16x16x32_bf16 v[48:51], a[176:179], a[128:131], v[48:51]\n\t"
      "ds_read_b64_tr_b16 a[160:161], v10 offset:43008\n\t"
      "v_mfma_f32_16x16x32_bf16 v[52:55], a[176:179], a[132:135], v[52:55]\n\t"
      "ds_read_b64_tr_b16 a[162:163], v10 offset:47104\n\t"
      "v_mfma_f32_16x16x32_bf16 v[84:87], a[180:183], a[132:135], v[84:87]\n\t"
      "ds_read_b64_tr_b16 a[164:165], v11 offset:43008\n\t"
      "v_mfma_f32_16x16x32_bf16 v[80:83], a[180:183], a[128:131], v[80:83]\n\t"
      "s_waitcnt lgkmcnt(3)\n\t"
      "ds_read_b64_tr_b16 a[166:167], v11 offset:47104\n\t"
      "v_mfma_f32_16x16x32_bf16 v[240:243], a[184:187], a[128:131], v[240:243]\n\t"
      "ds_read_b64_tr_b16 a[168:169], v14 offset:43008\n\t"
      "v_mfma_f32_16x16x32_bf16 v[244:247], a[184:187], a[132:135], v[244:247]\n\t"
      "ds_read_b64_tr_b16 a[170:171], v14 offset:47104\n\t"
      "v_mfma_f32_16x16x32_bf16 v[228:231], a[188:191], a[132:135], v[228:231]\n\t"
      "s_waitcnt vmcnt(12)\n\t"
      "ds_read_b64_tr_b16 a[172:173], v15 offset:43008\n\t"
      "v_mfma_f32_16x16x32_bf16 v[224:227], a[188:191], a[128:131], v[224:227]\n\t"
      "s_waitcnt lgkmcnt(3)\n\t"
      "ds_read_b64_tr_b16 a[174:175], v15 offset:47104\n\t"
      "v_mfma_f32_16x16x32_bf16 v[48:51], a[160:163], a[136:139], v[48:51]\n\t"
      "ds_read_b64_tr_b16 a[176:177], v10 offset:51200\n\t"
      "v_mfma_f32_16x16x32_bf16 v[52:55], a[160:163], a[140:143], v[52:55]\n\t"
      "ds_read_b64_tr_b16 a[178:179], v10 offset:55296\n\t"
      "v_mfma_f32_16x16x32_bf16 v[84:87], a[164:167], a[140:143], v[84:87]\n\t"
      "ds_read_b64_tr_b16 a[180:181], v11 offset:51200\n\t"
      "v_mfma_f32_16x16x32_bf16 v[80:83], a[164:167], a[136:139], v[80:83]\n\t"
      "s_waitcnt lgkmcnt(3)\n\t"
      "ds_read_b64_tr_b16 a[182:183], v11 offset:55296\n\t"
      "v_mfma_f32_16x16x32_bf16 v[240:243], a[168:171], a[136:139], v[240:243]\n\t"
      "ds_read_b64_tr_b16 a[184:185], v14 offset:51200\n\t"
      "v_mfma_f32_16x16x32_bf16 v[244:247], a[168:171], a[140:143], v[244:247]\n\t"
      "ds_read_b64_tr_b16 a[186:187], v14 offset:55296\n\t"
      "v_mfma_f32_16x16x32_bf16 v[228:231], a[172:175], a[140:143], v[228:231]\n\t"
      "s_waitcnt vmcnt(9)\n\t"
      "ds_read_b64_tr_b16 a[188:189], v15 offset:51200\n\t"
      "v_mfma_f32_16x16x32_bf16 v[224:227], a[172:175], a[136:139], v[224:227]\n\t"
      "s_waitcnt lgkmcnt(3)\n\t"
      "ds_read_b64_tr_b16 a[190:191], v15 offset:55296\n\t"
      "v_mfma_f32_16x16x32_bf16 v[48:51], a[176:179], a[144:147], v[48:51]\n\t"
      "ds_read_b64_tr_b16 a[160:161], v10 offset:59392\n\t"
      "v_mfma_f32_16x16x32_bf16 v[52:55], a[176:179], a[148:151], v[52:55]\n\t"
      "ds_read_b64_tr_b16 a[162:163], v10 offset:63488\n\t"
      "v_mfma_f32_16x16x32_bf16 v[84:87], a[180:183], a[148:151], v[84:87]\n\t"
      "ds_read_b64_tr_b16 a[164:165], v11 offset:59392\n\t"
      "s_waitcnt vmcnt(0)\n\t"
      "s_barrier\n\t"
      "s_lshl_b32 s5, s32, 24\n\t"
      "s_add_u32 s5, s5, s17\n\t"
      "v_mov_b32 v12, s8\n\t"
      "v_mov_b32 v13, s9\n\t"
      "s_mov_b64 exec, 1\n\t"
      "buffer_store_dwordx2 v[12:13], off, s[96:99], s5 sc1\n\t"
      "s_mov_b64 exec, -1\n\t"
      STAMP(40, 41)
      "v_mfma_f32_16x16x32_bf16 v[80:83], a[180:183], a[144:147], v[80:83]\n\t"
      "s_waitcnt lgkmcnt(3)\n\t"
      "ds_read_b64_tr_b16 a[166:167], v11 offset:63488\n\t"
      "v_mfma_f32_16x16x32_bf16 v[240:243], a[184:187], a[144:147], v[240:243]\n\t"
      "ds_read_b64_tr_b16 a[168:169], v14 offset:59392\n\t"
      "v_mfma_f32_16x16x32_bf16 v[244:247], a[184:187], a[148:151], v[244:247]\n\t"
      "ds_read_b64_tr_b16 a[170:171], v14 offset:63488\n\t"
      "v_mfma_f32_16x16x32_bf16 v[228:231], a[188:191], a[148:151], v[228:231]\n\t"
      "s_waitcnt vmcnt(7)\n\t"
      "ds_read_b64_tr_b16 a[172:173], v15 offset:59392\n\t"
      "v_mfma_f32_16x16x32_bf16 v[224:227], a[188:191], a[144:147], v[224:227]\n\t"
      "s_waitcnt lgkmcnt(3)\n\t"
      "ds_read_b64_tr_b16 a[174:175], v15 offset:63488\n\t"
      "v_mfma_f32_16x16x32_bf16 v[48:51], a[160:163], a[152:155], v[48:51]\n\t"
      "ds_read_b64_tr_b16 a[176:177], v18 offset:0\n\t"
      "v_mfma_f32_16x16x32_bf16 v[52:55], a[160:163], a[156:159], v[52:55]\n\t"
      "ds_read_b64_tr_b16 a[178:179], v18 offset:4096\n\t"
      "v_mfma_f32_16x16x32_bf16 v[84:87], a[164:167], a[156:159], v[84:87]\n\t"
      "ds_read_b64_tr_b16 a[180:181], v19 offset:0\n\t"
      "v_mfma_f32_16x16x32_bf16 v[80:83], a[164:167], a[152:155], v[80:83]\n\t"
      "s_waitcnt lgkmcnt(3)\n\t"
      "ds_read_b64_tr_b16 a[182:183], v19 offset:4096\n\t"
      "v_mfma_f32_16x16x32_bf16 v[240:243], a[168:171], a[152:155], v[240:243]\n\t"
      LSTW(40, "")
      "ds_read_b64_tr_b16 a[184:185], v20 offset:0\n\t"
      "v_mfma_f32_16x16x32_bf16 v[244:247], a[168:171], a[156:159], v[244:247]\n\t"
      "ds_read_b64_tr_b16 a[186:187], v20 offset:4096\n\t"
      "v_mfma_f32_16x16x32_bf16 v[228:231], a[172:175], a[156:159], v[228:231]\n\t"
      "ds_read_b64_tr_b16 a[188:189], v21 offset:0\n\t"
      "v_mfma_f32_16x16x32_bf16 v[224:227], a[172:175], a[152:155], v[224:227]\n\t"
      "s_waitcnt lgkmcnt(3)\n\t"
      "ds_read_b64_tr_b16 a[190:191], v21 offset:4096\n\t"
      "v_mfma_f32_16x16x32_bf16 v[144:147], a[176:179], a[192:195], v[144:147]\n\t"
      "ds_read_b64_tr_b16 a[160:161], v18 offset:8192\n\t"
      "v_mfma_f32_16x16x32_bf16 v[148:151], a[176:179], a[196:199], v[148:151]\n\t"
      "ds_read_b64_tr_b16 a[162:163], v18 offset:12288\n\t"
      "v_mfma_f32_16x16x32_bf16 v[180:183], a[180:183], a[196:199], v[180:183]\n\t"
      "ds_read_b64_tr_b16 a[164:165], v19 offset:8192\n\t"
      "v_cvt_pk_bf16_f32 v48, v48, v49\n\t"
      "v_cvt_pk_bf16_f32 v49, v50, v51\n\t"
      "v_mfma_f32_16x16x32_bf16 v[176:179], a[180:183], a[192:195], v[176:179]\n\t"
      "s_waitcnt lgkmcnt(3)\n\t"
      "ds_read_b64_tr_b16 a[166:167], v19 offset:12288\n\t"
      "v_cvt_pk_bf16_f32 v50, v80, v81\n\t"
      "v_cvt_pk_bf16_f32 v51, v82, v83\n\t"
      "v_mfma_f32_16x16x32_bf16 v[112:115], a[184:187], a[192:195], v[112:115]\n\t"
      "ds_read_b64_tr_b16 a[168:169], v20 offset:8192\n\t"
      "buffer_store_dwordx4 v[48:51], v40, s[60:63], s57 offen offset:0 sc1\n\t"
      "v_cvt_pk_bf16_f32 v240, v240, v241\n\t"
      "v_mfma_f32_16x16x32_bf16 v[116:119], a[184:187], a[196:199], v[116:119]\n\t"
      "ds_read_b64_tr_b16 a[170:171], v20 offset:12288\n\t"
      "v_cvt_pk_bf16_f32 v241, v242, v243\n\t"
      "v_cvt_pk_bf16_f32 v242, v224, v225\n\t"
      "v_mfma_f32_16x16x32_bf16 v[212:215], a[188:191], a[196:199], v[212:215]\n\t"
      "ds_read_b64_tr_b16 a[172:173], v21 offset:8192\n\t"
      "v_cvt_pk_bf16_f32 v243, v226, v227\n\t"
      "buffer_store_dwordx4 v[240:243], v40, s[60:63], s57 offen offset:1024 sc1\n\t"
      "v_mfma_f32_16x16x32_bf16 v[208:211], a[188:191], a[192:195], v[208:211]\n\t"
      "s_waitcnt lgkmcnt(3)\n\t"
      "ds_read_b64_tr_b16 a[174:175], v21 offset:12288\n\t"
      "v_cvt_pk_bf16_f32 v52, v52, v53\n\t"
      "v_cvt_pk_bf16_f32 v53, v54, v55\n\t"
      "v_mfma_f32_16x16x32_bf16 v[144:147], a[160:163], a[208:211], v[144:147]\n\t"
      "ds_read_b64_tr_b16 a[176:177], v18 offset:16384\n\t"
      "v_cvt_pk_bf16_f32 v54, v84, v85\n\t"
      "v_cvt_pk_bf16_f32 v55, v86, v87\n\t"
      "v_mfma_f32_16x16x32_bf16 v[148:151], a[160:163], a[212:215], v[148:151]\n\t"
      "ds_read_b64_tr_b16 a[178:179], v18 offset:20480\n\t"
      "buffer_store_dwordx4 v[52:55], v40, s[60:63], s57 offen offset:2048 sc1\n\t"
      "v_cvt_pk_bf16_f32 v244, v244, v245\n\t"
      "v_mfma_f32_16x16x32_bf16 v[180:183], a[164:167], a[212:215], v[180:183]\n\t"
      "ds_read_b64_tr_b16 a[180:181], v19 offset:16384\n\t"
      "v_cvt_pk_bf16_f32 v245, v246, v247\n\t"
      "v_cvt_pk_bf16_f32 v246, v228, v229\n\t"
      "v_mfma_f32_16x16x32_bf16 v[176:179], a[164:167], a[208:211], v[176:179]\n\t"
      "s_waitcnt lgkmcnt(3)\n\t"
      "ds_read_b64_tr_b16 a[182:183], v19 offset:20480\n\t"
      "v_cvt_pk_bf16_f32 v247, v230, v231\n\t"
      "buffer_store_dwordx4 v[244:247], v40, s[60:63], s57 offen offset:3072 sc1\n\t"
      "v_mfma_f32_16x16x32_bf16 v[112:115], a[168:171], a[208:211], v[112:115]\n\t"
      "ds_read_b64_tr_b16 a[184:185], v20 offset:16384\n\t"
      "v_mfma_f32_16x16x32_bf16 v[116:119], a[168:171], a[212:215], v[116:119]\n\t"
      "ds_read_b64_tr_b16 a[186:187], v20 offset:20480\n\t"
      "v_mfma_f32_16x16x32_bf16 v[212:215], a[172:175], a[212:215], v[212:215]\n\t"
      "ds_read_b64_tr_b16 a[188:189], v21 offset:16384\n\t"
      "v_mfma_f32_16x16x32_bf16 v[208:211], a[172:175], a[208:211], v[208:211]\n\t"
      "s_waitcnt lgkmcnt(3)\n\t"
      "ds_read_b64_tr_b16 a[190:191], v21 offset:20480\n\t"
      "v_mfma_f32_16x16x32_bf16 v[144:147], a[176:179], a[224:227], v[144:147]\n\t"
      "ds_read_b64_tr_b16 a[160:161], v18 offset:24576\n\t"
      "v_mfma_f32_16x16x32_bf16 v[148:151], a[176:179], a[228:231], v[148:151]\n\t"
      "ds_read_b64_tr_b16 a[162:163], v18 offset:28672\n\t"
      "v_mfma_f32_16x16x32_bf16 v[180:183], a[180:183], a[228:231], v[180:183]\n\t"
      "ds_read_b64_tr_b16 a[164:165], v19 offset:24576\n\t"
      "v_mfma_f32_16x16x32_bf16 v[176:179], a[180:183], a[224:227], v[176:179]\n\t"
      "s_waitcnt lgkmcnt(3)\n\t"
      "ds_read_b64_tr_b16 a[166:167], v19 offset:28672\n\t"
      "v_mfma_f32_16x16x32_bf16 v[112:115], a[184:187], a[224:227], v[112:115]\n\t"
      "ds_read_b64_tr_b16 a[168:169], v20 offset:24576\n\t"
      "v_mfma_f32_16x16x32_bf16 v[116:119], a[184:187], a[228:231], v[116:119]\n\t"
      "ds_read_b64_tr_b16 a[170:171], v20 offset:28672\n\t"
      "v_mfma_f32_16x16x32_bf16 v[212:215], a[188:191], a[228:231], v[212:215]\n\t"
      "ds_read_b64_tr_b16 a[172:173], v21 offset:24576\n\t"
      "v_mfma_f32_16x16x32_bf16 v[208:211], a[188:191], a[224:227], v[208:211]\n\t"
      "s_waitcnt lgkmcnt(3)\n\t"
      "ds_read_b64_tr_b16 a[174:175], v21 offset:28672\n\t"
      "v_mfma_f32_16x16x32_bf16 v[144:147], a[160:163], a[240:243], v[144:147]\n\t"
      "ds_read_b64_tr_b16 a[176:177], v18 offset:32768\n\t"
      "v_mfma_f32_16x16x32_bf16 v[148:151], a[160:163], a[244:247], v[148:151]\n\t"
      "ds_read_b64_tr_b16 a[178:179], v18 offset:36864\n\t"
      "v_mfma_f32_16x16x32_bf16 v[180:183], a[164:167], a[244:247], v[180:183]\n\t"
      "ds_read_b64_tr_b16 a[180:181], v19 offset:32768\n\t"
      "v_mfma_f32_16x16x32_bf16 v[176:179], a[164:167], a[240:243], v[176:179]\n\t"
      "s_waitcnt lgkmcnt(3)\n\t"
      "ds_read_b64_tr_b16 a[182:183], v19 offset:36864\n\t"
      "v_mfma_f32_16x16x32_bf16 v[112:115], a[168:171], a[240:243], v[112:115]\n\t"
      "ds_read_b64_tr_b16 a[184:185], v20 offset:32768\n\t"
      "v_mfma_f32_16x16x32_bf16 v[116:119], a[168:171], a[244:247], v[116:119]\n\t"
      "ds_read_b64_tr_b16 a[186:187], v20 offset:36864\n\t"
      "v_mfma_f32_16x16x32_bf16 v[212:215], a[172:175], a[244:247], v[212:215]\n\t"
      "ds_read_b64_tr_b16 a[188:189], v21 offset:32768\n\t"
      "v_mfma_f32_16x16x32_bf16 v[208:211], a[172:175], a[240:243], v[208:211]\n\t"
      "s_waitcnt lgkmcnt(3)\n\t"
      "ds_read_b64_tr_b16 a[190:191], v21 offset:36864\n\t"
      "v_mfma_f32_16x16x32_bf16 v[144:147], a[176:179], a[64:67], v[144:147]\n\t"
      "ds_read_b64_tr_b16 a[160:161], v18 offset:40960\n\t"
      "v_mfma_f32_16x16x32_bf16 v[148:151], a[176:179], a[68:71], v[148:151]\n\t"
      "ds_read_b64_tr_b16 a[162:163], v18 offset:45056\n\t"
      "v_mfma_f32_16x16x32_bf16 v[180:183], a[180:183], a[68:71], v[180:183]\n\t"
      "ds_read_b64_tr_b16 a[164:165], v19 offset:40960\n\t"
      "v_mfma_f32_16x16x32_bf16 v[176:179], a[180:183], a[64:67], v[176:179]\n\t"
      "s_waitcnt lgkmcnt(3)\n\t"
      "ds_read_b64_tr_b16 a[166:167], v19 offset:45056\n\t"
      "v_mfma_f32_16x16x32_bf16 v[112:115], a[184:187], a[64:67], v[112:115]\n\t"
      "ds_read_b64_tr_b16 a[168:169], v20 offset:40960\n\t"
      "v_mfma_f32_16x16x32_bf16 v[116:119], a[184:187], a[68:71], v[116:119]\n\t"
      "ds_read_b64_tr_b16 a[170:171], v20 offset:45056\n\t"
      "v_mfma_f32_16x16x32_bf16 v[212:215], a[188:191], a[68:71], v[212:215]\n\t"
      "ds_read_b64_tr_b16 a[172:173], v21 offset:40960\n\t"
      "v_mfma_f32_16x16x32_bf16 v[208:211], a[188:191], a[64:67], v[208:211]\n\t"
      "s_waitcnt lgkmcnt(3)\n\t"
      "ds_read_b64_tr_b16 a[174:175], v21 offset:45056\n\t"
      "v_mfma_f32_16x16x32_bf16 v[144:147], a[160:163], a[72:75], v[144:147]\n\t"
      "ds_read_b64_tr_b16 a[176:177], v18 offset:49152\n\t"
      "v_mfma_f32_16x16x32_bf16 v[148:151], a[160:163], a[76:79], v[148:151]\n\t"
      "ds_read_b64_tr_b16 a[178:179], v18 offset:53248\n\t"
      "v_mfma_f32_16x16x32_bf16 v[180:183], a[164:167], a[76:79], v[180:183]\n\t"
      "ds_read_b64_tr_b16 a[180:181], v19 offset:49152\n\t"
      "v_mfma_f32_16x16x32_bf16 v[176:179], a[164:167], a[72:75], v[176:179]\n\t"
      "s_waitcnt lgkmcnt(3)\n\t"
      "ds_read_b64_tr_b16 a[182:183], v19 offset:53248\n\t"
      "v_mfma_f32_16x16x32_bf16 v[112:115], a[168:171], a[72:75], v[112:115]\n\t"
      "ds_read_b64_tr_b16 a[184:185], v20 offset:49152\n\t"
      "v_mfma_f32_16x16x32_bf16 v[116:119], a[168:171], a[76:79], v[116:119]\n\t"
      "ds_read_b64_tr_b16 a[186:187], v20 offset:53248\n\t"
      "v_mfma_f32_16x16x32_bf16 v[212:215], a[172:175], a[76:79], v[212:215]\n\t"
      "ds_read_b64_tr_b16 a[188:189], v21 offset:49152\n\t"
      "buffer_load_dwordx2 v[8:9], v38, s[96:99], s16 offen sc1\n\t"
      "v_mfma_f32_16x16x32_bf16 v[208:211], a[172:175], a[72:75], v[208:211]\n\t"
      "s_waitcnt lgkmcnt(3)\n\t"
      "ds_read_b64_tr_b16 a[190:191], v21 offset:53248\n\t"
      "v_mfma_f32_16x16x32_bf16 v[144:147], a[176:179], a[80:83], v[144:147]\n\t"
      "ds_read_b64_tr_b16 a[160:161], v18 offset:57344\n\t"
      "v_mfma_f32_16x16x32_bf16 v[148:151], a[176:179], a[84:87], v[148:151]\n\t"
      "ds_read_b64_tr_b16 a[162:163], v18 offset:61440\n\t"
      "v_mfma_f32_16x16x32_bf16 v[180:183], a[180:183], a[84:87], v[180:183]\n\t"
      "ds_read_b64_tr_b16 a[164:165], v19 offset:57344\n\t"
      "v_mfma_f32_16x16x32_bf16 v[176:179], a[180:183], a[80:83], v[176:179]\n\t"
      "s_waitcnt lgkmcnt(3)\n\t"
      "ds_read_b64_tr_b16 a[166:167], v19 offset:61440\n\t"
      "v_mfma_f32_16x16x32_bf16 v[112:115], a[184:187], a[80:83], v[112:115]\n\t"
      "ds_read_b64_tr_b16 a[168:169], v20 offset:57344\n\t"
      "v_mfma_f32_16x16x32_bf16 v[116:119], a[184:187], a[84:87], v[116:119]\n\t"
      "ds_read_b64_tr_b16 a[170:171], v20 offset:61440\n\t"
      "v_mfma_f32_16x16x32_bf16 v[212:215], a[188:191], a[84:87], v[212:215]\n\t"
      "ds_read_b64_tr_b16 a[172:173], v21 offset:57344\n\t"
      "v_mfma_f32_16x16x32_bf16 v[208:211], a[188:191], a[80:83], v[208:211]\n\t"
      "s_waitcnt lgkmcnt(3)\n\t"
      "ds_read_b64_tr_b16 a[174:175], v21 offset:61440\n\t"
      "v_mfma_f32_16x16x32_bf16 v[144:147], a[160:163], a[88:91], v[144:147]\n\t"
      "ds_read_b64_tr_b16 a[176:177], v10 offset:0\n\t"
      "v_mfma_f32_16x16x32_bf16 v[148:151], a[160:163], a[92:95], v[148:151]\n\t"
      "ds_read_b64_tr_b16 a[178:179], v10 offset:4096\n\t"
      "v_mfma_f32_16x16x32_bf16 v[180:183], a[164:167], a[92:95], v[180:183]\n\t"
      "ds_read_b64_tr_b16 a[180:181], v11 offset:0\n\t"
      "v_mfma_f32_16x16x32_bf16 v[176:179], a[164:167], a[88:91], v[176:179]\n\t"
      "s_waitcnt lgkmcnt(3)\n\t"
      "ds_read_b64_tr_b16 a[182:183], v11 offset:4096\n\t"
      "v_mfma_f32_16x16x32_bf16 v[112:115], a[168:171], a[88:91], v[112:115]\n\t"
      "ds_read_b64_tr_b16 a[184:185], v14 offset:0\n\t"
      "v_mfma_f32_16x16x32_bf16 v[116:119], a[168:171], a[92:95], v[116:119]\n\t"
      "ds_read_b64_tr_b16 a[186:187], v14 offset:4096\n\t"
      "v_mfma_f32_16x16x32_bf16 v[212:215], a[172:175], a[92:95], v[212:215]\n\t"
      "ds_read_b64_tr_b16 a[188:189], v15 offset:0\n\t"
      "v_mfma_f32_16x16x32_bf16 v[208:211], a[172:175], a[88:91], v[208:211]\n\t"
      "s_waitcnt lgkmcnt(3)\n\t"
      "ds_read_b64_tr_b16 a[190:191], v15 offset:4096\n\t"
      "v_mfma_f32_16x16x32_bf16 v[144:147], a[176:179], a[96:99], v[144:147]\n\t"
      "ds_read_b64_tr_b16 a[160:161], v10 offset:8192\n\t"
      "v_mfma_f32_16x16x32_bf16 v[148:151], a[176:179], a[100:103], v[148:151]\n\t"
      "ds_read_b64_tr_b16 a[162:163], v10 offset:12288\n\t"
      "v_mfma_f32_16x16x32_bf16 v[180:183], a[180:183], a[100:103], v[180:183]\n\t"
      "ds_read_b64_tr_b16 a[164:165], v11 offset:8192\n\t"
      "v_mfma_f32_16x16x32_bf16 v[176:179], a[180:183], a[96:99], v[176:179]\n\t"
      "s_waitcnt lgkmcnt(3)\n\t"
      "ds_read_b64_tr_b16 a[166:167], v11 offset:12288\n\t"
      "v_mfma_f32_16x16x32_bf16 v[112:115], a[184:187], a[96:99], v[112:115]\n\t"
      "ds_read_b64_tr_b16 a[168:169], v14 offset:8192\n\t"
      "v_mfma_f32_16x16x32_bf16 v[116:119], a[184:187], a[100:103], v[116:119]\n\t"
      "ds_read_b64_tr_b16 a[170:171], v14 offset:12288\n\t"
      "v_mfma_f32_16x16x32_bf16 v[212:215], a[188:191], a[100:103], v[212:215]\n\t"
      "ds_read_b64_tr_b16 a[172:173], v15 offset:8192\n\t"
      "v_mfma_f32_16x16x32_bf16 v[208:211], a[188:191], a[96:99], v[208:211]\n\t"
      "s_waitcnt lgkmcnt(3)\n\t"
      "ds_read_b64_tr_b16 a[174:175], v15 offset:12288\n\t"
      "v_mfma_f32_16x16x32_bf16 v[144:147], a[160:163], a[104:107], v[144:147]\n\t"
      "ds_read_b64_tr_b16 a[176:177], v10 offset:16384\n\t"
      "v_mfma_f32_16x16x32_bf16 v[148:151], a[160:163], a[108:111], v[148:151]\n\t"
      "ds_read_b64_tr_b16 a[178:179], v10 offset:20480\n\t"
      "v_mfma_f32_16x16x32_bf16 v[180:183], a[164:167], a[108:111], v[180:183]\n\t"
      "ds_read_b64_tr_b16 a[180:181], v11 offset:16384\n\t"
      "v_mfma_f32_16x16x32_bf16 v[176:179], a[164:167], a[104:107], v[176:179]\n\t"
      "s_waitcnt lgkmcnt(3)\n\t"
      "ds_read_b64_tr_b16 a[182:183], v11 offset:20480\n\t"
      "v_mfma_f32_16x16x32_bf16 v[112:115], a[168:171], a[104:107], v[112:115]\n\t"
      "ds_read_b64_tr_b16 a[184:185], v14 offset:16384\n\t"
      "v_mfma_f32_16x16x32_bf16 v[116:119], a[168:171], a[108:111], v[116:119]\n\t"
      "ds_read_b64_tr_b16 a[186:187], v14 offset:20480\n\t"
      "v_mfma_f32_16x16x32_bf16 v[212:215], a[172:175], a[108:111], v[212:215]\n\t"
      "ds_read_b64_tr_b16 a[188:189], v15 offset:16384\n\t"
      "v_mfma_f32_16x16x32_bf16 v[208:211], a[172:175], a[104:107], v[208:211]\n\t"
      "s_waitcnt lgkmcnt(3)\n\t"
      "ds_read_b64_tr_b16 a[190:191], v15 offset:20480\n\t"
      "v_mfma_f32_16x16x32_bf16 v[144:147], a[176:179], a[112:115], v[144:147]\n\t"
      "ds_read_b64_tr_b16 a[160:161], v10 offset:24576\n\t"
      "v_mfma_f32_16x16x32_bf16 v[148:151], a[176:179], a[116:119], v[148:151]\n\t"
      "ds_read_b64_tr_b16 a[162:163], v10 offset:28672\n\t"
      "v_mfma_f32_16x16x32_bf16 v[180:183], a[180:183], a[116:119], v[180:183]\n\t"
      "ds_read_b64_tr_b16 a[164:165], v11 offset:24576\n\t"
      "v_mfma_f32_16x16x32_bf16 v[176:179], a[180:183], a[112:115], v[176:179]\n\t"
      "s_waitcnt lgkmcnt(3)\n\t"
      "ds_read_b64_tr_b16 a[166:167], v11 offset:28672\n\t"
      "v_mfma_f32_16x16x32_bf16 v[112:115], a[184:187], a[112:115], v[112:115]\n\t"
      "ds_read_b64_tr_b16 a[168:169], v14 offset:24576\n\t"
      "v_mfma_f32_16x16x32_bf16 v[116:119], a[184:187], a[116:119], v[116:119]\n\t"
      "ds_read_b64_tr_b16 a[170:171], v14 offset:28672\n\t"
      "v_mfma_f32_16x16x32_bf16 v[212:215], a[188:191], a[116:119], v[212:215]\n\t"
      "ds_read_b64_tr_b16 a[172:173], v15 offset:24576\n\t"
      "s_waitcnt vmcnt(1)\n\t"
      "s_barrier\n\t"
      "s_lshl_b32 s5, s32, 24\n\t"
      "s_add_u32 s5, s5, s18\n\t"
      "v_mov_b32 v12, s8\n\t"
      "v_mov_b32 v13, s9\n\t"
      "s_mov_b64 exec, 1\n\t"
      "buffer_store_dwordx2 v[12:13], off, s[96:99], s5 sc1\n\t"
      "s_mov_b64 exec, -1\n\t"
      STAMP(38, 39)
      "s_waitcnt vmcnt(1)\n\t"
      "v_cmp_eq_u64 vcc, s[8:9], v[8:9]\n\t"
      "s_nop 0\n\t"
      "s_cmp_eq_u64 vcc, -1\n\t"
      LSTW(38, "s_waitcnt lgkmcnt(0)\n\t")
      "s_cbranch_scc1 Lpd5_%=\n\t"
      "s_mov_b32 s10, 200000\n\t"
      "Lp5_%=:\n\t"
      "buffer_load_dwordx2 v[8:9], v38, s[96:99], s16 offen offset:0 sc1\n\t"
      "s_waitcnt vmcnt(0)\n\t"
      "v_cmp_eq_u64 vcc, s[8:9], v[8:9]\n\t"
      "s_nop 0\n\t"
      "s_cmp_eq_u64 vcc, -1\n\t"
      "s_cbranch_scc1 Lpd5_%=\n\t"
      "s_sleep 2\n\t"
      "s_sub_u32 s10, s10, 1\n\t"
      "s_cmp_lg_u32 s10, 0\n\t"
      "s_cbranch_scc1 Lp5_%=\n\t"
      "Lpd5_%=:\n\t"
      "buffer_load_dwordx4 v[128:131], v40, s[60:63], s14 offen offset:0 sc1\n\t"
      "buffer_load_dwordx4 v[132:135], v40, s[60:63], s14 offen offset:1024 sc1\n\t"
      "buffer_load_dwordx4 v[136:139], v40, s[60:63], s14 offen offset:2048 sc1\n\t"
      "buffer_load_dwordx4 v[140:143], v40, s[60:63], s14 offen offset:3072 sc1\n\t"
      "buffer_load_dwordx2 v[2:3], v39, s[96:99], s16 offen sc1\n\t"
      "buffer_load_dwordx4 v[160:163], v40, s[60:63], s15 offen offset:0 sc1\n\t"
      "buffer_load_dwordx4 v[164:167], v40, s[60:63], s15 offen offset:1024 sc1\n\t"
      "buffer_load_dwordx4 v[168:171], v40, s[60:63], s15 offen offset:2048 sc1\n\t"
      "buffer_load_dwordx4 v[172:175], v40, s[60:63], s15 offen offset:3072 sc1\n\t"
      "v_mfma_f32_16x16x32_bf16 v[208:211], a[188:191], a[112:115], v[208:211]\n\t"
      "s_waitcnt lgkmcnt(3)\n\t"
      "ds_read_b64_tr_b16 a[174:175], v15 offset:28672\n\t"
      "v_mfma_f32_16x16x32_bf16 v[144:147], a[160:163], a[120:123], v[144:147]\n\t"
      "ds_read_b64_tr_b16 a[176:177], v10 offset:32768\n\t"
      "v_mfma_f32_16x16x32_bf16 v[148:151], a[160:163], a[124:127], v[148:151]\n\t"
      "ds_read_b64_tr_b16 a[178:179], v10 offset:36864\n\t"
      "v_mfma_f32_16x16x32_bf16 v[180:183], a[164:167], a[124:127], v[180:183]\n\t"
      "ds_read_b64_tr_b16 a[180:181], v11 offset:32768\n\t"
      "v_mfma_f32_16x16x32_bf16 v[176:179], a[164:167], a[120:123], v[176:179]\n\t"
      "s_waitcnt lgkmcnt(3)\n\t"
      "ds_read_b64_tr_b16 a[182:183], v11 offset:36864\n\t"
      "v_mfma_f32_16x16x32_bf16 v[112:115], a[168:171], a[120:123], v[112:115]\n\t"
      "ds_read_b64_tr_b16 a[184:185], v14 offset:32768\n\t"
      "v_mfma_f32_16x16x32_bf16 v[116:119], a[168:171], a[124:127], v[116:119]\n\t"
      "ds_read_b64_tr_b16 a[186:187], v14 offset:36864\n\t"
      "v_mfma_f32_16x16x32_bf16 v[212:215], a[172:175], a[124:127], v[212:215]\n\t"
      "ds_read_b64_tr_b16 a[188:189], v15 offset:32768\n\t"
      "v_mfma_f32_16x16x32_bf16 v[208:211], a[172:175], a[120:123], v[208:211]\n\t"
      "s_waitcnt lgkmcnt(3)\n\t"
      "ds_read_b64_tr_b16 a[190:191], v15 offset:36864\n\t"
      "v_mfma_f32_16x16x32_bf16 v[144:147], a[176:179], a[128:131], v[144:147]\n\t"
      "ds_read_b64_tr_b16 a[160:161], v10 offset:40960\n\t"
      "v_mfma_f32_16x16x32_bf16 v[148:151], a[176:179], a[132:135], v[148:151]\n\t"
      "ds_read_b64_tr_b16 a[162:163], v10 offset:45056\n\t"
      "v_mfma_f32_16x16x32_bf16 v[180:183], a[180:183], a[132:135], v[180:183]\n\t"
      "ds_read_b64_tr_b16 a[164:165], v11 offset:40960\n\t"
      "v_mfma_f32_16x16x32_bf16 v[176:179], a[180:183], a[128:131], v[176:179]\n\t"
      "s_waitcnt lgkmcnt(3)\n\t"
      "ds_read_b64_tr_b16 a[166:167], v11 offset:45056\n\t"
      "v_mfma_f32_16x16x32_bf16 v[112:115], a[184:187], a[128:131], v[112:115]\n\t"
      "ds_read_b64_tr_b16 a[168:169], v14 offset:40960\n\t"
      "v_mfma_f32_16x16x32_bf16 v[116:119], a[184:187], a[132:135], v[116:119]\n\t"
      "ds_read_b64_tr_b16 a[170:171], v14 offset:45056\n\t"
      "v_mfma_f32_16x16x32_bf16 v[212:215], a[188:191], a[132:135], v[212:215]\n\t"
      "ds_read_b64_tr_b16 a[172:173], v15 offset:40960\n\t"
      "v_mfma_f32_16x16x32_bf16 v[208:211], a[188:191], a[128:131], v[208:211]\n\t"
      "s_waitcnt lgkmcnt(3)\n\t"
      "ds_read_b64_tr_b16 a[174:175], v15 offset:45056\n\t"
      "v_mfma_f32_16x16x32_bf16 v[144:147], a[160:163], a[136:139], v[144:147]\n\t"
      "ds_read_b64_tr_b16 a[176:177], v10 offset:49152\n\t"
      "v_mfma_f32_16x16x32_bf16 v[148:151], a[160:163], a[140:143], v[148:151]\n\t"
      "ds_read_b64_tr_b16 a[178:179], v10 offset:53248\n\t"
      "v_mfma_f32_16x16x32_bf16 v[180:183], a[164:167], a[140:143], v[180:183]\n\t"
      "ds_read_b64_tr_b16 a[180:181], v11 offset:49152\n\t"
      "v_mfma_f32_16x16x32_bf16 v[176:179], a[164:167], a[136:139], v[176:179]\n\t"
      "s_waitcnt lgkmcnt(3)\n\t"
      "ds_read_b64_tr_b16 a[182:183], v11 offset:53248\n\t"
      "v_mfma_f32_16x16x32_bf16 v[112:115], a[168:171], a[136:139], v[112:115]\n\t"
      "ds_read_b64_tr_b16 a[184:185], v14 offset:49152\n\t"
      "v_mfma_f32_16x16x32_bf16 v[116:119], a[168:171], a[140:143], v[116:119]\n\t"
      "ds_read_b64_tr_b16 a[186:187], v14 offset:53248\n\t"
      "v_mfma_f32_16x16x32_bf16 v[212:215], a[172:175], a[140:143], v[212:215]\n\t"
      "ds_read_b64_tr_b16 a[188:189], v15 offset:49152\n\t"
      "v_mfma_f32_16x16x32_bf16 v[208:211], a[172:175], a[136:139], v[208:211]\n\t"
      "s_waitcnt lgkmcnt(3)\n\t"
      "ds_read_b64_tr_b16 a[190:191], v15 offset:53248\n\t"
      "v_mfma_f32_16x16x32_bf16 v[144:147], a[176:179], a[144:147], v[144:147]\n\t"
      "ds_read_b64_tr_b16 a[160:161], v10 offset:57344\n\t"
      "v_mfma_f32_16x16x32_bf16 v[148:151], a[176:179], a[148:151], v[148:151]\n\t"
      "ds_read_b64_tr_b16 a[162:163], v10 offset:61440\n\t"
      "v_mfma_f32_16x16x32_bf16 v[180:183], a[180:183], a[148:151], v[180:183]\n\t"
      "ds_read_b64_tr_b16 a[164:165], v11 offset:57344\n\t"
      "v_mfma_f32_16x16x32_bf16 v[176:179], a[180:183], a[144:147], v[176:179]\n\t"
      "s_waitcnt lgkmcnt(3)\n\t"
      "ds_read_b64_tr_b16 a[166:167], v11 offset:61440\n\t"
      "v_mfma_f32_16x16x32_bf16 v[112:115], a[184:187], a[144:147], v[112:115]\n\t"
      "ds_read_b64_tr_b16 a[168:169], v14 offset:57344\n\t"
      "v_mfma_f32_16x16x32_bf16 v[116:119], a[184:187], a[148:151], v[116:119]\n\t"
      "ds_read_b64_tr_b16 a[170:171], v14 offset:61440\n\t"
      "v_mfma_f32_16x16x32_bf16 v[212:215], a[188:191], a[148:151], v[212:215]\n\t"
      "ds_read_b64_tr_b16 a[172:173], v15 offset:57344\n\t"
      "v_mfma_f32_16x16x32_bf16 v[208:211], a[188:191], a[144:147], v[208:211]\n\t"
      "s_waitcnt lgkmcnt(3)\n\t"
      "ds_read_b64_tr_b16 a[174:175], v15 offset:61440\n\t"
      "v_mfma_f32_16x16x32_bf16 v[144:147], a[160:163], a[152:155], v[144:147]\n\t"
      "v_mfma_f32_16x16x32_bf16 v[148:151], a[160:163], a[156:159], v[148:151]\n\t"
      "v_mfma_f32_16x16x32_bf16 v[180:183], a[164:167], a[156:159], v[180:183]\n\t"
      "v_mfma_f32_16x16x32_bf16 v[176:179], a[164:167], a[152:155], v[176:179]\n\t"
      "s_waitcnt lgkmcnt(0)\n\t"
      "v_mfma_f32_16x16x32_bf16 v[112:115], a[168:171], a[152:155], v[112:115]\n\t"
      "v_mfma_f32_16x16x32_bf16 v[116:119], a[168:171], a[156:159], v[116:119]\n\t"
      "v_mfma_f32_16x16x32_bf16 v[212:215], a[172:175], a[156:159], v[212:215]\n\t"
      "v_mfma_f32_16x16x32_bf16 v[208:211], a[172:175], a[152:155], v[208:211]\n\t"
      "s_waitcnt lgkmcnt(15)\n\t"
      "v_mfma_f32_16x16x32_bf16 v[144:147], v[64:67], a[32:35], v[144:147]\n\t"
      "v_mfma_f32_16x16x32_bf16 v[176:179], v[68:71], a[32:35], v[176:179]\n\t"
      "v_mfma_f32_16x16x32_bf16 v[144:147], v[64:67], a[48:51], v[144:147]\n\t"
      "v_mfma_f32_16x16x32_bf16 v[176:179], v[68:71], a[48:51], v[176:179]\n\t"
      "v_mfma_f32_16x16x32_bf16 v[112:115], v[64:67], a[36:39], v[112:115]\n\t"
      "v_mfma_f32_16x16x32_bf16 v[208:211], v[68:71], a[36:39], v[208:211]\n\t"
      "v_mfma_f32_16x16x32_bf16 v[112:115], v[64:67], a[52:55], v[112:115]\n\t"
      "v_mfma_f32_16x16x32_bf16 v[208:211], v[68:71], a[52:55], v[208:211]\n\t"
      "v_mfma_f32_16x16x32_bf16 v[148:151], v[64:67], a[40:43], v[148:151]\n\t"
      "v_mfma_f32_16x16x32_bf16 v[180:183], v[68:71], a[40:43], v[180:183]\n\t"
      "v_mfma_f32_16x16x32_bf16 v[148:151], v[64:67], a[48:51], v[148:151]\n\t"
      "v_mfma_f32_16x16x32_bf16 v[180:183], v[68:71], a[48:51], v[180:183]\n\t"
      "v_mfma_f32_16x16x32_bf16 v[116:119], v[64:67], a[44:47], v[116:119]\n\t"
      "v_mfma_f32_16x16x32_bf16 v[212:215], v[68:71], a[44:47], v[212:215]\n\t"
      "v_mfma_f32_16x16x32_bf16 v[116:119], v[64:67], a[52:55], v[116:119]\n\t"
      "v_mfma_f32_16x16x32_bf16 v[212:215], v[68:71], a[52:55], v[212:215]\n\t"
      "s_waitcnt vmcnt(4)\n\t"
      STAMP(28, 29)
      "v_cmp_eq_u64 vcc, s[8:9], v[2:3]\n\t"
      "s_nop 0\n\t"
      "s_cmp_eq_u64 vcc, -1\n\t"
      LSTW(28, "s_waitcnt lgkmcnt(0)\n\t")
      "s_cbranch_scc1 Lpd4_%=\n\t"
      "s_mov_b32 s10, 200000\n\t"
      "Lp4_%=:\n\t"
      "buffer_load_dwordx2 v[2:3], v39, s[96:99], s16 offen offset:0 sc1\n\t"
      "s_waitcnt vmcnt(0)\n\t"
      "v_cmp_eq_u64 vcc, s[8:9], v[2:3]\n\t"
      "s_nop 0\n\t"
      "s_cmp_eq_u64 vcc, -1\n\t"
      "s_cbranch_scc1 Lpd4_%=\n\t"
      "s_sleep 2\n\t"
      "s_sub_u32 s10, s10, 1\n\t"
      "s_cmp_lg_u32 s10, 0\n\t"
      "s_cbranch_scc1 Lp4_%=\n\t"
      "Lpd4_%=:\n\t"
      STAMP(30, 31)
      "buffer_load_dwordx4 v[192:195], v40, s[60:63], s56 offen offset:0 sc1\n\t"
      "buffer_load_dwordx4 v[196:199], v40, s[60:63], s56 offen offset:1024 sc1\n\t"
      "buffer_load_dwordx4 v[200:203], v40, s[60:63], s56 offen offset:2048 sc1\n\t"
      "buffer_load_dwordx4 v[204:207], v40, s[60:63], s56 offen offset:3072 sc1\n\t"
      LSTW(30, "s_waitcnt lgkmcnt(0)\n\t")
      STAMP(12, 13)
      "s_cmp_lg_u32 s29, 1\n\t"
      LSTW(12, "s_waitcnt lgkmcnt(0)\n\t")
      "s_cbranch_scc1 Le8_%=\n\t"
      "v_bfe_u32 v44, v40, 4, 3\n\t"
      "v_lshlrev_b32 v44, 12, v44\n\t"
      "v_add_u32 v44, 36864, v44\n\t"
      "s_mov_b32 s10, 200000\n\t"
      "Lp9_%=:\n\t"
      "buffer_load_dwordx2 v[2:3], v44, s[96:99], 0 offen offset:0 sc1\n\t"
      "s_waitcnt vmcnt(0)\n\t"
      "v_cmp_eq_u64 vcc, s[8:9], v[2:3]\n\t"
      "s_nop 0\n\t"
      "s_cmp_eq_u64 vcc, -1\n\t"
      "s_cbranch_scc1 Lpd9_%=\n\t"
      "s_sleep 2\n\t"
      "s_sub_u32 s10, s10, 1\n\t"
      "s_cmp_lg_u32 s10, 0\n\t"
      "s_cbranch_scc1 Lp9_%=\n\t"
      "Lpd9_%=:\n\t"
      "v_mov_b32 v2, s8\n\t"
      "s_mov_b64 exec, 1\n\t"
      "buffer_store_dword v2, off, s[96:99], 0 sc1\n\t"
      "s_mov_b64 exec, -1\n\t"
      "Le8_%=:\n\t"
      "s_waitcnt vmcnt(12)\n\t"
      "v_mfma_f32_16x16x32_bf16 v[144:147], v[34:37], v[128:131], v[144:147]\n\t"
      "v_mfma_f32_16x16x32_bf16 v[176:179], v[32:35], v[128:131], v[176:179]\n\t"
      "s_waitcnt vmcnt(11)\n\t"
      "v_mfma_f32_16x16x32_bf16 v[112:115], v[34:37], v[132:135], v[112:115]\n\t"
      "v_mfma_f32_16x16x32_bf16 v[208:211], v[32:35], v[132:135], v[208:211]\n\t"
      "s_waitcnt vmcnt(10)\n\t"
      "v_mfma_f32_16x16x32_bf16 v[148:151], v[34:37], v[136:139], v[148:151]\n\t"
      "v_mfma_f32_16x16x32_bf16 v[180:183], v[32:35], v[136:139], v[180:183]\n\t"
      "s_waitcnt vmcnt(9)\n\t"
      "v_mfma_f32_16x16x32_bf16 v[116:119], v[34:37], v[140:143], v[116:119]\n\t"
      "v_mfma_f32_16x16x32_bf16 v[212:215], v[32:35], v[140:143], v[212:215]\n\t"
      "s_waitcnt vmcnt(7)\n\t"
      "v_mfma_f32_16x16x32_bf16 v[144:147], v[34:37], v[160:163], v[144:147]\n\t"
      "v_mfma_f32_16x16x32_bf16 v[176:179], v[32:35], v[160:163], v[176:179]\n\t"
      "s_waitcnt vmcnt(6)\n\t"
      "v_mfma_f32_16x16x32_bf16 v[112:115], v[34:37], v[164:167], v[112:115]\n\t"
      "v_mfma_f32_16x16x32_bf16 v[208:211], v[32:35], v[164:167], v[208:211]\n\t"
      "s_waitcnt vmcnt(5)\n\t"
      "v_mfma_f32_16x16x32_bf16 v[148:151], v[34:37], v[168:171], v[148:151]\n\t"
      "v_mfma_f32_16x16x32_bf16 v[180:183], v[32:35], v[168:171], v[180:183]\n\t"
      "s_waitcnt vmcnt(4)\n\t"
      "v_mfma_f32_16x16x32_bf16 v[116:119], v[34:37], v[172:175], v[116:119]\n\t"
      "v_mfma_f32_16x16x32_bf16 v[212:215], v[32:35], v[172:175], v[212:215]\n\t"
      "s_waitcnt vmcnt(3)\n\t"
      "v_mfma_f32_16x16x32_bf16 v[144:147], v[34:37], v[192:195], v[144:147]\n\t"
      "v_mfma_f32_16x16x32_bf16 v[176:179], v[32:35], v[192:195], v[176:179]\n\t"
      "s_waitcnt vmcnt(2)\n\t"
      "v_mfma_f32_16x16x32_bf16 v[112:115], v[34:37], v[196:199], v[112:115]\n\t"
      "v_mfma_f32_16x16x32_bf16 v[208:211], v[32:35], v[196:199], v[208:211]\n\t"
      "s_waitcnt vmcnt(1)\n\t"
      "v_mfma_f32_16x16x32_bf16 v[148:151], v[34:37], v[200:203], v[148:151]\n\t"
      "v_mfma_f32_16x16x32_bf16 v[180:183], v[32:35], v[200:203], v[180:183]\n\t"
      "s_waitcnt vmcnt(0)\n\t"
      "v_mfma_f32_16x16x32_bf16 v[116:119], v[34:37], v[204:207], v[116:119]\n\t"
      "v_mfma_f32_16x16x32_bf16 v[212:215], v[32:35], v[204:207], v[212:215]\n\t"
      "v_cvt_pk_bf16_f32 v96, v144, v145\n\t"
      "v_cvt_pk_bf16_f32 v97, v146, v147\n\t"
      "ds_write_b64 v74, v[96:97] offset:0\n\t"
      "v_cvt_pk_bf16_f32 v98, v176, v177\n\t"
      "v_cvt_pk_bf16_f32 v99, v178, v179\n\t"
      "ds_write_b64 v75, v[98:99] offset:0\n\t"
      "v_cvt_pk_bf16_f32 v100, v112, v113\n\t"
      "v_cvt_pk_bf16_f32 v101, v114, v115\n\t"
      "ds_write_b64 v76, v[100:101] offset:0\n\t"
      "v_cvt_pk_bf16_f32 v102, v208, v209\n\t"
      "v_cvt_pk_bf16_f32 v103, v210, v211\n\t"
      "ds_write_b64 v77, v[102:103] offset:0\n\t"
      "v_cvt_pk_bf16_f32 v104, v148, v149\n\t"
      "v_cvt_pk_bf16_f32 v105, v150, v151\n\t"
      "ds_write_b64 v74, v[104:105] offset:2048\n\t"
      "v_cvt_pk_bf16_f32 v106, v180, v181\n\t"
      "v_cvt_pk_bf16_f32 v107, v182, v183\n\t"
      "ds_write_b64 v75, v[106:107] offset:2048\n\t"
      "v_cvt_pk_bf16_f32 v108, v116, v117\n\t"
      "v_cvt_pk_bf16_f32 v109, v118, v119\n\t"
      "ds_write_b64 v76, v[108:109] offset:2048\n\t"
      "v_cvt_pk_bf16_f32 v110, v212, v213\n\t"
      "v_cvt_pk_bf16_f32 v111, v214, v215\n\t"
      "ds_write_b64 v77, v[110:111] offset:2048\n\t"
      "s_add_u32 s11, s70, 16384\n\t"
      "s_add_u32 s12, s70, 32768\n\t"
      "s_add_u32 s13, s70, 49152\n\t"
      "s_waitcnt lgkmcnt(0)\n\t"
      "ds_read_b128 v[96:99], v78 offset:0\n\t"
      "ds_read_b128 v[100:103], v78 offset:1024\n\t"
      "ds_read_b128 v[104:107], v78 offset:2048\n\t"
      "ds_read_b128 v[108:111], v78 offset:3072\n\t"
      "s_waitcnt lgkmcnt(3)\n\t"
      "buffer_store_dwordx4 v[96:99], v17, s[92:95], s70 offen sc1\n\t"
      "s_waitcnt lgkmcnt(2)\n\t"
      "buffer_store_dwordx4 v[100:103], v17, s[92:95], s11 offen sc1\n\t"
      "s_waitcnt lgkmcnt(1)\n\t"
      "buffer_store_dwordx4 v[104:107], v17, s[92:95], s12 offen sc1\n\t"
      "s_waitcnt lgkmcnt(0)\n\t"
      "buffer_store_dwordx4 v[108:111], v17, s[92:95], s13 offen sc1\n\t"
      STAMPEND
      "s_endpgm\n\t"
      "Lh_%=:\n\t"
      "s_waitcnt vmcnt(16)\n\t"
      "v_cmp_eq_u64 vcc, s[8:9], v[2:3]\n\t"
      "s_nop 0\n\t"
      "s_cmp_eq_u64 vcc, -1\n\t"
      "s_cbranch_scc1 Lpd27_%=\n\t"
      "s_mov_b32 s10, 200000\n\t"
      "Lp27_%=:\n\t"
      "buffer_load_dwordx2 v[2:3], v27, s[96:99], 0 offen offset:2048 sc1\n\t"
      "s_waitcnt vmcnt(0)\n\t"
      "v_cmp_eq_u64 vcc, s[8:9], v[2:3]\n\t"
      "s_nop 0\n\t"
      "s_cmp_eq_u64 vcc, -1\n\t"
      "s_cbranch_scc1 Lpd27_%=\n\t"
      "s_sleep 2\n\t"
      "s_sub_u32 s10, s10, 1\n\t"
      "s_cmp_lg_u32 s10, 0\n\t"
      "s_cbranch_scc1 Lp27_%=\n\t"
      "Lpd27_%=:\n\t"
      STAMP(26, 27)
      LSTW(26, "s_waitcnt lgkmcnt(0)\n\t")
      "s_branch Lhr_%=\n\t"
      "Lend_%=:\n\t"
      : "+{v0}"(tid)
      : "{s[0:1]}"(__builtin_amdgcn_kernarg_segment_ptr()), "{s2}"(blockIdx.x)
      : "memory", "scc", "vcc", "s3","s4","s5","s6","s7","s8","s9","s10","s11","s12","s13","s14","s15","s16","s17","s18","s19","s20","s21","s22","s23","s24","s25","s26","s27","s28","s29","s30","s31","s33","s34","s35","s36","s37","s38","s39","s40","s41","s42","s43","s44","s45","s46","s47","s48","s49","s50","s51","s52","s53","s54","s55","s56","s57","s58","s59","s60","s61","s62","s63","s64","s65","s66","s67","s68","s69","s70","s71","s72","s73","s74","s75","s76","s77","s78","s79","s80","s81","s82","s83","s84","s85","s86","s87","s88","s89","s90","s91","s92","s93","s94","s95","s96","s97","s98","s99", "v1","v2","v3","v4","v5","v6","v7","v8","v9","v10","v11","v12","v13","v14","v15","v16","v17","v18","v19","v20","v21","v22","v23","v24","v25","v26","v27","v28","v29","v30","v31","v32","v33","v34","v35","v36","v37","v38","v39","v40","v41","v42","v43","v44","v45","v46","v47","v48","v49","v50","v51","v52","v53","v54","v55","v56","v57","v58","v59","v60","v61","v62","v63","v64","v65","v66","v67","v68","v69","v70","v71","v72","v73","v74","v75","v76","v77","v78","v79","v80","v81","v82","v83","v84","v85","v86","v87","v88","v89","v90","v91","v92","v93","v94","v95","v96","v97","v98","v99","v100","v101","v102","v103","v104","v105","v106","v107","v108","v109","v110","v111","v112","v113","v114","v115","v116","v117","v118","v119","v120","v121","v122","v123","v124","v125","v126","v127","v128","v129","v130","v131","v132","v133","v134","v135","v136","v137","v138","v139","v140","v141","v142","v143","v144","v145","v146","v147","v148","v149","v150","v151","v152","v153","v154","v155","v156","v157","v158","v159","v160","v161","v162","v163","v164","v165","v166","v167","v168","v169","v170","v171","v172","v173","v174","v175","v176","v177","v178","v179","v180","v181","v182","v183","v184","v185","v186","v187","v188","v189","v190","v191","v192","v193","v194","v195","v196","v197","v198","v199","v200","v201","v202","v203","v204","v205","v206","v207","v208","v209","v210","v211","v212","v213","v214","v215","v216","v217","v218","v219","v220","v221","v222","v223","v224","v225","v226","v227","v228","v229","v230","v231","v232","v233","v234","v235","v236","v237","v238","v239","v240","v241","v242","v243","v244","v245","v246","v247","v248","v249","v250","v251","v252","v253","v254","v255", "a0","a1","a2","a3","a4","a5","a6","a7","a8","a9","a10","a11","a12","a13","a14","a15","a16","a17","a18","a19","a20","a21","a22","a23","a24","a25","a26","a27","a28","a29","a30","a31","a32","a33","a34","a35","a36","a37","a38","a39","a40","a41","a42","a43","a44","a45","a46","a47","a48","a49","a50","a51","a52","a53","a54","a55","a56","a57","a58","a59","a60","a61","a62","a63","a64","a65","a66","a67","a68","a69","a70","a71","a72","a73","a74","a75","a76","a77","a78","a79","a80","a81","a82","a83","a84","a85","a86","a87","a88","a89","a90","a91","a92","a93","a94","a95","a96","a97","a98","a99","a100","a101","a102","a103","a104","a105","a106","a107","a108","a109","a110","a111","a112","a113","a114","a115","a116","a117","a118","a119","a120","a121","a122","a123","a124","a125","a126","a127","a128","a129","a130","a131","a132","a133","a134","a135","a136","a137","a138","a139","a140","a141","a142","a143","a144","a145","a146","a147","a148","a149","a150","a151","a152","a153","a154","a155","a156","a157","a158","a159","a160","a161","a162","a163","a164","a165","a166","a167","a168","a169","a170","a171","a172","a173","a174","a175","a176","a177","a178","a179","a180","a181","a182","a183","a184","a185","a186","a187","a188","a189","a190","a191","a192","a193","a194","a195","a196","a197","a198","a199","a200","a201","a202","a203","a204","a205","a206","a207","a208","a209","a210","a211","a212","a213","a214","a215","a216","a217","a218","a219","a220","a221","a222","a223","a224","a225","a226","a227","a228","a229","a230","a231","a232","a233","a234","a235","a236","a237","a238","a239","a240","a241","a242","a243","a244","a245","a246","a247","a248","a249","a250","a251","a252","a253","a254","a255");
}

extern "C" void kernel_launch(void* const* d_in, const int* in_sizes, int n_in, void* d_out, int out_size, void* d_ws, size_t ws_size,
                              hipStream_t stream) {
  (void)hipFuncSetAttribute((const void*)fused_kernel, hipFuncAttributeMaxDynamicSharedMemorySize, LDS_BYTES);
  fused_kernel<<<256, 256, LDS_BYTES, stream>>>(d_in[0], d_in[1], d_in[2], d_in[4], d_in[5], d_in[6], d_in[7], d_out, d_ws);
}
```
